```python
import torch

def _softmax(x, axis=-1):
    return torch.softmax(x, dim=axis)

def _gelu(x):
    return torch.nn.functional.gelu(x, approximate='tanh')

def _silu(x):
    return torch.nn.functional.silu(x)

def _sigmoid(x):
    return torch.sigmoid(x)

def _layernorm(x, axis=-1, eps=1e-05):
    m = x.mean(dim=axis, keepdim=True)
    v = x.var(dim=axis, keepdim=True, unbiased=False)
    return (x - m) / torch.sqrt(v + eps)

def _scatter_add(tgt, idx, src, axis=0):
    return tgt.clone().index_add_(axis, idx, src)

def _segment_sum(src, seg, n):
    out = torch.zeros((n,) + src.shape[1:], dtype=src.dtype)
    return out.index_add_(0, seg, src)

def _pad(x, pw):
    flat = tuple((v for pair in reversed(pw) for v in pair))
    return torch.nn.functional.pad(x, flat)
kernel_name = 'adaLN_cond__grouped_kv_mqa__dense_activation_silu__prompt'

def setup_inputs():

    _w = lambda a: (lambda t: t.to(torch.bfloat16) if t.is_floating_point() else t)(torch.as_tensor(a))

    def _A_setup():
        B, S, D, H = (1, 2048, 1024, 32)
        Dh = D // H
        return {'x': _w((lambda *s: torch.randn(*s, dtype=torch.bfloat16) * 0.05)(B, S, D)), 'wq': _w((lambda *s: torch.randn(*s, dtype=torch.bfloat16) * 0.05)(D, H * Dh)), 'wk': _w((lambda *s: torch.randn(*s, dtype=torch.bfloat16) * 0.05)(D, Dh)), 'wv': _w((lambda *s: torch.randn(*s, dtype=torch.bfloat16) * 0.05)(D, Dh)), 'wo': _w((lambda *s: torch.randn(*s, dtype=torch.bfloat16) * 0.05)(H * Dh, D))}
    _A_in = _A_setup()

    def _B_setup():
        B, S, D, F = (1, 2048, 1024, 3584)
        return {'x': _w((lambda *s: torch.randn(*s, dtype=torch.bfloat16) * 0.05)(B, S, D)), 'w1': _w((lambda *s: torch.randn(*s, dtype=torch.bfloat16) * 3.2)(D, F) * D ** (-0.5)), 'w2': _w((lambda *s: torch.randn(*s, dtype=torch.bfloat16) * 0.8)(F, D) * F ** (-0.5))}
    _B_in = _B_setup()
    return {'A_x': _A_in['x'], 'A_wq': _A_in['wq'], 'A_wk': _A_in['wk'], 'A_wv': _A_in['wv'], 'A_wo': _A_in['wo'], 'B_x': _B_in['x'], 'B_w1': _B_in['w1'], 'B_w2': _B_in['w2'], 'c': _w((lambda *s: torch.randn(*s, dtype=torch.bfloat16) * 0.1)(1, 1024) * 8.0), 'w_mod': _w((lambda *s: torch.randn(*s, dtype=torch.bfloat16) * 0.1)(1024, 6 * 1024) * 1024 ** (-0.5) * 8.0)}

def reference(A_x, A_wq, A_wk, A_wv, A_wo, B_x, B_w1, B_w2, c, w_mod):

    def _A_ref(x, wq, wk, wv, wo):
        B, S, D = x.shape
        H = 32
        Dh = D // H
        q = (lambda x, *d: x.permute(*d))((x @ wq).reshape(B, S, H, Dh), 0, 2, 1, 3)
        k = x @ wk
        v = x @ wv
        scores = torch.einsum('bhqd,bkd->bhqk', q, k) * Dh ** (-0.5)
        p = _softmax(scores, axis=-1)
        o = torch.einsum('bhqk,bkd->bhqd', p, v)
        return (lambda x, *d: x.permute(*d))(o, 0, 2, 1, 3).reshape(B, S, H * Dh) @ wo

    def _B_ref(x, w1, w2):
        h = torch.einsum('bsd,df->bsf', x, w1)
        h = _silu(h)
        out = torch.einsum('bsf,fd->bsd', h, w2)
        return out
    x = A_x
    mod = (_silu(c) @ w_mod).reshape(c.shape[0], 6, 1, -1)
    sa, ca, ga, sb, cb, gb = (mod[:, 0].clone(), mod[:, 1].clone(), mod[:, 2].clone(), mod[:, 3].clone(), mod[:, 4].clone(), mod[:, 5].clone())
    h = _layernorm(x, axis=-1) * (1 + ca) + sa
    x = x + ga * _A_ref(h, A_wq, A_wk, A_wv, A_wo)
    h = _layernorm(x, axis=-1) * (1 + cb) + sb
    x = x + gb * _B_ref(h, B_w1, B_w2)
    return x
```

```cpp
#include <hip/hip_runtime.h>
#include <hip/hip_bf16.h>
#include <stdint.h>

typedef __attribute__((ext_vector_type(2))) float f32x2;
typedef __attribute__((ext_vector_type(4))) float f32x4;
typedef __attribute__((ext_vector_type(16))) float f32x16;
typedef __attribute__((ext_vector_type(2))) __bf16 bf16x2;
typedef __attribute__((ext_vector_type(8))) __bf16 bf16x8;
typedef __attribute__((ext_vector_type(4))) short s16x4;
typedef __attribute__((ext_vector_type(2))) unsigned u32x2;
typedef __attribute__((ext_vector_type(4))) unsigned u32x4;
#define LDS __attribute__((address_space(3)))
typedef LDS char* lptr;
#define DEV __device__ __forceinline__

constexpr int SEQ = 2048, DM = 1024, FF = 3584, NMOD = 6 * DM;
constexpr size_t WS_MODP = 0;
constexpr size_t WS_UTP = 98304;
constexpr size_t WS_STAT = 262144;
constexpr size_t WS_KV = 786432;
constexpr size_t WS_H = (size_t)1 << 20;
constexpr size_t WS_O = (size_t)5 << 20;
constexpr size_t WS_X1 = (size_t)9 << 20;
constexpr size_t WS_Y = (size_t)13 << 20;
constexpr size_t WS_G = (size_t)17 << 20;

DEV unsigned pk(float a, float b) { return __builtin_bit_cast(unsigned, __builtin_convertvector((f32x2){a, b}, bf16x2)); }
DEV float rbf(float a) { return __uint_as_float(pk(a, 0.f) << 16); }
DEV float blo(unsigned u) { return __uint_as_float(u << 16); }
DEV float bhi(unsigned u) { return __uint_as_float(u & 0xffff0000u); }
DEV float silu(float z) { return z * __builtin_amdgcn_rcpf(1.f + __builtin_amdgcn_exp2f(-1.4426950408889634f * z)); }
DEV f32x16 mfma32(bf16x8 a, bf16x8 b, f32x16 c) { return __builtin_amdgcn_mfma_f32_32x32x16_bf16(a, b, c, 0, 0, 0); }
DEV f32x4 mfma16(bf16x8 a, bf16x8 b, f32x4 c) { return __builtin_amdgcn_mfma_f32_16x16x32_bf16(a, b, c, 0, 0, 0); }
DEV bf16x8 asb(u32x4 v) { return __builtin_bit_cast(bf16x8, v); }
DEV bf16x8 ldsA(lptr p) { return *(const LDS bf16x8*)p; }
DEV bf16x8 ldsT(lptr p, int second) {
  s16x4 a = __builtin_amdgcn_ds_read_tr16_b64_v4i16((LDS s16x4*)p);
  s16x4 b = __builtin_amdgcn_ds_read_tr16_b64_v4i16((LDS s16x4*)(p + second));
  return __builtin_bit_cast(bf16x8, __builtin_shufflevector(a, b, 0, 1, 2, 3, 4, 5, 6, 7));
}
template <int C> DEV float dppadd(float v) { return v + __int_as_float(__builtin_amdgcn_update_dpp(0, __float_as_int(v), C, 0xf, 0xf, true)); }
DEV float swap32add(float v) { auto r = __builtin_amdgcn_permlane32_swap(__float_as_uint(v), __float_as_uint(v), false, false); return __uint_as_float(r[0]) + __uint_as_float(r[1]); }
DEV float swap16add(float v) { auto r = __builtin_amdgcn_permlane16_swap(__float_as_uint(v), __float_as_uint(v), false, false); return __uint_as_float(r[0]) + __uint_as_float(r[1]); }
DEV float swap32max(float v) { auto r = __builtin_amdgcn_permlane32_swap(__float_as_uint(v), __float_as_uint(v), false, false); return __builtin_fmaxf(__uint_as_float(r[0]), __uint_as_float(r[1])); }
DEV float wavesum(float v) { v = dppadd<0xB1>(v); v = dppadd<0x4E>(v); v = dppadd<0x141>(v); v = dppadd<0x140>(v); v = swap16add(v); return swap32add(v); }

#define SETM0(x) asm volatile("s_mov_b32 m0, %0\n\ts_nop 0" ::"s"(x) : "memory")
template <int OFF> DEV void glds(unsigned voff, const void* base) { asm volatile("global_load_lds_dwordx4 %0, %1 offset:%2" ::"v"(voff), "s"(base), "i"(OFF) : "memory"); }
template <int N> DEV void waitbar() { asm volatile("s_waitcnt vmcnt(%0) lgkmcnt(0)\n\ts_barrier" ::"i"(N) : "memory"); }
template <int OFF> DEV u32x4 gload(unsigned voff, const void* base) { u32x4 r; asm volatile("global_load_dwordx4 %0, %1, %2 offset:%3" : "=v"(r) : "v"(voff), "s"(base), "i"(OFF) : "memory"); return r; }
#define SB() __builtin_amdgcn_sched_barrier(0)
extern __shared__ __attribute__((aligned(16))) char smem_raw[];

__global__ __launch_bounds__(256) void k_mod(const uint16_t* __restrict__ c, const char* __restrict__ wmod, float* __restrict__ modp) {
  __shared__ float red[4][32];
  const int b = blockIdx.x, t = threadIdx.x, xcd = b & 7, j = b >> 3;
  const int cg = (j & 1) + 2 * (xcd + 8 * ((j >> 1) % 12)), ks = j / 24;
  const int row0 = ks * 256 + (t >> 2);
  const char* p = wmod + (size_t)row0 * (NMOD * 2) + cg * 64 + (t & 3) * 16;
  u32x4 w[4]; float s[4];
#pragma unroll
  for (int i = 0; i < 4; ++i) { w[i] = *(const u32x4*)(p + (size_t)i * 64 * NMOD * 2); s[i] = __uint_as_float((unsigned)c[row0 + 64 * i] << 16); }
  float a[8] = {0, 0, 0, 0, 0, 0, 0, 0};
#pragma unroll
  for (int i = 0; i < 4; ++i) { const float f = rbf(silu(s[i]));
#pragma unroll
    for (int e = 0; e < 4; ++e) { a[2 * e] += f * blo(w[i][e]); a[2 * e + 1] += f * bhi(w[i][e]); } }
#pragma unroll
  for (int e = 0; e < 8; ++e) { float v = a[e]; v += __shfl_xor(v, 4); v += __shfl_xor(v, 8); v = swap16add(v); a[e] = swap32add(v); }
  if ((t & 63) < 4) {
#pragma unroll
    for (int e = 0; e < 8; ++e) red[t >> 6][(t & 3) * 8 + e] = a[e]; }
  __syncthreads();
  if (t < 32) modp[ks * NMOD + cg * 32 + t] = (red[0][t] + red[1][t]) + (red[2][t] + red[3][t]);
}

constexpr int K1_W = 0, K1_H = 65536, K1_MV = K1_H + 64 * 528, K1_RED = K1_MV + 8192, K1_LDS = K1_RED + 12288;
__global__ __launch_bounds__(256) void k_ln1kv(const char* __restrict__ x, const char* __restrict__ wk, const char* __restrict__ wv,
                                               const char* __restrict__ modp, char* __restrict__ hbuf, char* __restrict__ kv) {
  const lptr sm = (lptr)smem_raw;
  const int b = blockIdx.x, t = threadIdx.x, l = t & 63, w = __builtin_amdgcn_readfirstlane(t >> 6);
  const int which = (b >> 3) & 1, rb = (b & 7) * 16 + (b >> 4);
  u32x4 xv[8], mp[8];
  { const char* xb = x + (size_t)(rb * 16 + 4 * w) * 2048; const unsigned vo = l * 32;
    xv[0] = gload<0>(vo, xb); xv[1] = gload<16>(vo, xb); xv[2] = gload<2048>(vo, xb); xv[3] = gload<2064>(vo, xb);
    xb += 4096;
    xv[4] = gload<0>(vo, xb); xv[5] = gload<16>(vo, xb); xv[6] = gload<2048>(vo, xb); xv[7] = gload<2064>(vo, xb);
    const unsigned mo = t * 32;
#pragma unroll
    for (int s = 0; s < 4; ++s) { mp[2 * s] = gload<0>(mo, modp + s * NMOD * 4); mp[2 * s + 1] = gload<16>(mo, modp + s * NMOD * 4); } }
  { const char* wsrc = (which ? wv : wk) + w * 16384;
#pragma unroll
    for (int g = 0; g < 4; ++g) { SETM0(K1_W + w * 16384 + g * 4096); const char* s = wsrc + g * 4096;
      glds<0>(l * 16, s); glds<1024>(l * 16, s); glds<2048>(l * 16, s); glds<3072>(l * 16, s); } }
  asm volatile("s_waitcnt vmcnt(16)" : "+v"(xv[0]), "+v"(xv[1]), "+v"(xv[2]), "+v"(xv[3]), "+v"(xv[4]), "+v"(xv[5]), "+v"(xv[6]), "+v"(xv[7]),
               "+v"(mp[0]), "+v"(mp[1]), "+v"(mp[2]), "+v"(mp[3]), "+v"(mp[4]), "+v"(mp[5]), "+v"(mp[6]), "+v"(mp[7])::"memory");
  { f32x4 o[2];
#pragma unroll
    for (int hh = 0; hh < 2; ++hh)
#pragma unroll
      for (int e = 0; e < 4; ++e) { float v = (__uint_as_float(mp[hh][e]) + __uint_as_float(mp[2 + hh][e])) + (__uint_as_float(mp[4 + hh][e]) + __uint_as_float(mp[6 + hh][e]));
        v = rbf(v); o[hh][e] = t >= 128 ? rbf(1.f + v) : v; }
    *(LDS f32x4*)(sm + K1_MV + t * 32) = o[0]; *(LDS f32x4*)(sm + K1_MV + t * 32 + 16) = o[1]; }
  float v[4][16], rs[4];
#pragma unroll
  for (int r = 0; r < 4; ++r) { float s = 0.f;
#pragma unroll
    for (int e = 0; e < 8; ++e) { const unsigned u = xv[2 * r + (e >> 2)][e & 3]; v[r][2 * e] = blo(u); v[r][2 * e + 1] = bhi(u); s += v[r][2 * e] + v[r][2 * e + 1]; }
    const float m = wavesum(s) * (1.f / 1024.f); float q = 0.f;
#pragma unroll
    for (int e = 0; e < 16; ++e) { v[r][e] -= m; q += v[r][e] * v[r][e]; }
    rs[r] = __builtin_amdgcn_rsqf(wavesum(q) * (1.f / 1024.f) + 1e-5f); }
  asm volatile("s_waitcnt lgkmcnt(0)\n\ts_barrier" ::: "memory");
  { float sa[16], c1[16];
#pragma unroll
    for (int e = 0; e < 4; ++e) { const f32x4 a = *(const LDS f32x4*)(sm + K1_MV + l * 64 + e * 16), cc = *(const LDS f32x4*)(sm + K1_MV + 4096 + l * 64 + e * 16);
#pragma unroll
      for (int i = 0; i < 4; ++i) { sa[4 * e + i] = a[i]; c1[4 * e + i] = cc[i]; } }
#pragma unroll
    for (int r = 0; r < 4; ++r) { unsigned u[8];
#pragma unroll
      for (int e = 0; e < 8; ++e) u[e] = pk(v[r][2 * e] * rs[r] * c1[2 * e] + sa[2 * e], v[r][2 * e + 1] * rs[r] * c1[2 * e + 1] + sa[2 * e + 1]);
      const lptr d = sm + K1_H + l * 528 + (4 * w + r) * 16;
      *(LDS u32x4*)d = (u32x4){u[0], u[1], u[4], u[5]}; *(LDS u32x4*)(d + 256) = (u32x4){u[2], u[3], u[6], u[7]}; } }
  waitbar<0>();
#pragma unroll
  for (int i = 0; i < 4; ++i) { const int id = t + 256 * i, tok = id & 15, hi = (id >> 4) & 1, kb = which * 32 + (id >> 5);
    *(u32x4*)(hbuf + (size_t)((rb >> 1) * 64 + kb) * 1024 + (hi * 32 + (rb & 1) * 16 + tok) * 16) = *(const LDS u32x4*)(sm + K1_H + kb * 528 + hi * 256 + tok * 16); }
  f32x16 acc = {};
  { const int hi = l >> 5; const lptr hp = sm + K1_H + w * 16 * 528 + hi * 256 + (l & 15) * 16;
    const lptr wp = sm + K1_W + (w * 256 + 4 * hi + ((l >> 2) & 3)) * 64 + ((l >> 4) & 1) * 32 + (l & 3) * 8;
    if (which == 0) {
#pragma unroll
      for (int i = 0; i < 16; ++i) acc = mfma32(ldsT(wp + i * 1024, 512), ldsA(hp + i * 528), acc);
    } else {
#pragma unroll
      for (int i = 0; i < 16; ++i) acc = mfma32(ldsA(hp + i * 528), ldsT(wp + i * 1024, 512), acc);
    } }
  if (w) {
#pragma unroll
    for (int q = 0; q < 4; ++q) *(LDS f32x4*)(sm + K1_RED + (w - 1) * 4096 + q * 1024 + l * 16) = (f32x4){acc[4 * q], acc[4 * q + 1], acc[4 * q + 2], acc[4 * q + 3]}; }
  __syncthreads();
  if (w == 0) {
#pragma unroll
    for (int u = 0; u < 3; ++u)
#pragma unroll
      for (int q = 0; q < 4; ++q) { const f32x4 a = *(const LDS f32x4*)(sm + K1_RED + u * 4096 + q * 1024 + l * 16);
#pragma unroll
        for (int i = 0; i < 4; ++i) acc[4 * q + i] += a[i]; }
    const u32x4 p0 = {pk(acc[0], acc[1]), pk(acc[2], acc[3]), pk(acc[4], acc[5]), pk(acc[6], acc[7])};
    const u32x4 p1 = {pk(acc[8], acc[9]), pk(acc[10], acc[11]), pk(acc[12], acc[13]), pk(acc[14], acc[15])};
    char* base = kv + (rb >> 1) * 4096;
    if (which) *(u32x4*)(base + 2048 + (rb & 1) * 1024 + l * 16) = p0;
    else if ((l & 31) < 16) { char* d = base + ((l >> 5) * 32 + (rb & 1) * 16 + (l & 15)) * 16; *(u32x4*)d = p0; *(u32x4*)(d + 1024) = p1; }
  }
}

constexpr int GA_STAGE = 24576, GA_S = 4, GA_LDS = GA_S * GA_STAGE;
template <int KT, int LDW, int EXTRA, class F>
DEV f32x16 gemmA(const lptr sm, const char* act, const char* wgt, int l, int w, F&& extra) {
  const char* ap = act + (size_t)((w >> 1) * (KT * 4) + 2 * (w & 1)) * 1024;
  const unsigned va = l * 16;
  const int rl = 8 * w + (l >> 3);
  const unsigned vb = rl * (LDW * 2) + (((l & 7) ^ (((rl >> 1) & 1) << 2)) * 16);
  const int hi = l >> 5, q = (l >> 2) & 3, p = l & 3;
  const lptr ra = sm + (w & 3) * 4096 + l * 16;
  const lptr rb = sm + 16384 + (4 * hi + q) * 128 + ((((w >> 2) * 4 + ((l >> 4) & 1) * 2 + (p >> 1)) ^ ((q >> 1) << 2)) * 16) + (p & 1) * 8;
  const unsigned mA = w * 2048, mB = 16384 + w * 1024;
  auto issue = [&](int t, int slot) {
    SETM0(slot * GA_STAGE + mA); const char* a = ap + (size_t)t * 4096; glds<0>(va, a); glds<1024>(va, a);
    SETM0(slot * GA_STAGE + mB); glds<0>(vb, wgt + (size_t)t * (64 * LDW * 2)); };
  f32x16 acc = {};
  auto compute = [&](int slot) {
    bf16x8 a[4], bb[4];
#pragma unroll
    for (int k = 0; k < 4; ++k) { a[k] = ldsA(ra + slot * GA_STAGE + k * 1024); bb[k] = ldsT(rb + slot * GA_STAGE + k * 2048, 1024); }
#pragma unroll
    for (int k = 0; k < 4; ++k) acc = mfma32(bb[k], a[k], acc); };
  issue(0, 0); issue(1, 1); issue(2, 2);
  for (int t = 0; t < KT - 4; t += 4) {
#pragma unroll
    for (int s = 0; s < 4; ++s) { waitbar<6>(); issue(t + s + 3, (s + 3) & 3); compute(s); } }
  waitbar<6>(); issue(KT - 1, 3); extra(); compute(0);
  waitbar<6 + EXTRA>(); compute(1);
  waitbar<3 + EXTRA>(); compute(2);
  waitbar<EXTRA>(); compute(3);
  return acc;
}

constexpr int K2_KV = GA_LDS, K2_LDS = GA_LDS + 65536;
DEV void pack16(const f32x16& s, u32x4& a, u32x4& b) {
  a = (u32x4){pk(s[0], s[1]), pk(s[2], s[3]), pk(s[4], s[5]), pk(s[6], s[7])};
  b = (u32x4){pk(s[8], s[9]), pk(s[10], s[11]), pk(s[12], s[13]), pk(s[14], s[15])}; }
DEV float max16(const f32x16& s) { float m = s[0];
#pragma unroll
  for (int i = 1; i < 16; ++i) m = __builtin_fmaxf(m, s[i]);
  return swap32max(m); }
#define EX2(v) __builtin_amdgcn_exp2f(v)
#define PIN(x) asm volatile("" : "+v"(x))

__global__ __launch_bounds__(512, 2) void k_qattn(const char* __restrict__ hbuf, const char* __restrict__ wq, const char* __restrict__ kv, char* __restrict__ obuf) {
  const lptr sm = (lptr)smem_raw;
  const int b = blockIdx.x, l = threadIdx.x & 63, w = __builtin_amdgcn_readfirstlane(threadIdx.x >> 6);
  const int xcd = b & 7, j = b >> 3, tb = (xcd >> 1) * 4 + (j & 3), hp = (xcd & 1) * 8 + (j >> 2);
  const char* kvw = kv + w * 4096;
  auto kvload = [&](int st) { SETM0(K2_KV + (st & 1) * 32768 + w * 4096); const char* s = kvw + st * 32768;
    glds<0>(l * 16, s); glds<1024>(l * 16, s); glds<2048>(l * 16, s); glds<3072>(l * 16, s); };
  f32x16 qa = gemmA<16, DM, 4>(sm, hbuf + (size_t)tb * 4 * 64 * 1024, wq + hp * 128, l, w, [&] { kvload(0); });
  constexpr float QS = 0.17677669529663687f * 1.4426950408889634f;
#pragma unroll
  for (int i = 0; i < 16; ++i) qa[i] *= QS;
  u32x4 q0u, q1u; pack16(qa, q0u, q1u);
  const bf16x8 q0 = asb(q0u), q1 = asb(q1u);
  const unsigned one2 = ((l & 7) == ((l >> 4) & 1) * 4) ? 0x3f803f80u : 0u;
  const bf16x8 ones = asb((u32x4){one2, one2, one2, one2});
  const lptr kp = sm + K2_KV + l * 16;
  waitbar<0>();
  kvload(1);
  bf16x8 k0 = ldsA(kp), k1 = ldsA(kp + 1024), v0 = ldsA(kp + 2048), v1 = ldsA(kp + 3072);
  f32x16 negm, sA, sB, o = {}; f32x4 ls = {};
  { f32x16 z = {}; z = mfma32(k0, q0, z); z = mfma32(k1, q1, z); const float m = -max16(z);
#pragma unroll
    for (int i = 0; i < 16; ++i) negm[i] = m; }
  PIN(negm);
  sA = mfma32(k0, q0, negm); sA = mfma32(k1, q1, sA);
  k0 = ldsA(kp + 4096); k1 = ldsA(kp + 4096 + 1024);
  u32x4 p0, p1 = {0, 0, 0, 0}; bf16x8 v1p = v1;
#define TILE(S, N, KOFF, VOFF)                                                                                        \
  { N = mfma32(k0, q0, negm);                                                                                        \
    S[0] = EX2(S[0]); S[1] = EX2(S[1]); S[2] = EX2(S[2]); S[3] = EX2(S[3]); p0[0] = pk(S[0], S[1]); p0[1] = pk(S[2], S[3]); PIN(p0); SB(); \
    o = mfma32(v1p, asb(p1), o);                                                                                     \
    S[4] = EX2(S[4]); S[5] = EX2(S[5]); S[6] = EX2(S[6]); S[7] = EX2(S[7]); p0[2] = pk(S[4], S[5]); p0[3] = pk(S[6], S[7]); PIN(p0); SB(); \
    ls = mfma16(ones, asb(p1), ls);                                                                                  \
    S[8] = EX2(S[8]); S[9] = EX2(S[9]); PIN(S); SB();                                                                \
    N = mfma32(k1, q1, N); k0 = ldsA(kp + (KOFF)); v1p = v1;                                                         \
    S[10] = EX2(S[10]); S[11] = EX2(S[11]); S[12] = EX2(S[12]); S[13] = EX2(S[13]); p1[0] = pk(S[8], S[9]); p1[1] = pk(S[10], S[11]); PIN(p1); PIN(S); SB(); \
    o = mfma32(v0, asb(p0), o); k1 = ldsA(kp + (KOFF) + 1024); v1 = ldsA(kp + (VOFF) + 1024);                        \
    S[14] = EX2(S[14]); S[15] = EX2(S[15]); p1[2] = pk(S[12], S[13]); p1[3] = pk(S[14], S[15]); PIN(p1); SB();       \
    ls = mfma16(ones, asb(p0), ls); v0 = ldsA(kp + (VOFF)); SB(); }
#define STAGE(P, Q, LAST, NEXTLOAD)                                                                                   \
  TILE(sA, sB, P + 2 * 4096, P + 1 * 4096 + 2048) TILE(sB, sA, P + 3 * 4096, P + 2 * 4096 + 2048)                     \
  TILE(sA, sB, P + 4 * 4096, P + 3 * 4096 + 2048) TILE(sB, sA, P + 5 * 4096, P + 4 * 4096 + 2048)                     \
  TILE(sA, sB, P + 6 * 4096, P + 5 * 4096 + 2048) TILE(sB, sA, P + 7 * 4096, P + 6 * 4096 + 2048)                     \
  if (!(LAST)) waitbar<0>();                                                                                          \
  TILE(sA, sB, Q, P + 7 * 4096 + 2048)                                                                                \
  if (!(LAST)) { asm volatile("s_waitcnt lgkmcnt(0)\n\ts_barrier" ::: "memory"); NEXTLOAD; }                          \
  TILE(sB, sA, Q + 4096, Q + 2048)
  for (int st = 0; st < 8; st += 2) {
    STAGE(0, 32768, false, if (st < 6) kvload(st + 2))
    STAGE(32768, 0, st == 6, if (st < 6) kvload(st + 3))
  }
  o = mfma32(v1p, asb(p1), o); ls = mfma16(ones, asb(p1), ls);
  float lsum = ls[0];
  { float chk = lsum;
#pragma unroll
    for (int i = 0; i < 16; ++i) chk = __builtin_fmaxf(chk, __builtin_fabsf(o[i]) * 0x1p-40f);
    bool bad = !(chk < 0x1p80f);
#pragma unroll
    for (int i = 0; i < 16; ++i) bad |= (o[i] != o[i]);
    bad |= (lsum != lsum);
    if (__any(bad)) {
      float m = -INFINITY; lsum = 0.f; o = (f32x16){};
      for (int t = 0; t < 64; ++t) { const bf16x8* g = (const bf16x8*)(kv + t * 4096) + l;
        f32x16 s = {}; s = mfma32(g[0], q0, s); s = mfma32(g[64], q1, s);
        const float mn = __builtin_fmaxf(m, max16(s)), al = EX2(m - mn); m = mn; float rsum = 0.f;
#pragma unroll
        for (int i = 0; i < 16; ++i) { s[i] = rbf(EX2(s[i] - mn)); rsum += s[i]; o[i] *= al; }
        lsum = lsum * al + swap32add(rsum);
        u32x4 a, c; pack16(s, a, c); o = mfma32(g[128], asb(a), o); o = mfma32(g[192], asb(c), o); } } }
  const float inv = 1.f / lsum;
#pragma unroll
  for (int i = 0; i < 16; ++i) o[i] *= inv;
  u32x4 o0, o1; pack16(o, o0, o1);
  char* d = obuf + (size_t)((tb * 4 + (w & 3)) * 64 + (hp * 2 + (w >> 2)) * 2) * 1024 + l * 16;
  *(u32x4*)d = o0; *(u32x4*)(d + 1024) = o1;
}

constexpr int K3_MV = GA_LDS, K3_UT = K3_MV + 512, K3_RED = K3_UT + 2048, K3_LDS = K3_RED + 8 * 128 * 4;
__global__ __launch_bounds__(512, 2) void k_oproj(const char* __restrict__ obuf, const char* __restrict__ wo, const char* __restrict__ x, const float* __restrict__ modp,
                                                  const char* __restrict__ w1, float* __restrict__ utp, char* __restrict__ x1b, char* __restrict__ yb, f32x2* __restrict__ stat) {
  const lptr sm = (lptr)smem_raw;
  const int b = blockIdx.x, t = threadIdx.x, l = t & 63, w = __builtin_amdgcn_readfirstlane(t >> 6);
  const int xcd = b & 7, j = b >> 3, tb = (xcd >> 1) * 4 + (j & 3), nt = (xcd & 1) * 8 + (j >> 2);
  const int hi = l >> 5, tok = tb * 128 + (w & 3) * 32 + (l & 31), n0 = nt * 64 + (w >> 2) * 32;
  u32x2 xr[4];
#pragma unroll
  for (int q = 0; q < 4; ++q) xr[q] = *(const u32x2*)(x + (size_t)tok * 2048 + (n0 + 8 * q + 4 * hi) * 2);
  if (t < 128) { const int col = (t < 64 ? 2 * DM : 4 * DM - 64) + nt * 64 + t;
    float v = rbf((modp[col] + modp[NMOD + col]) + (modp[2 * NMOD + col] + modp[3 * NMOD + col]));
    *(LDS float*)(sm + K3_MV + t * 4) = t < 64 ? v : rbf(1.f + v); }
  if (b < 224) {
    const int cg = b % 56, ks = b / 56;
    { const int col = (t < 256 ? 4 * DM : 3 * DM - 256) + ks * 256 + t;
      float v = rbf((modp[col] + modp[NMOD + col]) + (modp[2 * NMOD + col] + modp[3 * NMOD + col]));
      *(LDS float*)(sm + K3_UT + t * 4) = t < 256 ? rbf(1.f + v) : v; }
    u32x4 wv[4];
#pragma unroll
    for (int i = 0; i < 4; ++i) wv[i] = *(const u32x4*)(w1 + (size_t)(ks * 256 + (t >> 3) + 64 * i) * (FF * 2) + cg * 128 + (t & 7) * 16);
    __syncthreads();
    float a[16];
#pragma unroll
    for (int e = 0; e < 16; ++e) a[e] = 0.f;
#pragma unroll
    for (int i = 0; i < 4; ++i) { const float cu = *(const LDS float*)(sm + K3_UT + ((t >> 3) + 64 * i) * 4), ct = *(const LDS float*)(sm + K3_UT + 1024 + ((t >> 3) + 64 * i) * 4);
#pragma unroll
      for (int e = 0; e < 4; ++e) { const float lo = blo(wv[i][e]), h2 = bhi(wv[i][e]); a[2 * e] += cu * lo; a[2 * e + 1] += cu * h2; a[8 + 2 * e] += ct * lo; a[9 + 2 * e] += ct * h2; } }
#pragma unroll
    for (int e = 0; e < 16; ++e) { float v = a[e]; v += __shfl_xor(v, 8); v = swap16add(v); a[e] = swap32add(v); }
    if (l < 8) {
#pragma unroll
      for (int e = 0; e < 16; ++e) *(LDS float*)(sm + K3_RED + (w * 128 + (e >> 3) * 64 + l * 8 + (e & 7)) * 4) = a[e]; }
    __syncthreads();
    if (t < 128) { float v = 0.f;
#pragma unroll
      for (int u = 0; u < 8; ++u) v += *(const LDS float*)(sm + K3_RED + (u * 128 + t) * 4);
      utp[(ks * 2 + (t >> 6)) * FF + cg * 64 + (t & 63)] = v; }
  }
  f32x16 acc = gemmA<16, DM, 0>(sm, obuf + (size_t)tb * 4 * 64 * 1024, wo + nt * 128, l, w, [] {});
  float x1[16], y[16], s = 0.f;
#pragma unroll
  for (int q = 0; q < 4; ++q) { const f32x4 ga = *(const LDS f32x4*)(sm + K3_MV + ((w >> 2) * 32 + 8 * q + 4 * hi) * 4), c1 = *(const LDS f32x4*)(sm + K3_MV + 256 + ((w >> 2) * 32 + 8 * q + 4 * hi) * 4);
    const float xx[4] = {blo(xr[q][0]), bhi(xr[q][0]), blo(xr[q][1]), bhi(xr[q][1])};
#pragma unroll
    for (int i = 0; i < 4; ++i) { const float v = rbf(xx[i] + ga[i] * acc[4 * q + i]); x1[4 * q + i] = v; y[4 * q + i] = v * c1[i]; s += v; } }
  const float mean = swap32add(s) * (1.f / 32.f); float m2 = 0.f;
#pragma unroll
  for (int i = 0; i < 16; ++i) m2 += (x1[i] - mean) * (x1[i] - mean);
  m2 = swap32add(m2);
  const int tb32 = tb * 4 + (w & 3);
  if (hi == 0) stat[(tb32 * 32 + nt * 2 + (w >> 2)) * 32 + l] = (f32x2){mean, m2};
  const size_t off = (size_t)(tb32 * 64 + (n0 >> 4)) * 1024 + l * 16;
  *(u32x4*)(x1b + off) = (u32x4){pk(x1[0], x1[1]), pk(x1[2], x1[3]), pk(x1[4], x1[5]), pk(x1[6], x1[7])};
  *(u32x4*)(x1b + off + 1024) = (u32x4){pk(x1[8], x1[9]), pk(x1[10], x1[11]), pk(x1[12], x1[13]), pk(x1[14], x1[15])};
  *(u32x4*)(yb + off) = (u32x4){pk(y[0], y[1]), pk(y[2], y[3]), pk(y[4], y[5]), pk(y[6], y[7])};
  *(u32x4*)(yb + off + 1024) = (u32x4){pk(y[8], y[9]), pk(y[10], y[11]), pk(y[12], y[13]), pk(y[14], y[15])};
}

constexpr int K4_STAGE = 45056, K4_UT = 3 * K4_STAGE, K4_LDS = K4_UT + 2048;
__global__ __launch_bounds__(256, 1) void k_ffn1(const char* __restrict__ yb, const char* __restrict__ w1, const f32x2* __restrict__ stat,
                                                 const float* __restrict__ utp, char* __restrict__ gb) {
  const lptr sm = (lptr)smem_raw;
  const int b = blockIdx.x, t = threadIdx.x, l = t & 63, w = __builtin_amdgcn_readfirstlane(t >> 6);
  const int xcd = b & 7, j = b >> 3, tb = (xcd >> 2) * 8 + (j & 7), nt = (xcd & 3) * 4 + (j >> 3);
  const int hi = l >> 5, tb32 = tb * 4 + w;
  f32x2 st[16]; float up[8];
#pragma unroll
  for (int i = 0; i < 16; ++i) st[i] = stat[(tb32 * 32 + 2 * i + hi) * 32 + (l & 31)];
  { const int col = nt * 224 + (t < 224 ? t : 0);
#pragma unroll
    for (int i = 0; i < 8; ++i) up[i] = utp[i * FF + col]; }
  const char* ap = yb + (size_t)tb32 * 64 * 1024;
  const char* wp = w1 + nt * 448;
  unsigned vb[7];
#pragma unroll
  for (int i = 0; i < 7; ++i) { const int c = (w * 7 + i) * 64 + l, r = c / 28; vb[i] = r * (FF * 2) + (c - r * 28) * 16 + 3072 - i * 1024; }
  const unsigned va = l * 16;
  auto issue = [&](int tt, int slot) {
    SETM0(slot * K4_STAGE + w * 4096); const char* a = ap + (size_t)tt * 4096;
    glds<0>(va, a); glds<1024>(va, a); glds<2048>(va, a); glds<3072>(va, a);
    SETM0(slot * K4_STAGE + 16384 + w * 7168 + 3072); const char* s = wp + (size_t)tt * (64 * FF * 2);
    glds<-3072>(vb[0], s); glds<-2048>(vb[1], s); glds<-1024>(vb[2], s); glds<0>(vb[3], s); glds<1024>(vb[4], s); glds<2048>(vb[5], s); glds<3072>(vb[6], s); };
  const lptr ra = sm + w * 4096 + l * 16;
  const lptr rb = sm + 16384 + (4 * hi + ((l >> 2) & 3)) * 448 + ((l >> 4) & 1) * 32 + (l & 3) * 8;
  f32x16 acc[7];
#pragma unroll
  for (int n = 0; n < 7; ++n) acc[n] = (f32x16){};
  auto compute = [&](int slot) {
#pragma unroll
    for (int k = 0; k < 4; ++k) { const bf16x8 a = ldsA(ra + slot * K4_STAGE + k * 1024);
#pragma unroll
      for (int n = 0; n < 7; ++n) acc[n] = mfma32(ldsT(rb + slot * K4_STAGE + k * 7168 + n * 64, 3584), a, acc[n]); } };
  issue(0, 0); issue(1, 1);
  for (int tt = 0; tt < 12; tt += 3) {
#pragma unroll
    for (int s = 0; s < 3; ++s) { waitbar<11>(); issue(tt + s + 2, (s + 2) % 3); compute(s); } }
  waitbar<11>(); issue(14, 2); compute(0);
  waitbar<11>(); issue(15, 0); compute(1);
  waitbar<11>(); compute(2);
  if (t < 224) { *(LDS float*)(sm + K4_UT + t * 4) = (up[0] + up[2]) + (up[4] + up[6]); *(LDS float*)(sm + K4_UT + 1024 + t * 4) = (up[1] + up[3]) + (up[5] + up[7]); }
  waitbar<0>(); compute(0);
  float mean = 0.f;
#pragma unroll
  for (int i = 0; i < 16; ++i) mean += st[i][0];
  mean = swap32add(mean) * (1.f / 32.f); float m2 = 0.f;
#pragma unroll
  for (int i = 0; i < 16; ++i) m2 += st[i][1] + 32.f * (st[i][0] - mean) * (st[i][0] - mean);
  const float rs = __builtin_amdgcn_rsqf(swap32add(m2) * (1.f / 1024.f) + 1e-5f), nm = -rs * mean;
  char* d = gb + (size_t)(tb32 * 224 + nt * 14) * 1024 + l * 16;
#pragma unroll
  for (int n = 0; n < 7; ++n) { float g[16];
#pragma unroll
    for (int q = 0; q < 4; ++q) { const f32x4 u = *(const LDS f32x4*)(sm + K4_UT + (n * 32 + 8 * q + 4 * hi) * 4), tv = *(const LDS f32x4*)(sm + K4_UT + 1024 + (n * 32 + 8 * q + 4 * hi) * 4);
#pragma unroll
      for (int i = 0; i < 4; ++i) g[4 * q + i] = silu(rs * acc[n][4 * q + i] + (nm * u[i] + tv[i])); }
    *(u32x4*)(d + n * 2048) = (u32x4){pk(g[0], g[1]), pk(g[2], g[3]), pk(g[4], g[5]), pk(g[6], g[7])};
    *(u32x4*)(d + n * 2048 + 1024) = (u32x4){pk(g[8], g[9]), pk(g[10], g[11]), pk(g[12], g[13]), pk(g[14], g[15])}; }
}

constexpr int K5_MV = GA_LDS, K5_LDS = K5_MV + 256;
__global__ __launch_bounds__(512, 2) void k_ffn2(const char* __restrict__ gb, const char* __restrict__ w2, const char* __restrict__ x1b,
                                                 const float* __restrict__ modp, char* __restrict__ out) {
  const lptr sm = (lptr)smem_raw;
  const int b = blockIdx.x, t = threadIdx.x, l = t & 63, w = __builtin_amdgcn_readfirstlane(t >> 6);
  const int xcd = b & 7, j = b >> 3, tb = (xcd >> 1) * 4 + (j & 3), nt = (xcd & 1) * 8 + (j >> 2);
  const int hi = l >> 5, tb32 = tb * 4 + (w & 3), n0 = nt * 64 + (w >> 2) * 32;
  const size_t off = (size_t)(tb32 * 64 + (n0 >> 4)) * 1024 + l * 16;
  const u32x4 xa = *(const u32x4*)(x1b + off), xb = *(const u32x4*)(x1b + off + 1024);
  float gv = 0.f;
  if (t < 64) { const int col = 5 * DM + nt * 64 + t; gv = (modp[col] + modp[NMOD + col]) + (modp[2 * NMOD + col] + modp[3 * NMOD + col]); *(LDS float*)(sm + K5_MV + t * 4) = rbf(gv); }
  f32x16 acc = gemmA<56, DM, 0>(sm, gb + (size_t)tb * 4 * 224 * 1024, w2 + nt * 128, l, w, [] {});
  char* d = out + (size_t)(tb32 * 32 + (l & 31)) * 2048 + (n0 + 4 * hi) * 2;
#pragma unroll
  for (int q = 0; q < 4; ++q) { const f32x4 g = *(const LDS f32x4*)(sm + K5_MV + ((w >> 2) * 32 + 8 * q + 4 * hi) * 4);
    const unsigned u0 = q < 2 ? xa[2 * q] : xb[2 * q - 4], u1 = q < 2 ? xa[2 * q + 1] : xb[2 * q - 3];
    *(u32x2*)(d + q * 16) = (u32x2){pk(blo(u0) + g[0] * acc[4 * q], bhi(u0) + g[1] * acc[4 * q + 1]), pk(blo(u1) + g[2] * acc[4 * q + 2], bhi(u1) + g[3] * acc[4 * q + 3])}; }
}

extern "C" void kernel_launch(void* const* d_in, const int* in_sizes, int n_in, void* d_out, int out_size, void* d_ws, size_t ws_size, hipStream_t stream) {
  const char* x = (const char*)d_in[0]; const char* wq = (const char*)d_in[1]; const char* wk = (const char*)d_in[2]; const char* wv = (const char*)d_in[3];
  const char* wo = (const char*)d_in[4]; const char* w1 = (const char*)d_in[6]; const char* w2 = (const char*)d_in[7];
  const uint16_t* c = (const uint16_t*)d_in[8]; const char* wmod = (const char*)d_in[9];
  char* ws = (char*)d_ws;
  (void)hipFuncSetAttribute((const void*)k_ln1kv, hipFuncAttributeMaxDynamicSharedMemorySize, K1_LDS);
  (void)hipFuncSetAttribute((const void*)k_qattn, hipFuncAttributeMaxDynamicSharedMemorySize, K2_LDS);
  (void)hipFuncSetAttribute((const void*)k_oproj, hipFuncAttributeMaxDynamicSharedMemorySize, K3_LDS);
  (void)hipFuncSetAttribute((const void*)k_ffn1, hipFuncAttributeMaxDynamicSharedMemorySize, K4_LDS);
  (void)hipFuncSetAttribute((const void*)k_ffn2, hipFuncAttributeMaxDynamicSharedMemorySize, K5_LDS);
  float* modp = (float*)(ws + WS_MODP);
  k_mod<<<768, 256, 0, stream>>>(c, wmod, modp);
  k_ln1kv<<<256, 256, K1_LDS, stream>>>(x, wk, wv, (const char*)modp, ws + WS_H, ws + WS_KV);
  k_qattn<<<256, 512, K2_LDS, stream>>>(ws + WS_H, wq, ws + WS_KV, ws + WS_O);
  k_oproj<<<256, 512, K3_LDS, stream>>>(ws + WS_O, wo, x, modp, w1, (float*)(ws + WS_UTP), ws + WS_X1, ws + WS_Y, (f32x2*)(ws + WS_STAT));
  k_ffn1<<<256, 256, K4_LDS, stream>>>(ws + WS_Y, w1, (const f32x2*)(ws + WS_STAT), (const float*)(ws + WS_UTP), ws + WS_G);
  k_ffn2<<<256, 512, K5_LDS, stream>>>(ws + WS_G, w2, ws + WS_X1, modp, (char*)d_out);
}
```

```cpp
#include <hip/hip_runtime.h>
#include <hip/hip_bf16.h>
#include <stdint.h>

typedef __attribute__((ext_vector_type(2))) float f32x2;
typedef __attribute__((ext_vector_type(4))) float f32x4;
typedef __attribute__((ext_vector_type(16))) float f32x16;
typedef __attribute__((ext_vector_type(2))) __bf16 bf16x2;
typedef __attribute__((ext_vector_type(8))) __bf16 bf16x8;
typedef __attribute__((ext_vector_type(4))) short s16x4;
typedef __attribute__((ext_vector_type(2))) unsigned u32x2;
typedef __attribute__((ext_vector_type(4))) unsigned u32x4;
#define LDS __attribute__((address_space(3)))
typedef LDS char* lptr;
#define PREFETCH 1
#define DEV __device__ __forceinline__

constexpr int SEQ = 2048, DM = 1024, FF = 3584, NMOD = 6 * DM;
constexpr size_t WS_MODP = 0;
constexpr size_t WS_UTP = 98304;
constexpr size_t WS_MODF = 212992;
constexpr size_t WS_MODH = 237568;
constexpr size_t WS_STAT = 262144;
constexpr size_t WS_KV = 786432;
constexpr size_t WS_H = (size_t)1 << 20;
constexpr size_t WS_O = (size_t)5 << 20;
constexpr size_t WS_X1 = (size_t)9 << 20;
constexpr size_t WS_Y = (size_t)13 << 20;
constexpr size_t WS_G = (size_t)17 << 20;

DEV unsigned pk(float a, float b) { return __builtin_bit_cast(unsigned, __builtin_convertvector((f32x2){a, b}, bf16x2)); }
DEV float rbf(float a) { return __uint_as_float(pk(a, 0.f) << 16); }
DEV float blo(unsigned u) { return __uint_as_float(u << 16); }
DEV float bhi(unsigned u) { return __uint_as_float(u & 0xffff0000u); }
DEV float silu(float z) { return z * __builtin_amdgcn_rcpf(1.f + __builtin_amdgcn_exp2f(-1.4426950408889634f * z)); }
DEV f32x16 mfma32(bf16x8 a, bf16x8 b, f32x16 c) { return __builtin_amdgcn_mfma_f32_32x32x16_bf16(a, b, c, 0, 0, 0); }
DEV f32x4 mfma16(bf16x8 a, bf16x8 b, f32x4 c) { return __builtin_amdgcn_mfma_f32_16x16x32_bf16(a, b, c, 0, 0, 0); }
DEV bf16x8 asb(u32x4 v) { return __builtin_bit_cast(bf16x8, v); }
DEV bf16x8 ldsA(lptr p) { return *(const LDS bf16x8*)p; }
DEV bf16x8 ldsT(lptr p, int second) {
  s16x4 a = __builtin_amdgcn_ds_read_tr16_b64_v4i16((LDS s16x4*)p);
  s16x4 b = __builtin_amdgcn_ds_read_tr16_b64_v4i16((LDS s16x4*)(p + second));
  return __builtin_bit_cast(bf16x8, __builtin_shufflevector(a, b, 0, 1, 2, 3, 4, 5, 6, 7));
}
template <int C> DEV float dppadd(float v) { return v + __int_as_float(__builtin_amdgcn_update_dpp(0, __float_as_int(v), C, 0xf, 0xf, true)); }
DEV float swap32add(float v) { auto r = __builtin_amdgcn_permlane32_swap(__float_as_uint(v), __float_as_uint(v), false, false); return __uint_as_float(r[0]) + __uint_as_float(r[1]); }
DEV float swap16add(float v) { auto r = __builtin_amdgcn_permlane16_swap(__float_as_uint(v), __float_as_uint(v), false, false); return __uint_as_float(r[0]) + __uint_as_float(r[1]); }
DEV float swap32max(float v) { auto r = __builtin_amdgcn_permlane32_swap(__float_as_uint(v), __float_as_uint(v), false, false); return __builtin_fmaxf(__uint_as_float(r[0]), __uint_as_float(r[1])); }
DEV float wavesum(float v) { v = dppadd<0xB1>(v); v = dppadd<0x4E>(v); v = dppadd<0x141>(v); v = dppadd<0x140>(v); v = swap16add(v); return swap32add(v); }

#define SETM0(x) asm volatile("s_mov_b32 m0, %0\n\ts_nop 0" ::"s"(x) : "memory")
template <int OFF> DEV void glds(unsigned voff, const void* base) { asm volatile("global_load_lds_dwordx4 %0, %1 offset:%2" ::"v"(voff), "s"(base), "i"(OFF) : "memory"); }
template <int N> DEV void waitbar() { asm volatile("s_waitcnt vmcnt(%0) lgkmcnt(0)\n\ts_barrier" ::"i"(N) : "memory"); }
template <int OFF> DEV u32x4 gload(unsigned voff, const void* base) { u32x4 r; asm volatile("global_load_dwordx4 %0, %1, %2 offset:%3" : "=v"(r) : "v"(voff), "s"(base), "i"(OFF) : "memory"); return r; }
#define SB() __builtin_amdgcn_sched_barrier(0)
extern __shared__ __attribute__((aligned(16))) char smem_raw[];

struct Stamps {};
#define ST_INIT() Stamps S_
#define ST(i)
#define ST_END(k)

__global__ __launch_bounds__(256) void k_mod(const uint16_t* __restrict__ c, const char* __restrict__ wmod, float* __restrict__ modp) {
  __shared__ float red[4][32];
  ST_INIT();
  const int b = blockIdx.x, t = threadIdx.x, xcd = b & 7, j = b >> 3;
  const int cg = (j & 1) + 2 * (xcd + 8 * ((j >> 1) % 12)), ks = j / 24;
  const int row0 = ks * 256 + (t >> 2);
  const char* p = wmod + (size_t)row0 * (NMOD * 2) + cg * 64 + (t & 3) * 16;
  u32x4 w[4]; float s[4];
#pragma unroll
  for (int i = 0; i < 4; ++i) { w[i] = *(const u32x4*)(p + (size_t)i * 64 * NMOD * 2); s[i] = __uint_as_float((unsigned)c[row0 + 64 * i] << 16); }
  float a[8] = {0, 0, 0, 0, 0, 0, 0, 0};
#pragma unroll
  for (int i = 0; i < 4; ++i) { const float f = rbf(silu(s[i]));
#pragma unroll
    for (int e = 0; e < 4; ++e) { a[2 * e] += f * blo(w[i][e]); a[2 * e + 1] += f * bhi(w[i][e]); } }
#pragma unroll
  for (int e = 0; e < 8; ++e) { float v = a[e]; v += __shfl_xor(v, 4); v += __shfl_xor(v, 8); v = swap16add(v); a[e] = swap32add(v); }
  if ((t & 63) < 4) {
#pragma unroll
    for (int e = 0; e < 8; ++e) red[t >> 6][(t & 3) * 8 + e] = a[e]; }
  __syncthreads();
  if (t < 32) modp[ks * NMOD + cg * 32 + t] = (red[0][t] + red[1][t]) + (red[2][t] + red[3][t]);
  ST_END(0);
}

constexpr int K1_W = 0, K1_H = 65536, K1_MV = K1_H + 64 * 528, K1_RED = K1_MV + 8192, K1_LDS = K1_RED + 32768;
__global__ __launch_bounds__(512, 2) void k_ln1kv(const char* __restrict__ x, const char* __restrict__ wk, const char* __restrict__ wv,
                                                  const char* __restrict__ modp, float* __restrict__ modf, uint16_t* __restrict__ modh,
                                                  char* __restrict__ hbuf, char* __restrict__ kv) {
  const lptr sm = (lptr)smem_raw;
  ST_INIT();
  const int b = blockIdx.x, t = threadIdx.x, l = t & 63, w = __builtin_amdgcn_readfirstlane(t >> 6);
  const int which = (b >> 3) & 1, rb = (b & 7) * 16 + (b >> 4);
  u32x4 xv[4], mp[4];
  { const char* xb = x + (size_t)(rb * 16 + 2 * w) * 2048; const unsigned vo = l * 32;
    xv[0] = gload<0>(vo, xb); xv[1] = gload<16>(vo, xb); xv[2] = gload<2048>(vo, xb); xv[3] = gload<2064>(vo, xb);
#pragma unroll
    for (int s = 0; s < 4; ++s) mp[s] = gload<0>(t * 16, modp + s * NMOD * 4); }
  { const char* wsrc = (which ? wv : wk) + w * 8192;
#pragma unroll
    for (int g = 0; g < 2; ++g) { SETM0(K1_W + w * 8192 + g * 4096); const char* s = wsrc + g * 4096;
      glds<0>(l * 16, s); glds<1024>(l * 16, s); glds<2048>(l * 16, s); glds<3072>(l * 16, s); } }
  asm volatile("s_waitcnt vmcnt(8)" : "+v"(xv[0]), "+v"(xv[1]), "+v"(xv[2]), "+v"(xv[3]), "+v"(mp[0]), "+v"(mp[1]), "+v"(mp[2]), "+v"(mp[3])::"memory");
  ST(1);
  { f32x4 o;
#pragma unroll
    for (int e = 0; e < 4; ++e) { const float v = rbf((__uint_as_float(mp[0][e]) + __uint_as_float(mp[1][e])) + (__uint_as_float(mp[2][e]) + __uint_as_float(mp[3][e])));
      o[e] = t >= 256 ? rbf(1.f + v) : v; }
    *(LDS f32x4*)(sm + K1_MV + (t & 256) * 16 + (t & 3) * 1024 + ((t & 255) >> 2) * 16) = o; }
  f32x2 v[2][8]; float rs[2];
#pragma unroll
  for (int r = 0; r < 2; ++r) { f32x2 s = {0.f, 0.f};
#pragma unroll
    for (int e = 0; e < 8; ++e) { const unsigned u = xv[2 * r + (e >> 2)][e & 3]; v[r][e] = (f32x2){blo(u), bhi(u)}; s += v[r][e]; }
    const float m = wavesum(s[0] + s[1]) * (1.f / 1024.f); f32x2 q = {0.f, 0.f};
#pragma unroll
    for (int e = 0; e < 8; ++e) { v[r][e] -= m; q += v[r][e] * v[r][e]; }
    rs[r] = __builtin_amdgcn_rsqf(wavesum(q[0] + q[1]) * (1.f / 1024.f) + 1e-5f); }
  asm volatile("s_waitcnt lgkmcnt(0)\n\ts_barrier" ::: "memory");
  { unsigned u[2][8];
#pragma unroll
    for (int e = 0; e < 4; ++e) { const f32x4 a = *(const LDS f32x4*)(sm + K1_MV + e * 1024 + l * 16), cc = *(const LDS f32x4*)(sm + K1_MV + 4096 + e * 1024 + l * 16);
#pragma unroll
      for (int r = 0; r < 2; ++r)
#pragma unroll
        for (int i = 0; i < 2; ++i) { const f32x2 h = v[r][2 * e + i] * rs[r] * (f32x2){cc[2 * i], cc[2 * i + 1]} + (f32x2){a[2 * i], a[2 * i + 1]}; u[r][2 * e + i] = pk(h[0], h[1]); } }
#pragma unroll
    for (int r = 0; r < 2; ++r) { const lptr d = sm + K1_H + l * 528 + (2 * w + r) * 16;
      *(LDS u32x4*)d = (u32x4){u[r][0], u[r][1], u[r][4], u[r][5]}; *(LDS u32x4*)(d + 256) = (u32x4){u[r][2], u[r][3], u[r][6], u[r][7]}; } }
  ST(2); waitbar<0>(); ST(3);
  f32x16 acc = {};
  { const int hi = l >> 5; const lptr hp = sm + K1_H + w * 8 * 528 + hi * 256 + (l & 15) * 16;
    const lptr wp = sm + K1_W + (w * 128 + 4 * hi + ((l >> 2) & 3)) * 64 + ((l >> 4) & 1) * 32 + (l & 3) * 8;
    bf16x8 fa[8], fb[8];
#pragma unroll
    for (int i = 0; i < 8; ++i) { fa[i] = ldsA(hp + i * 528); fb[i] = ldsT(wp + i * 1024, 512); }
#pragma unroll
    for (int i = 0; i < 2; ++i) { const int id = t + 512 * i, tok = id & 15, h2 = (id >> 4) & 1, kb = which * 32 + (id >> 5);
      *(u32x4*)(hbuf + (size_t)((rb >> 1) * 64 + kb) * 1024 + (h2 * 32 + (rb & 1) * 16 + tok) * 16) = *(const LDS u32x4*)(sm + K1_H + kb * 528 + h2 * 256 + tok * 16); }
    if (which == 0) {
#pragma unroll
      for (int i = 0; i < 8; ++i) acc = mfma32(fb[i], fa[i], acc);
    } else {
#pragma unroll
      for (int i = 0; i < 8; ++i) acc = mfma32(fa[i], fb[i], acc);
    } }
#pragma unroll
  for (int q = 0; q < 4; ++q) *(LDS f32x4*)(sm + K1_RED + w * 4096 + q * 1024 + l * 16) = (f32x4){acc[4 * q], acc[4 * q + 1], acc[4 * q + 2], acc[4 * q + 3]};
  asm volatile("s_waitcnt lgkmcnt(0)\n\ts_barrier" ::: "memory");
  if (w == 0) {
    const lptr rp = sm + K1_RED + (which ? l * 16 : (l >> 5) * 2048 + (((l >> 4) & 1) * 32 + (l & 15)) * 16);
    f32x4 a = {}, c = {};
#pragma unroll
    for (int u = 0; u < 8; ++u) { a += *(const LDS f32x4*)(rp + u * 4096); c += *(const LDS f32x4*)(rp + u * 4096 + 1024); }
    char* base = kv + (rb >> 1) * 4096;
    char* d = which ? base + 2048 + (rb & 1) * 1024 + l * 16 : base + (l >> 5) * 1024 + (((l >> 4) & 1) * 32 + (rb & 1) * 16 + (l & 15)) * 16;
    *(u32x4*)d = (u32x4){pk(a[0], a[1]), pk(a[2], a[3]), pk(c[0], c[1]), pk(c[2], c[3])};
  } else if (b < 64 && w == 1) {
    const int col = 2048 + b * 64 + l; const float* mpf = (const float*)modp;
    float v2 = rbf((mpf[col] + mpf[NMOD + col]) + (mpf[2 * NMOD + col] + mpf[3 * NMOD + col]));
    if ((col >> 10) == 4) v2 = rbf(1.f + v2);
    modf[col] = v2; modh[col] = (uint16_t)(__float_as_uint(v2) >> 16);
  }
  ST_END(1);
}

DEV void bar() { asm volatile("s_waitcnt lgkmcnt(0)\n\ts_barrier" ::: "memory"); }
template <int N, int G, int EXTRA> DEV void loader_tail() { waitbar<G * N + EXTRA>(); if constexpr (N > 0) loader_tail<N - 1, G, EXTRA>(); }
template <int KT, int S, int G, int STAGE, int EXTRA, class I, class X>
DEV void loader(I&& issue, X&& extra) {
#pragma unroll
  for (int s = 0; s < S; ++s) issue(s, s * STAGE);
  waitbar<G*(S - 1)>();
  int slot = 0;
  for (int t = 1; t <= KT - S; ++t) { waitbar<G*(S - 2)>(); issue(t - 1 + S, slot); slot = slot == (S - 1) * STAGE ? 0 : slot + STAGE; }
  extra();
  loader_tail<S - 2, G, EXTRA>();
}
constexpr int GA_STAGE = 24576;
template <int KT, int S, int LDW, int EXTRA, class X>
DEV void gemmA_load(const char* act, const char* wgt, int l, int lw, X&& extra) {
  const char* ap = act + (size_t)lw * (KT * 4096);
  const char* wp = wgt + (size_t)lw * (16 * LDW * 2);
  const unsigned va = l * 16, vb = (l >> 3) * (LDW * 2) + (((l & 7) ^ (((l >> 4) & 1) << 2)) * 16);
  loader<KT, S, 6, GA_STAGE, EXTRA>([&](int t, int slot) {
    SETM0(slot + lw * 4096); const char* a = ap + (size_t)t * 4096; glds<0>(va, a); glds<1024>(va, a); glds<2048>(va, a); glds<3072>(va, a);
    SETM0(slot + 16384 + lw * 2048); const char* s = wp + (size_t)t * (64 * LDW * 2); glds<0>(vb, s); glds<1024>(vb, s + 8 * LDW * 2 - 1024); }, extra);
}
struct FragA { bf16x8 a0[4], a1[4], b[4]; };
template <int KT, int S>
DEV void gemmA_compute(const lptr sm, int l, int w, f32x16& c0, f32x16& c1, Stamps& S_) {
  const int q = (l >> 2) & 3, p = l & 3;
  const lptr ra = sm + (w & 1) * 8192 + l * 16;
  const lptr rb = sm + 16384 + (4 * (l >> 5) + q) * 128 + ((((w >> 1) * 4 + ((l >> 4) & 1) * 2 + (p >> 1)) ^ ((q >> 1) << 2)) * 16) + (p & 1) * 8;
  c0 = (f32x16){}; c1 = (f32x16){};
  FragA f, g; int slot = 0;
  auto rd = [&](FragA& d, int k) { d.a0[k] = ldsA(ra + slot + k * 1024); d.b[k] = ldsT(rb + slot + k * 2048, 1024); d.a1[k] = ldsA(ra + slot + 4096 + k * 1024); };
  auto step = [&](FragA& cur, FragA& nxt) { bar(); slot = slot == (S - 1) * GA_STAGE ? 0 : slot + GA_STAGE;
#pragma unroll
    for (int k = 0; k < 4; ++k) { rd(nxt, k); c0 = mfma32(cur.b[k], cur.a0[k], c0); c1 = mfma32(cur.b[k], cur.a1[k], c1); SB(); } };
  bar(); ST(2);
#pragma unroll
  for (int k = 0; k < 4; ++k) rd(f, k);
  for (int t = 0; t < KT - 2; t += 2) { step(f, g); step(g, f); }
  step(f, g);
#pragma unroll
  for (int k = 0; k < 4; ++k) { c0 = mfma32(g.b[k], g.a0[k], c0); c1 = mfma32(g.b[k], g.a1[k], c1); }
  ST(3);
}

constexpr int K2_KV0 = 98304, K2_KV1 = 131072, K2_KV2 = 0, K2_UW = 32768, K2_Q = 65536, K2_RED = 73728, K2_DUMP = 81920, K2_LDS = 163840;
DEV void pack16(const f32x16& s, u32x4& a, u32x4& b) {
  a = (u32x4){pk(s[0], s[1]), pk(s[2], s[3]), pk(s[4], s[5]), pk(s[6], s[7])};
  b = (u32x4){pk(s[8], s[9]), pk(s[10], s[11]), pk(s[12], s[13]), pk(s[14], s[15])}; }
DEV float max16(const f32x16& s) { float m = s[0];
#pragma unroll
  for (int i = 1; i < 16; ++i) m = __builtin_fmaxf(m, s[i]);
  return swap32max(m); }
#define EX2(v) __builtin_amdgcn_exp2f(v)
#define PIN(x) asm volatile("" : "+v"(x))

__global__ __launch_bounds__(512, 2) void k_qattn(const char* __restrict__ hbuf, const char* __restrict__ wq, const char* __restrict__ kv, char* __restrict__ obuf,
                                                  const char* __restrict__ w1, const char* __restrict__ modh, float* __restrict__ utp,
                                                  const char* __restrict__ wo, const char* __restrict__ w2) {
  const lptr sm = (lptr)smem_raw;
  ST_INIT();
  const int b = blockIdx.x, l = threadIdx.x & 63, w = __builtin_amdgcn_readfirstlane(threadIdx.x >> 6);
  const int xcd = b & 7, j = b >> 3, tb = (xcd >> 1) * 4 + (j & 3), hp = (xcd & 1) * 8 + (j >> 2);
  const unsigned l16 = l * 16;
  u32x4 q0u, q1u;
  if (w >= 4) {
    gemmA_load<16, 4, DM, 8>(hbuf + (size_t)tb * 4 * 64 * 1024, wq + hp * 128, l, w - 4, [&] {
#pragma unroll
      for (int g = 0; g < 2; ++g) { SETM0(K2_KV0 + (w - 4) * 8192 + g * 4096); const char* s = kv + (w - 4) * 8192 + g * 4096;
        glds<0>(l16, s); glds<1024>(l16, s); glds<2048>(l16, s); glds<3072>(l16, s); } });
    waitbar<0>();
    q0u = *(const LDS u32x4*)(sm + K2_Q + (w - 4) * 2048 + l16); q1u = *(const LDS u32x4*)(sm + K2_Q + (w - 4) * 2048 + 1024 + l16);
  } else {
    f32x16 c0, c1; gemmA_compute<16, 4>(sm, l, w, c0, c1, S_);
    constexpr float QS = 0.17677669529663687f * 1.4426950408889634f;
    c0 *= QS; c1 *= QS;
    u32x4 a, c; pack16(c1, a, c); *(LDS u32x4*)(sm + K2_Q + w * 2048 + l16) = a; *(LDS u32x4*)(sm + K2_Q + w * 2048 + 1024 + l16) = c;
    pack16(c0, q0u, q1u);
    bar();
  }
  const bf16x8 q0 = asb(q0u), q1 = asb(q1u);
  const char* kvw = kv + w * 4096;
  auto kvload = [&](int st, int slot) { SETM0(slot + w * 4096); const char* s = kvw + st * 32768; glds<0>(l16, s); glds<1024>(l16, s); glds<2048>(l16, s); glds<3072>(l16, s); };
  kvload(1, K2_KV1);
  const unsigned one2 = ((l & 7) == ((l >> 4) & 1) * 4) ? 0x3f803f80u : 0u;
  const bf16x8 ones = asb((u32x4){one2, one2, one2, one2});
  const lptr kp0 = sm + K2_KV0 + l16, kp1 = sm + K2_KV1 + l16, kp2 = sm + K2_KV2 + l16;
  bf16x8 k0 = ldsA(kp0), k1 = ldsA(kp0 + 1024), v0 = ldsA(kp0 + 2048), v1 = ldsA(kp0 + 3072);
  f32x16 negm, sA, sB, o = {}; f32x4 ls = {};
  { f32x16 z = {}; z = mfma32(k0, q0, z); z = mfma32(k1, q1, z); const float m = -max16(z);
#pragma unroll
    for (int i = 0; i < 16; ++i) negm[i] = m; }
  PIN(negm);
  sA = mfma32(k0, q0, negm); sA = mfma32(k1, q1, sA);
  k0 = ldsA(kp0 + 4096); k1 = ldsA(kp0 + 4096 + 1024);
  u32x4 p0, p1 = {0, 0, 0, 0}; bf16x8 v1p = v1;
#define TILE(S, N, KP, VP)                                                                                            \
  { N = mfma32(k0, q0, negm); SB();                                                                                  \
    S[0] = EX2(S[0]); S[1] = EX2(S[1]); S[2] = EX2(S[2]); S[3] = EX2(S[3]); p0[0] = pk(S[0], S[1]); p0[1] = pk(S[2], S[3]); PIN(p0); SB(); \
    o = mfma32(v1p, asb(p1), o); k0 = ldsA(KP); SB();                                                                \
    S[4] = EX2(S[4]); S[5] = EX2(S[5]); S[6] = EX2(S[6]); S[7] = EX2(S[7]); p0[2] = pk(S[4], S[5]); p0[3] = pk(S[6], S[7]); PIN(p0); SB(); \
    ls = mfma16(ones, asb(p1), ls); v1p = v1; v1 = ldsA((VP) + 1024); SB();                                          \
    S[8] = EX2(S[8]); S[9] = EX2(S[9]); PIN(S); SB();                                                                \
    N = mfma32(k1, q1, N); SB();                                                                                     \
    S[10] = EX2(S[10]); S[11] = EX2(S[11]); S[12] = EX2(S[12]); S[13] = EX2(S[13]); p1[0] = pk(S[8], S[9]); p1[1] = pk(S[10], S[11]); PIN(p1); PIN(S); SB(); \
    o = mfma32(v0, asb(p0), o); k1 = ldsA((KP) + 1024); SB();                                                        \
    S[14] = EX2(S[14]); S[15] = EX2(S[15]); p1[2] = pk(S[12], S[13]); p1[3] = pk(S[14], S[15]); PIN(p1); SB();       \
    ls = mfma16(ones, asb(p0), ls); v0 = ldsA(VP); SB(); }
#define STAGE(P, Q, ...)                                                                                              \
  TILE(sA, sB, P + 2 * 4096, P + 1 * 4096 + 2048) TILE(sB, sA, P + 3 * 4096, P + 2 * 4096 + 2048)                     \
  TILE(sA, sB, P + 4 * 4096, P + 3 * 4096 + 2048) TILE(sB, sA, P + 5 * 4096, P + 4 * 4096 + 2048)                     \
  TILE(sA, sB, P + 6 * 4096, P + 5 * 4096 + 2048) TILE(sB, sA, P + 7 * 4096, P + 6 * 4096 + 2048)                     \
  { __VA_ARGS__ }                                                                                                     \
  TILE(sA, sB, Q, P + 7 * 4096 + 2048) TILE(sB, sA, Q + 4096, Q + 2048)
  const bool ut = b < 224; const int cg = b % 56, ks = b / 56;
  u32x2 vf[4] = {{0, 0}, {0, 0}, {0, 0}, {0, 0}};
  ST(4);
  STAGE(kp0, kp1, waitbar<0>(); kvload(2, K2_KV2);
        if (ut) { SETM0(K2_UW + w * 4096); const char* s = w1 + (size_t)(ks * 256 + w * 32) * (FF * 2) + cg * 128;
          const unsigned vo = (l >> 3) * (FF * 2) + (((l & 7) ^ (((l >> 4) & 1) << 2)) * 16);
          glds<0>(vo, s); glds<1024>(vo, s + 8 * FF * 2 - 1024); glds<2048>(vo, s + 16 * FF * 2 - 2048); glds<3072>(vo, s + 24 * FF * 2 - 3072);
          if ((l & 31) < 2) { const char* mv = modh + ((4 - (l & 31)) * DM + ks * 256 + w * 32 + 4 * (l >> 5)) * 2;
            _Pragma("unroll") for (int i = 0; i < 4; ++i) vf[i] = *(const u32x2*)(mv + i * 16); } })
  STAGE(kp1, kp2, waitbar<8>(); kvload(3, K2_KV0);)
  STAGE(kp2, kp0, waitbar<0>();
        if (ut) { const int q = (l >> 2) & 3; const int p = l & 3; f32x16 d0 = {}; f32x16 d1 = {};
          const lptr rb = sm + K2_UW + (w * 32 + 4 * (l >> 5) + q) * 128 + (((((l >> 4) & 1) * 2 + (p >> 1)) ^ ((q >> 1) << 2)) * 16) + (p & 1) * 8;
          const lptr rc = sm + K2_UW + (w * 32 + 4 * (l >> 5) + q) * 128 + (((4 + ((l >> 4) & 1) * 2 + (p >> 1)) ^ ((q >> 1) << 2)) * 16) + (p & 1) * 8;
          _Pragma("unroll") for (int kk = 0; kk < 2; ++kk) { const bf16x8 vv = asb((u32x4){vf[2 * kk][0], vf[2 * kk][1], vf[2 * kk + 1][0], vf[2 * kk + 1][1]});
            d0 = mfma32(ldsT(rb + kk * 2048, 1024), vv, d0); d1 = mfma32(ldsT(rc + kk * 2048, 1024), vv, d1); }
          if ((l & 31) < 2) { const lptr d = sm + K2_RED + w * 512 + (l & 31) * 256 + (l >> 5) * 16;
            _Pragma("unroll") for (int i = 0; i < 4; ++i) { *(LDS f32x4*)(d + i * 32) = (f32x4){d0[4 * i], d0[4 * i + 1], d0[4 * i + 2], d0[4 * i + 3]};
              *(LDS f32x4*)(d + 128 + i * 32) = (f32x4){d1[4 * i], d1[4 * i + 1], d1[4 * i + 2], d1[4 * i + 3]}; } } }
        kvload(4, K2_KV1);
        if (PREFETCH) { SETM0(K2_DUMP + w * 1024); glds<0>(l16, wo + b * 8192 + w * 1024); glds<0>(l16, w2 + (size_t)b * 28672 + w * 1024); })
  STAGE(kp0, kp1, waitbar<2>();
        if (ut && w < 2) { float v = 0.f;
          _Pragma("unroll") for (int u = 0; u < 8; ++u) v += *(const LDS float*)(sm + K2_RED + u * 512 + w * 256 + l * 4);
          utp[(ks * 2 + w) * FF + cg * 64 + l] = v; }
        kvload(5, K2_KV2);
        if (PREFETCH) { SETM0(K2_DUMP + w * 1024); glds<0>(l16, w2 + (size_t)b * 28672 + 8192 + w * 1024); glds<0>(l16, w2 + (size_t)b * 28672 + 16384 + w * 1024); })
  STAGE(kp1, kp2, waitbar<2>(); kvload(6, K2_KV0);
        if (PREFETCH) { SETM0(K2_DUMP + w * 1024); glds<0>(l16, w2 + (size_t)b * 28672 + 24576 + (w & 3) * 1024); })
  STAGE(kp2, kp0, waitbar<1>(); kvload(7, K2_KV1);)
  STAGE(kp0, kp1, waitbar<0>();)
  STAGE(kp1, kp2, )
  ST(5);
  o = mfma32(v1p, asb(p1), o); ls = mfma16(ones, asb(p1), ls);
  float lsum = ls[0];
  { float chk = lsum;
#pragma unroll
    for (int i = 0; i < 16; ++i) chk = __builtin_fmaxf(chk, __builtin_fabsf(o[i]) * 0x1p-40f);
    bool bad = !(chk < 0x1p80f);
#pragma unroll
    for (int i = 0; i < 16; ++i) bad |= (o[i] != o[i]);
    bad |= (lsum != lsum);
    if (__any(bad)) {
      float m = -INFINITY; lsum = 0.f; o = (f32x16){};
      for (int t = 0; t < 64; ++t) { const bf16x8* g = (const bf16x8*)(kv + t * 4096) + l;
        f32x16 s = {}; s = mfma32(g[0], q0, s); s = mfma32(g[64], q1, s);
        const float mn = __builtin_fmaxf(m, max16(s)), al = EX2(m - mn); m = mn; float rsum = 0.f;
#pragma unroll
        for (int i = 0; i < 16; ++i) { s[i] = rbf(EX2(s[i] - mn)); rsum += s[i]; o[i] *= al; }
        lsum = lsum * al + swap32add(rsum);
        u32x4 a, c; pack16(s, a, c); o = mfma32(g[128], asb(a), o); o = mfma32(g[192], asb(c), o); } } }
  o *= 1.f / lsum;
  u32x4 o0, o1; pack16(o, o0, o1);
  char* d = obuf + (size_t)((tb * 4 + 2 * (w & 1) + (w >> 2)) * 64 + (hp * 2 + ((w >> 1) & 1)) * 2) * 1024 + l16;
  *(u32x4*)d = o0; *(u32x4*)(d + 1024) = o1;
  ST_END(2);
}

DEV u32x4 rowswap(u32x4 v) { auto a = __builtin_amdgcn_permlane32_swap(v[0], v[2], false, false); auto c = __builtin_amdgcn_permlane32_swap(v[1], v[3], false, false);
  return (u32x4){a[0], c[0], a[1], c[1]}; }

constexpr int K3_S = 6, K3_LDS = K3_S * GA_STAGE;
__global__ __launch_bounds__(512, 2) void k_oproj(const char* __restrict__ obuf, const char* __restrict__ wo, const char* __restrict__ x, const float* __restrict__ modf,
                                                  char* __restrict__ x1b, char* __restrict__ yb, f32x2* __restrict__ stat) {
  const lptr sm = (lptr)smem_raw;
  ST_INIT();
  const int b = blockIdx.x, l = threadIdx.x & 63, w = __builtin_amdgcn_readfirstlane(threadIdx.x >> 6);
  const int xcd = b & 7, j = b >> 3, tb = (xcd >> 1) * 4 + (j & 3), nt = (xcd & 1) * 8 + (j >> 2);
  if (w >= 4) { gemmA_load<16, K3_S, DM, 0>(obuf + (size_t)tb * 4 * 64 * 1024, wo + nt * 128, l, w - 4, [] {}); return; }
  const int hi = l >> 5, tb32 = tb * 4 + 2 * (w & 1), n0 = nt * 64 + (w >> 1) * 32;
  u32x4 xr[2][2]; f32x4 ga[4], cb[4];
#pragma unroll
  for (int i = 0; i < 2; ++i)
#pragma unroll
    for (int h = 0; h < 2; ++h) xr[i][h] = *(const u32x4*)(x + (size_t)((tb32 + i) * 32 + (l & 31)) * 2048 + (n0 + 16 * h + 8 * hi) * 2);
#pragma unroll
  for (int q = 0; q < 4; ++q) { ga[q] = *(const f32x4*)(modf + 2 * DM + n0 + 8 * q + 4 * hi); cb[q] = *(const f32x4*)(modf + 4 * DM + n0 + 8 * q + 4 * hi); }
  f32x16 acc[2]; gemmA_compute<16, K3_S>(sm, l, w, acc[0], acc[1], S_);
#pragma unroll
  for (int i = 0; i < 2; ++i) { unsigned xo[8], yo[8]; float x1[16], s = 0.f;
#pragma unroll
    for (int h = 0; h < 2; ++h) { const u32x4 xs = rowswap(xr[i][h]);
#pragma unroll
      for (int e = 0; e < 4; ++e) { const int r = 8 * h + 2 * e, q = r >> 2, c = r & 3;
        const unsigned u = pk(blo(xs[e]) + ga[q][c] * acc[i][r], bhi(xs[e]) + ga[q][c + 1] * acc[i][r + 1]);
        x1[r] = blo(u); x1[r + 1] = bhi(u); xo[4 * h + e] = u; yo[4 * h + e] = pk(x1[r] * cb[q][c], x1[r + 1] * cb[q][c + 1]); s += x1[r] + x1[r + 1]; } }
    const size_t off = (size_t)((tb32 + i) * 64 + (n0 >> 4)) * 1024 + l * 16;
    *(u32x4*)(yb + off) = (u32x4){yo[0], yo[1], yo[2], yo[3]}; *(u32x4*)(yb + off + 1024) = (u32x4){yo[4], yo[5], yo[6], yo[7]};
    *(u32x4*)(x1b + off) = (u32x4){xo[0], xo[1], xo[2], xo[3]}; *(u32x4*)(x1b + off + 1024) = (u32x4){xo[4], xo[5], xo[6], xo[7]};
    const float mean = swap32add(s) * (1.f / 32.f); float m2 = 0.f;
#pragma unroll
    for (int e = 0; e < 16; ++e) m2 += (x1[e] - mean) * (x1[e] - mean);
    m2 = swap32add(m2);
    if (hi == 0) stat[((tb32 + i) * 32 + (n0 >> 5)) * 32 + l] = (f32x2){mean, m2}; }
  ST_END(3);
}

constexpr int K4_STAGE = 45056, K4_UT = 3 * K4_STAGE, K4_LDS = K4_UT + 2048;
struct FragB { bf16x8 a, b[7]; };
__global__ __launch_bounds__(512, 2) void k_ffn1(const char* __restrict__ yb, const char* __restrict__ w1, const f32x2* __restrict__ stat,
                                                 const float* __restrict__ utp, char* __restrict__ gb) {
  const lptr sm = (lptr)smem_raw;
  ST_INIT();
  const int b = blockIdx.x, t = threadIdx.x, l = t & 63, w = __builtin_amdgcn_readfirstlane(t >> 6);
  const int xcd = b & 7, j = b >> 3, tb = (xcd >> 2) * 8 + (j & 7), nt = (xcd & 3) * 4 + (j >> 3);
  if (w >= 4) { const int lw = w - 4;
    const char* ap = yb + (size_t)(tb * 4 + lw) * 64 * 1024; const char* wp = w1 + nt * 448;
    unsigned vb[7];
#pragma unroll
    for (int i = 0; i < 7; ++i) { const int c = (lw * 7 + i) * 64 + l, r = c / 28; vb[i] = r * (FF * 2) + (c - r * 28) * 16 + 3072 - i * 1024; }
    const unsigned va = l * 16;
    loader<16, 3, 11, K4_STAGE, 0>([&](int tt, int slot) {
      SETM0(slot + lw * 4096); const char* a = ap + (size_t)tt * 4096;
      glds<0>(va, a); glds<1024>(va, a); glds<2048>(va, a); glds<3072>(va, a);
      SETM0(slot + 16384 + lw * 7168 + 3072); const char* s = wp + (size_t)tt * (64 * FF * 2);
      glds<-3072>(vb[0], s); glds<-2048>(vb[1], s); glds<-1024>(vb[2], s); glds<0>(vb[3], s); glds<1024>(vb[4], s); glds<2048>(vb[5], s); glds<3072>(vb[6], s); }, [] {});
    return; }
  const int hi = l >> 5, tb32 = tb * 4 + w;
  constexpr float NL2E = -1.4426950408889634f;
  float rs, nm;
  { f32x2 st[16]; float up[8];
#pragma unroll
    for (int i = 0; i < 16; ++i) st[i] = stat[(tb32 * 32 + 2 * i + hi) * 32 + (l & 31)];
    const int col = nt * 224 + (t < 224 ? t : 0);
#pragma unroll
    for (int i = 0; i < 8; ++i) up[i] = utp[i * FF + col];
    float mean = 0.f;
#pragma unroll
    for (int i = 0; i < 16; ++i) mean += st[i][0];
    mean = swap32add(mean) * (1.f / 32.f); float m2 = 0.f;
#pragma unroll
    for (int i = 0; i < 16; ++i) m2 += st[i][1] + 32.f * (st[i][0] - mean) * (st[i][0] - mean);
    rs = NL2E * __builtin_amdgcn_rsqf(swap32add(m2) * (1.f / 1024.f) + 1e-5f); nm = -rs * mean;
    if (t < 224) { *(LDS float*)(sm + K4_UT + t * 4) = (up[0] + up[2]) + (up[4] + up[6]); *(LDS float*)(sm + K4_UT + 1024 + t * 4) = NL2E * ((up[1] + up[3]) + (up[5] + up[7])); } }
  ST(1);
  const lptr ra = sm + w * 4096 + l * 16;
  const lptr rb = sm + 16384 + (4 * hi + ((l >> 2) & 3)) * 448 + ((l >> 4) & 1) * 32 + (l & 3) * 8;
  f32x16 acc[7];
#pragma unroll
  for (int n = 0; n < 7; ++n) acc[n] = (f32x16){};
  FragB f, g; int slot = 0;
  auto kstep = [&](FragB& cur, FragB& nxt, int k) {
    nxt.a = ldsA(ra + slot + k * 1024);
#pragma unroll
    for (int n = 0; n < 7; ++n) { acc[n] = mfma32(cur.b[n], cur.a, acc[n]); nxt.b[n] = ldsT(rb + slot + k * 7168 + n * 64, 3584); SB(); } };
  bar(); ST(2);
  f.a = ldsA(ra);
#pragma unroll
  for (int n = 0; n < 7; ++n) f.b[n] = ldsT(rb + n * 64, 3584);
  for (int tt = 0; tt < 15; ++tt) { kstep(f, g, 1); kstep(g, f, 2); kstep(f, g, 3); bar(); slot = slot == 2 * K4_STAGE ? 0 : slot + K4_STAGE; kstep(g, f, 0); }
  kstep(f, g, 1); kstep(g, f, 2); kstep(f, g, 3);
#pragma unroll
  for (int n = 0; n < 7; ++n) acc[n] = mfma32(g.b[n], g.a, acc[n]);
  ST(3);
  char* d = gb + (size_t)(tb32 * 224 + nt * 14) * 1024 + l * 16;
#pragma unroll
  for (int n = 0; n < 7; ++n) { unsigned o[8];
#pragma unroll
    for (int q = 0; q < 4; ++q) { const f32x4 u = *(const LDS f32x4*)(sm + K4_UT + (n * 32 + 8 * q + 4 * hi) * 4), tv = *(const LDS f32x4*)(sm + K4_UT + 1024 + (n * 32 + 8 * q + 4 * hi) * 4);
#pragma unroll
      for (int i = 0; i < 2; ++i) { const f32x2 z = rs * (f32x2){acc[n][4 * q + 2 * i], acc[n][4 * q + 2 * i + 1]} + (nm * (f32x2){u[2 * i], u[2 * i + 1]} + (f32x2){tv[2 * i], tv[2 * i + 1]});
        const f32x2 dn = NL2E * (f32x2){EX2(z[0]), EX2(z[1])} + NL2E;
        const f32x2 r = z * (f32x2){__builtin_amdgcn_rcpf(dn[0]), __builtin_amdgcn_rcpf(dn[1])}; o[2 * q + i] = pk(r[0], r[1]); } }
    *(u32x4*)(d + n * 2048) = (u32x4){o[0], o[1], o[2], o[3]}; *(u32x4*)(d + n * 2048 + 1024) = (u32x4){o[4], o[5], o[6], o[7]}; }
  ST_END(4);
}

constexpr int K5_S = 6, K5_LDS = K5_S * GA_STAGE;
__global__ __launch_bounds__(512, 2) void k_ffn2(const char* __restrict__ gb, const char* __restrict__ w2, const char* __restrict__ x1b,
                                                 const float* __restrict__ modf, char* __restrict__ out) {
  const lptr sm = (lptr)smem_raw;
  ST_INIT();
  const int b = blockIdx.x, l = threadIdx.x & 63, w = __builtin_amdgcn_readfirstlane(threadIdx.x >> 6);
  const int xcd = b & 7, j = b >> 3, tb = (xcd >> 1) * 4 + (j & 3), nt = (xcd & 1) * 8 + (j >> 2);
  if (w >= 4) { gemmA_load<56, K5_S, DM, 0>(gb + (size_t)tb * 4 * 224 * 1024, w2 + nt * 128, l, w - 4, [] {}); return; }
  const int hi = l >> 5, tb32 = tb * 4 + 2 * (w & 1), n0 = nt * 64 + (w >> 1) * 32;
  u32x4 xr[2][2]; f32x4 g[4];
#pragma unroll
  for (int i = 0; i < 2; ++i)
#pragma unroll
    for (int h = 0; h < 2; ++h) xr[i][h] = *(const u32x4*)(x1b + (size_t)((tb32 + i) * 64 + (n0 >> 4) + h) * 1024 + l * 16);
#pragma unroll
  for (int q = 0; q < 4; ++q) g[q] = *(const f32x4*)(modf + 5 * DM + n0 + 8 * q + 4 * hi);
  f32x16 acc[2]; gemmA_compute<56, K5_S>(sm, l, w, acc[0], acc[1], S_);
#pragma unroll
  for (int i = 0; i < 2; ++i)
#pragma unroll
    for (int h = 0; h < 2; ++h) { u32x4 o;
#pragma unroll
      for (int e = 0; e < 4; ++e) { const int r = 8 * h + 2 * e, q = r >> 2, c = r & 3; o[e] = pk(blo(xr[i][h][e]) + g[q][c] * acc[i][r], bhi(xr[i][h][e]) + g[q][c + 1] * acc[i][r + 1]); }
      *(u32x4*)(out + (size_t)((tb32 + i) * 32 + (l & 31)) * 2048 + (n0 + 16 * h + 8 * hi) * 2) = rowswap(o); }
  ST_END(5);
}

extern "C" void kernel_launch(void* const* d_in, const int* in_sizes, int n_in, void* d_out, int out_size, void* d_ws, size_t ws_size, hipStream_t stream) {
  const char* x = (const char*)d_in[0]; const char* wq = (const char*)d_in[1]; const char* wk = (const char*)d_in[2]; const char* wv = (const char*)d_in[3];
  const char* wo = (const char*)d_in[4]; const char* w1 = (const char*)d_in[6]; const char* w2 = (const char*)d_in[7];
  const uint16_t* c = (const uint16_t*)d_in[8]; const char* wmod = (const char*)d_in[9];
  char* ws = (char*)d_ws;
  (void)hipFuncSetAttribute((const void*)k_ln1kv, hipFuncAttributeMaxDynamicSharedMemorySize, K1_LDS);
  (void)hipFuncSetAttribute((const void*)k_qattn, hipFuncAttributeMaxDynamicSharedMemorySize, K2_LDS);
  (void)hipFuncSetAttribute((const void*)k_oproj, hipFuncAttributeMaxDynamicSharedMemorySize, K3_LDS);
  (void)hipFuncSetAttribute((const void*)k_ffn1, hipFuncAttributeMaxDynamicSharedMemorySize, K4_LDS);
  (void)hipFuncSetAttribute((const void*)k_ffn2, hipFuncAttributeMaxDynamicSharedMemorySize, K5_LDS);
  float* modp = (float*)(ws + WS_MODP); float* modf = (float*)(ws + WS_MODF);
  k_mod<<<768, 256, 0, stream>>>(c, wmod, modp);
  k_ln1kv<<<256, 512, K1_LDS, stream>>>(x, wk, wv, (const char*)modp, modf, (uint16_t*)(ws + WS_MODH), ws + WS_H, ws + WS_KV);
  k_qattn<<<256, 512, K2_LDS, stream>>>(ws + WS_H, wq, ws + WS_KV, ws + WS_O, w1, ws + WS_MODH, (float*)(ws + WS_UTP), wo, w2);
  k_oproj<<<256, 512, K3_LDS, stream>>>(ws + WS_O, wo, x, modf, ws + WS_X1, ws + WS_Y, (f32x2*)(ws + WS_STAT));
  k_ffn1<<<256, 512, K4_LDS, stream>>>(ws + WS_Y, w1, (const f32x2*)(ws + WS_STAT), (const float*)(ws + WS_UTP), ws + WS_G);
  k_ffn2<<<256, 512, K5_LDS, stream>>>(ws + WS_G, w2, ws + WS_X1, modf, (char*)d_out);
}
```

```cpp
#include <hip/hip_runtime.h>
#include <hip/hip_bf16.h>
#include <stdint.h>

typedef __attribute__((ext_vector_type(2))) float f32x2;
typedef __attribute__((ext_vector_type(4))) float f32x4;
typedef __attribute__((ext_vector_type(16))) float f32x16;
typedef __attribute__((ext_vector_type(2))) __bf16 bf16x2;
typedef __attribute__((ext_vector_type(8))) __bf16 bf16x8;
typedef __attribute__((ext_vector_type(4))) short s16x4;
typedef __attribute__((ext_vector_type(2))) unsigned u32x2;
typedef __attribute__((ext_vector_type(4))) unsigned u32x4;
#define LDS __attribute__((address_space(3)))
typedef LDS char* lptr;
#define PREFETCH 1
#define DEV __device__ __forceinline__

constexpr int SEQ = 2048, DM = 1024, FF = 3584, NMOD = 6 * DM;
constexpr size_t WS_MODP = 0;
constexpr size_t WS_UTP = 98304;
constexpr size_t WS_MODF = 212992;
constexpr size_t WS_MODH = 237568;
constexpr size_t WS_STAT = 262144;
constexpr size_t WS_KV = 786432;
constexpr size_t WS_H = (size_t)1 << 20;
constexpr size_t WS_O = (size_t)5 << 20;
constexpr size_t WS_X1 = (size_t)9 << 20;
constexpr size_t WS_Y = (size_t)13 << 20;
constexpr size_t WS_G = (size_t)17 << 20;

DEV unsigned pk(float a, float b) { return __builtin_bit_cast(unsigned, __builtin_convertvector((f32x2){a, b}, bf16x2)); }
DEV float rbf(float a) { return __uint_as_float(pk(a, 0.f) << 16); }
DEV float blo(unsigned u) { return __uint_as_float(u << 16); }
DEV float bhi(unsigned u) { return __uint_as_float(u & 0xffff0000u); }
DEV float silu(float z) { return z * __builtin_amdgcn_rcpf(1.f + __builtin_amdgcn_exp2f(-1.4426950408889634f * z)); }
DEV f32x16 mfma32(bf16x8 a, bf16x8 b, f32x16 c) { return __builtin_amdgcn_mfma_f32_32x32x16_bf16(a, b, c, 0, 0, 0); }
DEV f32x4 mfma16(bf16x8 a, bf16x8 b, f32x4 c) { return __builtin_amdgcn_mfma_f32_16x16x32_bf16(a, b, c, 0, 0, 0); }
DEV bf16x8 asb(u32x4 v) { return __builtin_bit_cast(bf16x8, v); }
DEV bf16x8 ldsA(lptr p) { return *(const LDS bf16x8*)p; }
DEV bf16x8 ldsT(lptr p, int second) {
  s16x4 a = __builtin_amdgcn_ds_read_tr16_b64_v4i16((LDS s16x4*)p);
  s16x4 b = __builtin_amdgcn_ds_read_tr16_b64_v4i16((LDS s16x4*)(p + second));
  return __builtin_bit_cast(bf16x8, __builtin_shufflevector(a, b, 0, 1, 2, 3, 4, 5, 6, 7));
}
template <int C> DEV float dppadd(float v) { return v + __int_as_float(__builtin_amdgcn_update_dpp(0, __float_as_int(v), C, 0xf, 0xf, true)); }
DEV float swap32add(float v) { auto r = __builtin_amdgcn_permlane32_swap(__float_as_uint(v), __float_as_uint(v), false, false); return __uint_as_float(r[0]) + __uint_as_float(r[1]); }
DEV float swap16add(float v) { auto r = __builtin_amdgcn_permlane16_swap(__float_as_uint(v), __float_as_uint(v), false, false); return __uint_as_float(r[0]) + __uint_as_float(r[1]); }
DEV float swap32max(float v) { auto r = __builtin_amdgcn_permlane32_swap(__float_as_uint(v), __float_as_uint(v), false, false); return __builtin_fmaxf(__uint_as_float(r[0]), __uint_as_float(r[1])); }
DEV float wavesum(float v) { v = dppadd<0xB1>(v); v = dppadd<0x4E>(v); v = dppadd<0x141>(v); v = dppadd<0x140>(v); v = swap16add(v); return swap32add(v); }

#define SETM0(x) asm volatile("s_mov_b32 m0, %0\n\ts_nop 0" ::"s"(x) : "memory")
template <int OFF> DEV void glds(unsigned voff, const void* base) { asm volatile("global_load_lds_dwordx4 %0, %1 offset:%2" ::"v"(voff), "s"(base), "i"(OFF) : "memory"); }
template <int N> DEV void waitbar() { asm volatile("s_waitcnt vmcnt(%0) lgkmcnt(0)\n\ts_barrier" ::"i"(N) : "memory"); }
template <int OFF> DEV u32x4 gload(unsigned voff, const void* base) { u32x4 r; asm volatile("global_load_dwordx4 %0, %1, %2 offset:%3" : "=v"(r) : "v"(voff), "s"(base), "i"(OFF) : "memory"); return r; }
#define STPOL "sc1"
DEV void gstore(void* p, u32x4 v) { asm volatile("global_store_dwordx4 %0, %1, off " STPOL "\n\ts_nop 1" ::"v"(p), "v"(v) : "memory"); }
#define SB() __builtin_amdgcn_sched_barrier(0)
extern __shared__ __attribute__((aligned(16))) char smem_raw[];

struct Stamps {};
#define ST_INIT() Stamps S_
#define ST(i)
#define ST_END(k)

__global__ __launch_bounds__(256) void k_mod(const uint16_t* __restrict__ c, const char* __restrict__ wmod, float* __restrict__ modp) {
  __shared__ float red[4][32];
  ST_INIT();
  const int b = blockIdx.x, t = threadIdx.x, xcd = b & 7, j = b >> 3;
  const int cg = (j & 1) + 2 * (xcd + 8 * ((j >> 1) % 12)), ks = j / 24;
  const int row0 = ks * 256 + (t >> 2);
  const char* p = wmod + (size_t)row0 * (NMOD * 2) + cg * 64 + (t & 3) * 16;
  u32x4 w[4]; float s[4];
#pragma unroll
  for (int i = 0; i < 4; ++i) { w[i] = *(const u32x4*)(p + (size_t)i * 64 * NMOD * 2); s[i] = __uint_as_float((unsigned)c[row0 + 64 * i] << 16); }
  float a[8] = {0, 0, 0, 0, 0, 0, 0, 0};
#pragma unroll
  for (int i = 0; i < 4; ++i) { const float f = rbf(silu(s[i]));
#pragma unroll
    for (int e = 0; e < 4; ++e) { a[2 * e] += f * blo(w[i][e]); a[2 * e + 1] += f * bhi(w[i][e]); } }
#pragma unroll
  for (int e = 0; e < 8; ++e) { float v = a[e]; v += __shfl_xor(v, 4); v += __shfl_xor(v, 8); v = swap16add(v); a[e] = swap32add(v); }
  if ((t & 63) < 4) {
#pragma unroll
    for (int e = 0; e < 8; ++e) red[t >> 6][(t & 3) * 8 + e] = a[e]; }
  __syncthreads();
  if (t < 32) modp[ks * NMOD + cg * 32 + t] = (red[0][t] + red[1][t]) + (red[2][t] + red[3][t]);
  ST_END(0);
}

constexpr int K1_W = 0, K1_H = 65536, K1_MV = K1_H + 64 * 528, K1_RED = K1_MV + 8192, K1_LDS = K1_RED + 32768;
__global__ __launch_bounds__(512, 2) void k_ln1kv(const char* __restrict__ x, const char* __restrict__ wk, const char* __restrict__ wv,
                                                  const char* __restrict__ modp, float* __restrict__ modf, uint16_t* __restrict__ modh,
                                                  char* __restrict__ hbuf, char* __restrict__ kv) {
  const lptr sm = (lptr)smem_raw;
  ST_INIT();
  const int b = blockIdx.x, t = threadIdx.x, l = t & 63, w = __builtin_amdgcn_readfirstlane(t >> 6);
  const int which = (b >> 3) & 1, rb = (b & 7) * 16 + (b >> 4);
  u32x4 xv[4], mp[4];
  { const char* xb = x + (size_t)(rb * 16 + 2 * w) * 2048; const unsigned vo = l * 32;
    xv[0] = gload<0>(vo, xb); xv[1] = gload<16>(vo, xb); xv[2] = gload<2048>(vo, xb); xv[3] = gload<2064>(vo, xb);
#pragma unroll
    for (int s = 0; s < 4; ++s) mp[s] = gload<0>(t * 16, modp + s * NMOD * 4); }
  { const char* wsrc = (which ? wv : wk) + w * 8192;
#pragma unroll
    for (int g = 0; g < 2; ++g) { SETM0(K1_W + w * 8192 + g * 4096); const char* s = wsrc + g * 4096;
      glds<0>(l * 16, s); glds<1024>(l * 16, s); glds<2048>(l * 16, s); glds<3072>(l * 16, s); } }
  asm volatile("s_waitcnt vmcnt(8)" : "+v"(xv[0]), "+v"(xv[1]), "+v"(xv[2]), "+v"(xv[3]), "+v"(mp[0]), "+v"(mp[1]), "+v"(mp[2]), "+v"(mp[3])::"memory");
  ST(1);
  { f32x4 o;
#pragma unroll
    for (int e = 0; e < 4; ++e) { const float v = rbf((__uint_as_float(mp[0][e]) + __uint_as_float(mp[1][e])) + (__uint_as_float(mp[2][e]) + __uint_as_float(mp[3][e])));
      o[e] = t >= 256 ? rbf(1.f + v) : v; }
    *(LDS f32x4*)(sm + K1_MV + (t & 256) * 16 + (t & 3) * 1024 + ((t & 255) >> 2) * 16) = o; }
  f32x2 v[2][8]; float rs[2];
#pragma unroll
  for (int r = 0; r < 2; ++r) { f32x2 s = {0.f, 0.f};
#pragma unroll
    for (int e = 0; e < 8; ++e) { const unsigned u = xv[2 * r + (e >> 2)][e & 3]; v[r][e] = (f32x2){blo(u), bhi(u)}; s += v[r][e]; }
    const float m = wavesum(s[0] + s[1]) * (1.f / 1024.f); f32x2 q = {0.f, 0.f};
#pragma unroll
    for (int e = 0; e < 8; ++e) { v[r][e] -= m; q += v[r][e] * v[r][e]; }
    rs[r] = __builtin_amdgcn_rsqf(wavesum(q[0] + q[1]) * (1.f / 1024.f) + 1e-5f); }
  asm volatile("s_waitcnt lgkmcnt(0)\n\ts_barrier" ::: "memory");
  { unsigned u[2][8];
#pragma unroll
    for (int e = 0; e < 4; ++e) { const f32x4 a = *(const LDS f32x4*)(sm + K1_MV + e * 1024 + l * 16), cc = *(const LDS f32x4*)(sm + K1_MV + 4096 + e * 1024 + l * 16);
#pragma unroll
      for (int r = 0; r < 2; ++r)
#pragma unroll
        for (int i = 0; i < 2; ++i) { const f32x2 h = v[r][2 * e + i] * rs[r] * (f32x2){cc[2 * i], cc[2 * i + 1]} + (f32x2){a[2 * i], a[2 * i + 1]}; u[r][2 * e + i] = pk(h[0], h[1]); } }
#pragma unroll
    for (int r = 0; r < 2; ++r) { const lptr d = sm + K1_H + l * 528 + (2 * w + r) * 16;
      *(LDS u32x4*)d = (u32x4){u[r][0], u[r][1], u[r][4], u[r][5]}; *(LDS u32x4*)(d + 256) = (u32x4){u[r][2], u[r][3], u[r][6], u[r][7]}; } }
  ST(2); waitbar<0>(); ST(3);
  f32x16 acc = {};
  { const int hi = l >> 5; const lptr hp = sm + K1_H + w * 8 * 528 + hi * 256 + (l & 15) * 16;
    const lptr wp = sm + K1_W + (w * 128 + 4 * hi + ((l >> 2) & 3)) * 64 + ((l >> 4) & 1) * 32 + (l & 3) * 8;
    bf16x8 fa[8], fb[8];
#pragma unroll
    for (int i = 0; i < 8; ++i) { fa[i] = ldsA(hp + i * 528); fb[i] = ldsT(wp + i * 1024, 512); }
#pragma unroll
    for (int i = 0; i < 2; ++i) { const int id = t + 512 * i, tok = id & 15, h2 = (id >> 4) & 1, kb = which * 32 + (id >> 5);
      gstore(hbuf + (size_t)((rb >> 1) * 64 + kb) * 1024 + (h2 * 32 + (rb & 1) * 16 + tok) * 16, *(const LDS u32x4*)(sm + K1_H + kb * 528 + h2 * 256 + tok * 16)); }
    if (which == 0) {
#pragma unroll
      for (int i = 0; i < 8; ++i) acc = mfma32(fb[i], fa[i], acc);
    } else {
#pragma unroll
      for (int i = 0; i < 8; ++i) acc = mfma32(fa[i], fb[i], acc);
    } }
#pragma unroll
  for (int q = 0; q < 4; ++q) *(LDS f32x4*)(sm + K1_RED + w * 4096 + q * 1024 + l * 16) = (f32x4){acc[4 * q], acc[4 * q + 1], acc[4 * q + 2], acc[4 * q + 3]};
  asm volatile("s_waitcnt lgkmcnt(0)\n\ts_barrier" ::: "memory");
  if (w == 0) {
    const lptr rp = sm + K1_RED + (which ? l * 16 : (l >> 5) * 2048 + (((l >> 4) & 1) * 32 + (l & 15)) * 16);
    f32x4 a = {}, c = {};
#pragma unroll
    for (int u = 0; u < 8; ++u) { a += *(const LDS f32x4*)(rp + u * 4096); c += *(const LDS f32x4*)(rp + u * 4096 + 1024); }
    char* base = kv + (rb >> 1) * 4096;
    char* d = which ? base + 2048 + (rb & 1) * 1024 + l * 16 : base + (l >> 5) * 1024 + (((l >> 4) & 1) * 32 + (rb & 1) * 16 + (l & 15)) * 16;
    *(u32x4*)d = (u32x4){pk(a[0], a[1]), pk(a[2], a[3]), pk(c[0], c[1]), pk(c[2], c[3])};
  } else if (b < 64 && w == 1) {
    const int col = 2048 + b * 64 + l; const float* mpf = (const float*)modp;
    float v2 = rbf((mpf[col] + mpf[NMOD + col]) + (mpf[2 * NMOD + col] + mpf[3 * NMOD + col]));
    if ((col >> 10) == 4) v2 = rbf(1.f + v2);
    modf[col] = v2; modh[col] = (uint16_t)(__float_as_uint(v2) >> 16);
  }
  ST_END(1);
}

DEV void bar() { asm volatile("s_waitcnt lgkmcnt(0)\n\ts_barrier" ::: "memory"); }
template <int N, int G, int EXTRA> DEV void loader_tail() { waitbar<G * N + EXTRA>(); if constexpr (N > 0) loader_tail<N - 1, G, EXTRA>(); }
constexpr int issued(int t, int S) { return t == 0 ? 2 : (2 + 2 * t < S + t - 1 ? 2 + 2 * t : S + t - 1); }
template <int T, int S, int G, int STAGE, class I> DEV void loader_ramp(I&& issue) {
#pragma unroll
  for (int s = T == 0 ? 0 : issued(T - 1, S); s < issued(T, S); ++s) issue(s, (s % S) * STAGE);
  waitbar<G*(issued(T, S) - T - 1)>();
  if constexpr (issued(T, S) < S + T - 1 || T == 0) loader_ramp<T + 1, S, G, STAGE>(issue);
}
constexpr int ramp_end(int S) { int t = 1; while (issued(t, S) < S + t - 1) ++t; return t; }
template <int KT, int S, int G, int STAGE, int EXTRA, class I, class X>
DEV void loader(I&& issue, X&& extra) {
  loader_ramp<0, S, G, STAGE>(issue);
  constexpr int T0 = ramp_end(S);
  int slot = ((S + T0 - 1) % S) * STAGE;
  for (int t = T0; t <= KT - S; ++t) { issue(t - 1 + S, slot); slot = slot == (S - 1) * STAGE ? 0 : slot + STAGE; if (t < KT - S) waitbar<G*(S - 2)>(); }
  extra();
  loader_tail<S - 2, G, EXTRA>();
}
constexpr int GA_STAGE = 24576;
template <int KT, int S, int LDW, int EXTRA, class X>
DEV void gemmA_load(const char* act, const char* wgt, int l, int lw, X&& extra) {
  const char* ap = act + (size_t)lw * (KT * 4096);
  const char* wp = wgt + (size_t)lw * (16 * LDW * 2);
  const unsigned va = l * 16, vb = (l >> 3) * (LDW * 2) + (((l & 7) ^ (((l >> 4) & 1) << 2)) * 16);
  loader<KT, S, 6, GA_STAGE, EXTRA>([&](int t, int slot) {
    SETM0(slot + lw * 4096); const char* a = ap + (size_t)t * 4096; glds<0>(va, a); glds<1024>(va, a); glds<2048>(va, a); glds<3072>(va, a);
    SETM0(slot + 16384 + lw * 2048); const char* s = wp + (size_t)t * (64 * LDW * 2); glds<0>(vb, s); glds<1024>(vb, s + 8 * LDW * 2 - 1024); }, extra);
}
struct FragA { bf16x8 a0[4], a1[4], b[4]; };
template <int KT, int S>
DEV void gemmA_compute(const lptr sm, int l, int w, f32x16& c0, f32x16& c1, Stamps& S_) {
  const int q = (l >> 2) & 3, p = l & 3;
  const lptr ra = sm + (w & 1) * 8192 + l * 16;
  const lptr rb = sm + 16384 + (4 * (l >> 5) + q) * 128 + ((((w >> 1) * 4 + ((l >> 4) & 1) * 2 + (p >> 1)) ^ ((q >> 1) << 2)) * 16) + (p & 1) * 8;
  c0 = (f32x16){}; c1 = (f32x16){};
  FragA f, g; int slot = 0;
  auto rd = [&](FragA& d, int k) { d.a0[k] = ldsA(ra + slot + k * 1024); d.b[k] = ldsT(rb + slot + k * 2048, 1024); d.a1[k] = ldsA(ra + slot + 4096 + k * 1024); };
  auto step = [&](FragA& cur, FragA& nxt) { bar(); slot = slot == (S - 1) * GA_STAGE ? 0 : slot + GA_STAGE;
#pragma unroll
    for (int k = 0; k < 4; ++k) { if (k < 2) rd(nxt, 2 * k); c0 = mfma32(cur.b[k], cur.a0[k], c0); SB(); if (k < 2) rd(nxt, 2 * k + 1); c1 = mfma32(cur.b[k], cur.a1[k], c1); SB(); } };
  bar(); ST(2);
#pragma unroll
  for (int k = 0; k < 4; ++k) rd(f, k);
  for (int t = 0; t < KT - 2; t += 2) { step(f, g); step(g, f); }
  step(f, g);
#pragma unroll
  for (int k = 0; k < 4; ++k) { c0 = mfma32(g.b[k], g.a0[k], c0); c1 = mfma32(g.b[k], g.a1[k], c1); }
  ST(3);
}

constexpr int K2_KV0 = 98304, K2_KV1 = 131072, K2_KV2 = 0, K2_UW = 32768, K2_Q = 65536, K2_RED = 73728, K2_DUMP = 81920, K2_LDS = 163840;
DEV void pack16(const f32x16& s, u32x4& a, u32x4& b) {
  a = (u32x4){pk(s[0], s[1]), pk(s[2], s[3]), pk(s[4], s[5]), pk(s[6], s[7])};
  b = (u32x4){pk(s[8], s[9]), pk(s[10], s[11]), pk(s[12], s[13]), pk(s[14], s[15])}; }
DEV float max16(const f32x16& s) { float m = s[0];
#pragma unroll
  for (int i = 1; i < 16; ++i) m = __builtin_fmaxf(m, s[i]);
  return swap32max(m); }
#define EX2(v) __builtin_amdgcn_exp2f(v)
#define PIN(x) asm volatile("" : "+v"(x))

__global__ __launch_bounds__(512, 2) void k_qattn(const char* __restrict__ hbuf, const char* __restrict__ wq, const char* __restrict__ kv, char* __restrict__ obuf,
                                                  const char* __restrict__ w1, const char* __restrict__ modh, float* __restrict__ utp,
                                                  const char* __restrict__ wo, const char* __restrict__ w2) {
  const lptr sm = (lptr)smem_raw;
  ST_INIT();
  const int b = blockIdx.x, l = threadIdx.x & 63, w = __builtin_amdgcn_readfirstlane(threadIdx.x >> 6);
  const int xcd = b & 7, j = b >> 3, tb = (xcd >> 1) * 4 + (j & 3), hp = (xcd & 1) * 8 + (j >> 2);
  const unsigned l16 = l * 16;
  u32x4 q0u, q1u;
  if (w >= 4) {
    gemmA_load<16, 4, DM, 8>(hbuf + (size_t)tb * 4 * 64 * 1024, wq + hp * 128, l, w - 4, [&] {
#pragma unroll
      for (int g = 0; g < 2; ++g) { SETM0(K2_KV0 + (w - 4) * 8192 + g * 4096); const char* s = kv + (w - 4) * 8192 + g * 4096;
        glds<0>(l16, s); glds<1024>(l16, s); glds<2048>(l16, s); glds<3072>(l16, s); } });
    waitbar<0>();
    q0u = *(const LDS u32x4*)(sm + K2_Q + (w - 4) * 2048 + l16); q1u = *(const LDS u32x4*)(sm + K2_Q + (w - 4) * 2048 + 1024 + l16);
  } else {
    f32x16 c0, c1; gemmA_compute<16, 4>(sm, l, w, c0, c1, S_);
    constexpr float QS = 0.17677669529663687f * 1.4426950408889634f;
    c0 *= QS; c1 *= QS;
    u32x4 a, c; pack16(c1, a, c); *(LDS u32x4*)(sm + K2_Q + w * 2048 + l16) = a; *(LDS u32x4*)(sm + K2_Q + w * 2048 + 1024 + l16) = c;
    pack16(c0, q0u, q1u);
    bar();
  }
  const bf16x8 q0 = asb(q0u), q1 = asb(q1u);
  const char* kvw = kv + w * 4096;
  auto kvload = [&](int st, int slot) { SETM0(slot + w * 4096); const char* s = kvw + st * 32768; glds<0>(l16, s); glds<1024>(l16, s); glds<2048>(l16, s); glds<3072>(l16, s); };
  kvload(1, K2_KV1);
  const unsigned one2 = ((l & 7) == ((l >> 4) & 1) * 4) ? 0x3f803f80u : 0u;
  const bf16x8 ones = asb((u32x4){one2, one2, one2, one2});
  const lptr kp0 = sm + K2_KV0 + l16, kp1 = sm + K2_KV1 + l16, kp2 = sm + K2_KV2 + l16;
  bf16x8 k0 = ldsA(kp0), k1 = ldsA(kp0 + 1024), v0 = ldsA(kp0 + 2048), v1 = ldsA(kp0 + 3072);
  f32x16 negm, sA, sB, o = {}; f32x4 ls = {};
  { f32x16 z = {}; z = mfma32(k0, q0, z); z = mfma32(k1, q1, z); const float m = -max16(z);
#pragma unroll
    for (int i = 0; i < 16; ++i) negm[i] = m; }
  PIN(negm);
  sA = mfma32(k0, q0, negm); sA = mfma32(k1, q1, sA);
  k0 = ldsA(kp0 + 4096); k1 = ldsA(kp0 + 4096 + 1024);
  u32x4 p0, p1 = {0, 0, 0, 0}; bf16x8 v1p = v1;
#define TILE(S, N, KP, VP)                                                                                            \
  { N = mfma32(k0, q0, negm); SB();                                                                                  \
    S[0] = EX2(S[0]); S[1] = EX2(S[1]); S[2] = EX2(S[2]); S[3] = EX2(S[3]); p0[0] = pk(S[0], S[1]); p0[1] = pk(S[2], S[3]); PIN(p0); SB(); \
    o = mfma32(v1p, asb(p1), o); k0 = ldsA(KP); SB();                                                                \
    S[4] = EX2(S[4]); S[5] = EX2(S[5]); S[6] = EX2(S[6]); S[7] = EX2(S[7]); p0[2] = pk(S[4], S[5]); p0[3] = pk(S[6], S[7]); PIN(p0); SB(); \
    ls = mfma16(ones, asb(p1), ls); v1p = v1; v1 = ldsA((VP) + 1024); SB();                                          \
    S[8] = EX2(S[8]); S[9] = EX2(S[9]); PIN(S); SB();                                                                \
    N = mfma32(k1, q1, N); SB();                                                                                     \
    S[10] = EX2(S[10]); S[11] = EX2(S[11]); S[12] = EX2(S[12]); S[13] = EX2(S[13]); p1[0] = pk(S[8], S[9]); p1[1] = pk(S[10], S[11]); PIN(p1); PIN(S); SB(); \
    o = mfma32(v0, asb(p0), o); k1 = ldsA((KP) + 1024); SB();                                                        \
    S[14] = EX2(S[14]); S[15] = EX2(S[15]); p1[2] = pk(S[12], S[13]); p1[3] = pk(S[14], S[15]); PIN(p1); SB();       \
    ls = mfma16(ones, asb(p0), ls); v0 = ldsA(VP); SB(); }
#define STAGE(P, Q, ...)                                                                                              \
  TILE(sA, sB, P + 2 * 4096, P + 1 * 4096 + 2048) TILE(sB, sA, P + 3 * 4096, P + 2 * 4096 + 2048)                     \
  TILE(sA, sB, P + 4 * 4096, P + 3 * 4096 + 2048) TILE(sB, sA, P + 5 * 4096, P + 4 * 4096 + 2048)                     \
  TILE(sA, sB, P + 6 * 4096, P + 5 * 4096 + 2048) TILE(sB, sA, P + 7 * 4096, P + 6 * 4096 + 2048)                     \
  { __VA_ARGS__ }                                                                                                     \
  TILE(sA, sB, Q, P + 7 * 4096 + 2048) TILE(sB, sA, Q + 4096, Q + 2048)
  const bool ut = b < 224; const int cg = b % 56, ks = b / 56;
  u32x2 vf[4] = {{0, 0}, {0, 0}, {0, 0}, {0, 0}};
  ST(4);
  STAGE(kp0, kp1, waitbar<0>(); kvload(2, K2_KV2);
        if (ut) { SETM0(K2_UW + w * 4096); const char* s = w1 + (size_t)(ks * 256 + w * 32) * (FF * 2) + cg * 128;
          const unsigned vo = (l >> 3) * (FF * 2) + (((l & 7) ^ (((l >> 4) & 1) << 2)) * 16);
          glds<0>(vo, s); glds<1024>(vo, s + 8 * FF * 2 - 1024); glds<2048>(vo, s + 16 * FF * 2 - 2048); glds<3072>(vo, s + 24 * FF * 2 - 3072);
          if ((l & 31) < 2) { const char* mv = modh + ((4 - (l & 31)) * DM + ks * 256 + w * 32 + 4 * (l >> 5)) * 2;
            _Pragma("unroll") for (int i = 0; i < 4; ++i) vf[i] = *(const u32x2*)(mv + i * 16); } })
  STAGE(kp1, kp2, waitbar<8>(); kvload(3, K2_KV0);)
  STAGE(kp2, kp0, waitbar<0>();
        if (ut) { const int q = (l >> 2) & 3; const int p = l & 3; f32x16 d0 = {}; f32x16 d1 = {};
          const lptr rb = sm + K2_UW + (w * 32 + 4 * (l >> 5) + q) * 128 + (((((l >> 4) & 1) * 2 + (p >> 1)) ^ ((q >> 1) << 2)) * 16) + (p & 1) * 8;
          const lptr rc = sm + K2_UW + (w * 32 + 4 * (l >> 5) + q) * 128 + (((4 + ((l >> 4) & 1) * 2 + (p >> 1)) ^ ((q >> 1) << 2)) * 16) + (p & 1) * 8;
          _Pragma("unroll") for (int kk = 0; kk < 2; ++kk) { const bf16x8 vv = asb((u32x4){vf[2 * kk][0], vf[2 * kk][1], vf[2 * kk + 1][0], vf[2 * kk + 1][1]});
            d0 = mfma32(ldsT(rb + kk * 2048, 1024), vv, d0); d1 = mfma32(ldsT(rc + kk * 2048, 1024), vv, d1); }
          if ((l & 31) < 2) { const lptr d = sm + K2_RED + w * 512 + (l & 31) * 256 + (l >> 5) * 16;
            _Pragma("unroll") for (int i = 0; i < 4; ++i) { *(LDS f32x4*)(d + i * 32) = (f32x4){d0[4 * i], d0[4 * i + 1], d0[4 * i + 2], d0[4 * i + 3]};
              *(LDS f32x4*)(d + 128 + i * 32) = (f32x4){d1[4 * i], d1[4 * i + 1], d1[4 * i + 2], d1[4 * i + 3]}; } } }
        kvload(4, K2_KV1);
        if (PREFETCH) { SETM0(K2_DUMP + w * 1024); glds<0>(l16, wo + b * 8192 + w * 1024); glds<0>(l16, w2 + (size_t)b * 28672 + w * 1024); })
  STAGE(kp0, kp1, waitbar<2>();
        if (ut && w < 2) { float v = 0.f;
          _Pragma("unroll") for (int u = 0; u < 8; ++u) v += *(const LDS float*)(sm + K2_RED + u * 512 + w * 256 + l * 4);
          utp[(ks * 2 + w) * FF + cg * 64 + l] = v; }
        kvload(5, K2_KV2);
        if (PREFETCH) { SETM0(K2_DUMP + w * 1024); glds<0>(l16, w2 + (size_t)b * 28672 + 8192 + w * 1024); glds<0>(l16, w2 + (size_t)b * 28672 + 16384 + w * 1024); })
  STAGE(kp1, kp2, waitbar<2>(); kvload(6, K2_KV0);
        if (PREFETCH) { SETM0(K2_DUMP + w * 1024); glds<0>(l16, w2 + (size_t)b * 28672 + 24576 + (w & 3) * 1024); })
  STAGE(kp2, kp0, waitbar<1>(); kvload(7, K2_KV1);)
  STAGE(kp0, kp1, waitbar<0>();)
  STAGE(kp1, kp2, )
  ST(5);
  o = mfma32(v1p, asb(p1), o); ls = mfma16(ones, asb(p1), ls);
  float lsum = ls[0];
  { float chk = lsum;
#pragma unroll
    for (int i = 0; i < 16; ++i) chk = __builtin_fmaxf(chk, __builtin_fabsf(o[i]) * 0x1p-40f);
    bool bad = !(chk < 0x1p80f);
#pragma unroll
    for (int i = 0; i < 16; ++i) bad |= (o[i] != o[i]);
    bad |= (lsum != lsum);
    if (__any(bad)) {
      float m = -INFINITY; lsum = 0.f; o = (f32x16){};
      for (int t = 0; t < 64; ++t) { const bf16x8* g = (const bf16x8*)(kv + t * 4096) + l;
        f32x16 s = {}; s = mfma32(g[0], q0, s); s = mfma32(g[64], q1, s);
        const float mn = __builtin_fmaxf(m, max16(s)), al = EX2(m - mn); m = mn; float rsum = 0.f;
#pragma unroll
        for (int i = 0; i < 16; ++i) { s[i] = rbf(EX2(s[i] - mn)); rsum += s[i]; o[i] *= al; }
        lsum = lsum * al + swap32add(rsum);
        u32x4 a, c; pack16(s, a, c); o = mfma32(g[128], asb(a), o); o = mfma32(g[192], asb(c), o); } } }
  o *= 1.f / lsum;
  u32x4 o0, o1; pack16(o, o0, o1);
  char* d = obuf + (size_t)((tb * 4 + 2 * (w & 1) + (w >> 2)) * 64 + (hp * 2 + ((w >> 1) & 1)) * 2) * 1024 + l16;
  gstore(d, o0); gstore(d + 1024, o1);
  ST_END(2);
}

DEV u32x4 rowswap(u32x4 v) { auto a = __builtin_amdgcn_permlane32_swap(v[0], v[2], false, false); auto c = __builtin_amdgcn_permlane32_swap(v[1], v[3], false, false);
  return (u32x4){a[0], c[0], a[1], c[1]}; }

constexpr int K3_S = 6, K3_LDS = K3_S * GA_STAGE;
__global__ __launch_bounds__(512, 2) void k_oproj(const char* __restrict__ obuf, const char* __restrict__ wo, const char* __restrict__ x, const float* __restrict__ modf,
                                                  char* __restrict__ x1b, char* __restrict__ yb, f32x2* __restrict__ stat) {
  const lptr sm = (lptr)smem_raw;
  ST_INIT();
  const int b = blockIdx.x, l = threadIdx.x & 63, w = __builtin_amdgcn_readfirstlane(threadIdx.x >> 6);
  const int xcd = b & 7, j = b >> 3, tb = (xcd >> 1) * 4 + (j & 3), nt = (xcd & 1) * 8 + (j >> 2);
  if (w >= 4) { gemmA_load<16, K3_S, DM, 0>(obuf + (size_t)tb * 4 * 64 * 1024, wo + nt * 128, l, w - 4, [] {}); return; }
  const int hi = l >> 5, tb32 = tb * 4 + 2 * (w & 1), n0 = nt * 64 + (w >> 1) * 32;
  u32x4 xr[2][2]; f32x4 ga[4], cb[4];
#pragma unroll
  for (int i = 0; i < 2; ++i)
#pragma unroll
    for (int h = 0; h < 2; ++h) xr[i][h] = *(const u32x4*)(x + (size_t)((tb32 + i) * 32 + (l & 31)) * 2048 + (n0 + 16 * h + 8 * hi) * 2);
#pragma unroll
  for (int q = 0; q < 4; ++q) { ga[q] = *(const f32x4*)(modf + 2 * DM + n0 + 8 * q + 4 * hi); cb[q] = *(const f32x4*)(modf + 4 * DM + n0 + 8 * q + 4 * hi); }
  f32x16 acc[2]; gemmA_compute<16, K3_S>(sm, l, w, acc[0], acc[1], S_);
#pragma unroll
  for (int i = 0; i < 2; ++i) { unsigned xo[8], yo[8]; float x1[16], s = 0.f;
#pragma unroll
    for (int h = 0; h < 2; ++h) { const u32x4 xs = rowswap(xr[i][h]);
#pragma unroll
      for (int e = 0; e < 4; ++e) { const int r = 8 * h + 2 * e, q = r >> 2, c = r & 3;
        const unsigned u = pk(blo(xs[e]) + ga[q][c] * acc[i][r], bhi(xs[e]) + ga[q][c + 1] * acc[i][r + 1]);
        x1[r] = blo(u); x1[r + 1] = bhi(u); xo[4 * h + e] = u; yo[4 * h + e] = pk(x1[r] * cb[q][c], x1[r + 1] * cb[q][c + 1]); s += x1[r] + x1[r + 1]; } }
    const size_t off = (size_t)((tb32 + i) * 64 + (n0 >> 4)) * 1024 + l * 16;
    gstore(yb + off, (u32x4){yo[0], yo[1], yo[2], yo[3]}); gstore(yb + off + 1024, (u32x4){yo[4], yo[5], yo[6], yo[7]});
    gstore(x1b + off, (u32x4){xo[0], xo[1], xo[2], xo[3]}); gstore(x1b + off + 1024, (u32x4){xo[4], xo[5], xo[6], xo[7]});
    const float mean = swap32add(s) * (1.f / 32.f); float m2 = 0.f;
#pragma unroll
    for (int e = 0; e < 16; ++e) m2 += (x1[e] - mean) * (x1[e] - mean);
    m2 = swap32add(m2);
    if (hi == 0) stat[((tb32 + i) * 32 + (n0 >> 5)) * 32 + l] = (f32x2){mean, m2}; }
  ST_END(3);
}

constexpr int K4_STAGE = 45056, K4_UT = 3 * K4_STAGE, K4_LDS = K4_UT + 2048;
struct FragB { bf16x8 a, b[7]; };
__global__ __launch_bounds__(512, 2) void k_ffn1(const char* __restrict__ yb, const char* __restrict__ w1, const f32x2* __restrict__ stat,
                                                 const float* __restrict__ utp, char* __restrict__ gb) {
  const lptr sm = (lptr)smem_raw;
  ST_INIT();
  const int b = blockIdx.x, t = threadIdx.x, l = t & 63, w = __builtin_amdgcn_readfirstlane(t >> 6);
  const int xcd = b & 7, j = b >> 3, tb = (xcd >> 2) * 8 + (j & 7), nt = (xcd & 3) * 4 + (j >> 3);
  if (w >= 4) { const int lw = w - 4;
    const char* ap = yb + (size_t)(tb * 4 + lw) * 64 * 1024; const char* wp = w1 + nt * 448;
    unsigned vb[7];
#pragma unroll
    for (int i = 0; i < 7; ++i) { const int c = (lw * 7 + i) * 64 + l, r = c / 28; vb[i] = r * (FF * 2) + (c - r * 28) * 16 + 3072 - i * 1024; }
    const unsigned va = l * 16;
    loader<16, 3, 11, K4_STAGE, 0>([&](int tt, int slot) {
      SETM0(slot + lw * 4096); const char* a = ap + (size_t)tt * 4096;
      glds<0>(va, a); glds<1024>(va, a); glds<2048>(va, a); glds<3072>(va, a);
      SETM0(slot + 16384 + lw * 7168 + 3072); const char* s = wp + (size_t)tt * (64 * FF * 2);
      glds<-3072>(vb[0], s); glds<-2048>(vb[1], s); glds<-1024>(vb[2], s); glds<0>(vb[3], s); glds<1024>(vb[4], s); glds<2048>(vb[5], s); glds<3072>(vb[6], s); }, [] {});
    return; }
  const int hi = l >> 5, tb32 = tb * 4 + w;
  constexpr float NL2E = -1.4426950408889634f;
  float rs, nm;
  { f32x2 st[16]; float up[8];
#pragma unroll
    for (int i = 0; i < 16; ++i) st[i] = stat[(tb32 * 32 + 2 * i + hi) * 32 + (l & 31)];
    const int col = nt * 224 + (t < 224 ? t : 0);
#pragma unroll
    for (int i = 0; i < 8; ++i) up[i] = utp[i * FF + col];
    float mean = 0.f;
#pragma unroll
    for (int i = 0; i < 16; ++i) mean += st[i][0];
    mean = swap32add(mean) * (1.f / 32.f); float m2 = 0.f;
#pragma unroll
    for (int i = 0; i < 16; ++i) m2 += st[i][1] + 32.f * (st[i][0] - mean) * (st[i][0] - mean);
    rs = NL2E * __builtin_amdgcn_rsqf(swap32add(m2) * (1.f / 1024.f) + 1e-5f); nm = -rs * mean;
    if (t < 224) { *(LDS float*)(sm + K4_UT + t * 4) = (up[0] + up[2]) + (up[4] + up[6]); *(LDS float*)(sm + K4_UT + 1024 + t * 4) = NL2E * ((up[1] + up[3]) + (up[5] + up[7])); } }
  ST(1);
  const lptr ra = sm + w * 4096 + l * 16;
  const lptr rb = sm + 16384 + (4 * hi + ((l >> 2) & 3)) * 448 + ((l >> 4) & 1) * 32 + (l & 3) * 8;
  f32x16 acc[7];
#pragma unroll
  for (int n = 0; n < 7; ++n) acc[n] = (f32x16){};
  FragB f, g; int slot = 0;
  auto kstep = [&](FragB& cur, FragB& nxt, int k) {
    nxt.a = ldsA(ra + slot + k * 1024);
#pragma unroll
    for (int n = 0; n < 7; ++n) { acc[n] = mfma32(cur.b[n], cur.a, acc[n]); nxt.b[n] = ldsT(rb + slot + k * 7168 + n * 64, 3584); SB(); } };
  bar(); ST(2);
  f.a = ldsA(ra);
#pragma unroll
  for (int n = 0; n < 7; ++n) f.b[n] = ldsT(rb + n * 64, 3584);
  for (int tt = 0; tt < 15; ++tt) { kstep(f, g, 1); kstep(g, f, 2); kstep(f, g, 3); bar(); slot = slot == 2 * K4_STAGE ? 0 : slot + K4_STAGE; kstep(g, f, 0); }
  kstep(f, g, 1); kstep(g, f, 2); kstep(f, g, 3);
#pragma unroll
  for (int n = 0; n < 7; ++n) acc[n] = mfma32(g.b[n], g.a, acc[n]);
  ST(3);
  char* d = gb + (size_t)(tb32 * 224 + nt * 14) * 1024 + l * 16;
#pragma unroll
  for (int n = 0; n < 7; ++n) { unsigned o[8];
#pragma unroll
    for (int q = 0; q < 4; ++q) { const f32x4 u = *(const LDS f32x4*)(sm + K4_UT + (n * 32 + 8 * q + 4 * hi) * 4), tv = *(const LDS f32x4*)(sm + K4_UT + 1024 + (n * 32 + 8 * q + 4 * hi) * 4);
#pragma unroll
      for (int i = 0; i < 2; ++i) { const f32x2 z = rs * (f32x2){acc[n][4 * q + 2 * i], acc[n][4 * q + 2 * i + 1]} + (nm * (f32x2){u[2 * i], u[2 * i + 1]} + (f32x2){tv[2 * i], tv[2 * i + 1]});
        const f32x2 dn = NL2E * (f32x2){EX2(z[0]), EX2(z[1])} + NL2E;
        const f32x2 r = z * (f32x2){__builtin_amdgcn_rcpf(dn[0]), __builtin_amdgcn_rcpf(dn[1])}; o[2 * q + i] = pk(r[0], r[1]); } }
    gstore(d + n * 2048, (u32x4){o[0], o[1], o[2], o[3]}); gstore(d + n * 2048 + 1024, (u32x4){o[4], o[5], o[6], o[7]}); }
  ST_END(4);
}

constexpr int K5_S = 6, K5_LDS = K5_S * GA_STAGE;
__global__ __launch_bounds__(512, 2) void k_ffn2(const char* __restrict__ gb, const char* __restrict__ w2, const char* __restrict__ x1b,
                                                 const float* __restrict__ modf, char* __restrict__ out) {
  const lptr sm = (lptr)smem_raw;
  ST_INIT();
  const int b = blockIdx.x, l = threadIdx.x & 63, w = __builtin_amdgcn_readfirstlane(threadIdx.x >> 6);
  const int xcd = b & 7, j = b >> 3, tb = (xcd >> 1) * 4 + (j & 3), nt = (xcd & 1) * 8 + (j >> 2);
  if (w >= 4) { gemmA_load<56, K5_S, DM, 0>(gb + (size_t)tb * 4 * 224 * 1024, w2 + nt * 128, l, w - 4, [] {}); return; }
  const int hi = l >> 5, tb32 = tb * 4 + 2 * (w & 1), n0 = nt * 64 + (w >> 1) * 32;
  u32x4 xr[2][2]; f32x4 g[4];
#pragma unroll
  for (int i = 0; i < 2; ++i)
#pragma unroll
    for (int h = 0; h < 2; ++h) xr[i][h] = *(const u32x4*)(x1b + (size_t)((tb32 + i) * 64 + (n0 >> 4) + h) * 1024 + l * 16);
#pragma unroll
  for (int q = 0; q < 4; ++q) g[q] = *(const f32x4*)(modf + 5 * DM + n0 + 8 * q + 4 * hi);
  f32x16 acc[2]; gemmA_compute<56, K5_S>(sm, l, w, acc[0], acc[1], S_);
#pragma unroll
  for (int i = 0; i < 2; ++i)
#pragma unroll
    for (int h = 0; h < 2; ++h) { u32x4 o;
#pragma unroll
      for (int e = 0; e < 4; ++e) { const int r = 8 * h + 2 * e, q = r >> 2, c = r & 3; o[e] = pk(blo(xr[i][h][e]) + g[q][c] * acc[i][r], bhi(xr[i][h][e]) + g[q][c + 1] * acc[i][r + 1]); }
      gstore(out + (size_t)((tb32 + i) * 32 + (l & 31)) * 2048 + (n0 + 16 * h + 8 * hi) * 2, rowswap(o)); }
  ST_END(5);
}

extern "C" void kernel_launch(void* const* d_in, const int* in_sizes, int n_in, void* d_out, int out_size, void* d_ws, size_t ws_size, hipStream_t stream) {
  const char* x = (const char*)d_in[0]; const char* wq = (const char*)d_in[1]; const char* wk = (const char*)d_in[2]; const char* wv = (const char*)d_in[3];
  const char* wo = (const char*)d_in[4]; const char* w1 = (const char*)d_in[6]; const char* w2 = (const char*)d_in[7];
  const uint16_t* c = (const uint16_t*)d_in[8]; const char* wmod = (const char*)d_in[9];
  char* ws = (char*)d_ws;
  (void)hipFuncSetAttribute((const void*)k_ln1kv, hipFuncAttributeMaxDynamicSharedMemorySize, K1_LDS);
  (void)hipFuncSetAttribute((const void*)k_qattn, hipFuncAttributeMaxDynamicSharedMemorySize, K2_LDS);
  (void)hipFuncSetAttribute((const void*)k_oproj, hipFuncAttributeMaxDynamicSharedMemorySize, K3_LDS);
  (void)hipFuncSetAttribute((const void*)k_ffn1, hipFuncAttributeMaxDynamicSharedMemorySize, K4_LDS);
  (void)hipFuncSetAttribute((const void*)k_ffn2, hipFuncAttributeMaxDynamicSharedMemorySize, K5_LDS);
  float* modp = (float*)(ws + WS_MODP); float* modf = (float*)(ws + WS_MODF);
  k_mod<<<768, 256, 0, stream>>>(c, wmod, modp);
  k_ln1kv<<<256, 512, K1_LDS, stream>>>(x, wk, wv, (const char*)modp, modf, (uint16_t*)(ws + WS_MODH), ws + WS_H, ws + WS_KV);
  k_qattn<<<256, 512, K2_LDS, stream>>>(ws + WS_H, wq, ws + WS_KV, ws + WS_O, w1, ws + WS_MODH, (float*)(ws + WS_UTP), wo, w2);
  k_oproj<<<256, 512, K3_LDS, stream>>>(ws + WS_O, wo, x, modf, ws + WS_X1, ws + WS_Y, (f32x2*)(ws + WS_STAT));
  k_ffn1<<<256, 512, K4_LDS, stream>>>(ws + WS_Y, w1, (const f32x2*)(ws + WS_STAT), (const float*)(ws + WS_UTP), ws + WS_G);
  k_ffn2<<<256, 512, K5_LDS, stream>>>(ws + WS_G, w2, ws + WS_X1, modf, (char*)d_out);
}
```

```cpp
#include <hip/hip_runtime.h>
#include <hip/hip_bf16.h>
#include <stdint.h>

typedef __attribute__((ext_vector_type(2))) float f32x2;
typedef __attribute__((ext_vector_type(4))) float f32x4;
typedef __attribute__((ext_vector_type(16))) float f32x16;
typedef __attribute__((ext_vector_type(2))) __bf16 bf16x2;
typedef __attribute__((ext_vector_type(8))) __bf16 bf16x8;
typedef __attribute__((ext_vector_type(4))) short s16x4;
typedef __attribute__((ext_vector_type(2))) unsigned u32x2;
typedef __attribute__((ext_vector_type(4))) unsigned u32x4;
#define LDS __attribute__((address_space(3)))
typedef LDS char* lptr;
#define PREFETCH 1
#define TLBTOUCH 1
#define WQL2 1
#define DEV __device__ __forceinline__

constexpr int SEQ = 2048, DM = 1024, FF = 3584, NMOD = 6 * DM;
constexpr size_t WS_MODP = 0;
constexpr size_t WS_UTP = 98304;
constexpr size_t WS_MODF = 212992;
constexpr size_t WS_MODH = 237568;
constexpr size_t WS_STAT = 262144;
constexpr size_t WS_KV = 786432;
constexpr size_t WS_H = (size_t)1 << 20;
constexpr size_t WS_O = (size_t)5 << 20;
constexpr size_t WS_X1 = (size_t)9 << 20;
constexpr size_t WS_Y = (size_t)13 << 20;
constexpr size_t WS_G = (size_t)17 << 20;

DEV unsigned pk(float a, float b) { return __builtin_bit_cast(unsigned, __builtin_convertvector((f32x2){a, b}, bf16x2)); }
DEV float rbf(float a) { return __uint_as_float(pk(a, 0.f) << 16); }
DEV float blo(unsigned u) { return __uint_as_float(u << 16); }
DEV float bhi(unsigned u) { return __uint_as_float(u & 0xffff0000u); }
DEV float silu(float z) { return z * __builtin_amdgcn_rcpf(1.f + __builtin_amdgcn_exp2f(-1.4426950408889634f * z)); }
DEV f32x16 mfma32(bf16x8 a, bf16x8 b, f32x16 c) { return __builtin_amdgcn_mfma_f32_32x32x16_bf16(a, b, c, 0, 0, 0); }
DEV f32x4 mfma16(bf16x8 a, bf16x8 b, f32x4 c) { return __builtin_amdgcn_mfma_f32_16x16x32_bf16(a, b, c, 0, 0, 0); }
DEV bf16x8 asb(u32x4 v) { return __builtin_bit_cast(bf16x8, v); }
DEV bf16x8 ldsA(lptr p) { return *(const LDS bf16x8*)p; }
DEV bf16x8 ldsT(lptr p, int second) {
  s16x4 a = __builtin_amdgcn_ds_read_tr16_b64_v4i16((LDS s16x4*)p);
  s16x4 b = __builtin_amdgcn_ds_read_tr16_b64_v4i16((LDS s16x4*)(p + second));
  return __builtin_bit_cast(bf16x8, __builtin_shufflevector(a, b, 0, 1, 2, 3, 4, 5, 6, 7));
}
template <int C> DEV float dppadd(float v) { return v + __int_as_float(__builtin_amdgcn_update_dpp(0, __float_as_int(v), C, 0xf, 0xf, true)); }
DEV float swap32add(float v) { auto r = __builtin_amdgcn_permlane32_swap(__float_as_uint(v), __float_as_uint(v), false, false); return __uint_as_float(r[0]) + __uint_as_float(r[1]); }
DEV float swap16add(float v) { auto r = __builtin_amdgcn_permlane16_swap(__float_as_uint(v), __float_as_uint(v), false, false); return __uint_as_float(r[0]) + __uint_as_float(r[1]); }
DEV float swap32max(float v) { auto r = __builtin_amdgcn_permlane32_swap(__float_as_uint(v), __float_as_uint(v), false, false); return __builtin_fmaxf(__uint_as_float(r[0]), __uint_as_float(r[1])); }
DEV float wavesum(float v) { v = dppadd<0xB1>(v); v = dppadd<0x4E>(v); v = dppadd<0x141>(v); v = dppadd<0x140>(v); v = swap16add(v); return swap32add(v); }

#define SETM0(x) asm volatile("s_mov_b32 m0, %0\n\ts_nop 0" ::"s"(x) : "memory")
template <int OFF> DEV void glds(unsigned voff, const void* base) { asm volatile("global_load_lds_dwordx4 %0, %1 offset:%2" ::"v"(voff), "s"(base), "i"(OFF) : "memory"); }
template <int N> DEV void waitbar() { asm volatile("s_waitcnt vmcnt(%0) lgkmcnt(0)\n\ts_barrier" ::"i"(N) : "memory"); }
template <int OFF> DEV u32x4 gload(unsigned voff, const void* base) { u32x4 r; asm volatile("global_load_dwordx4 %0, %1, %2 offset:%3" : "=v"(r) : "v"(voff), "s"(base), "i"(OFF) : "memory"); return r; }
#define STPOL "sc1"
DEV void gstore(void* p, u32x4 v) { asm volatile("global_store_dwordx4 %0, %1, off " STPOL "\n\ts_nop 1" ::"v"(p), "v"(v) : "memory"); }
#define SB() __builtin_amdgcn_sched_barrier(0)
extern __shared__ __attribute__((aligned(16))) char smem_raw[];

struct Stamps {};
#define ST_INIT() Stamps S_
#define ST(i)
#define ST_END(k)

template <int OFF> DEV unsigned gload1(unsigned voff, const void* base) { unsigned r; asm volatile("global_load_dword %0, %1, %2 offset:%3" : "=v"(r) : "v"(voff), "s"(base), "i"(OFF) : "memory"); return r; }
template <int OFF> DEV unsigned gloadh(unsigned voff, const void* base) { unsigned r; asm volatile("global_load_ushort %0, %1, %2 offset:%3" : "=v"(r) : "v"(voff), "s"(base), "i"(OFF) : "memory"); return r; }
template <int R, bool PAIR> DEV f32x2 mod_wave(const u32x4 (&w)[R], const unsigned (&c)[R]) {
  float a[8] = {0, 0, 0, 0, 0, 0, 0, 0};
#pragma unroll
  for (int i = 0; i < R; ++i) { const float f = rbf(silu(blo(c[i])));
#pragma unroll
    for (int e = 0; e < 4; ++e) { a[2 * e] += f * blo(w[i][e]); a[2 * e + 1] += f * bhi(w[i][e]); } }
#pragma unroll
  for (int e = 0; e < 4; ++e) { auto r = __builtin_amdgcn_permlane32_swap(__float_as_uint(a[e]), __float_as_uint(a[e + 4]), false, false); a[e] = __uint_as_float(r[0]) + __uint_as_float(r[1]); }
#pragma unroll
  for (int e = 0; e < 2; ++e) { auto r = __builtin_amdgcn_permlane16_swap(__float_as_uint(a[e]), __float_as_uint(a[e + 2]), false, false); float v = __uint_as_float(r[0]) + __uint_as_float(r[1]);
    v = dppadd<0x128>(v); v = dppadd<0x124>(v); v = dppadd<0x122>(v); if (!PAIR) v = dppadd<0x121>(v); a[e] = v; }
  return (f32x2){a[0], a[1]};
}
DEV void mod_put(float v, int col, float* modf, uint16_t* modh) { v = rbf(v); if (((col >> 10) % 3) == 1) v = rbf(1.f + v); modf[col] = v; modh[col] = (uint16_t)(__float_as_uint(v) >> 16); }
struct ModJob { u32x4 w[2]; unsigned c[2]; };
DEV void modjob_load(ModJob& m, const char* c, const char* wmod, int col0, int t) {
  m.c[0] = gloadh<0>(t * 2, c); m.c[1] = gloadh<1024>(t * 2, c);
  m.w[0] = gload<0>(t * (NMOD * 2), wmod + col0 * 2); m.w[1] = gload<0>(t * (NMOD * 2), wmod + col0 * 2 + 512 * NMOD * 2); }
DEV void modjob_sum(ModJob& m, lptr red, int l, int w) {
  asm volatile("" : "+v"(m.c[0]), "+v"(m.c[1]), "+v"(m.w[0]), "+v"(m.w[1]));
  const f32x2 v = mod_wave<2, false>(m.w, m.c); if ((l & 15) == 0) *(LDS f32x2*)(red + w * 32 + (l >> 4) * 8) = v; }
DEV void modjob_put(lptr red, int col0, int l, float* modf, uint16_t* modh) {
  if (l < 8) { float v = 0.f;
#pragma unroll
    for (int u = 0; u < 8; ++u) v += *(const LDS float*)(red + u * 32 + l * 4);
    mod_put(v, col0 + l, modf, modh); } }

__global__ __launch_bounds__(512) void k_mod(const char* __restrict__ c, const char* __restrict__ wmod, float* __restrict__ modf, uint16_t* __restrict__ modh,
                                             const char* __restrict__ x, const char* __restrict__ wq, const char* __restrict__ wk, const char* __restrict__ wv,
                                             const char* __restrict__ ws, const char* __restrict__ out) {
  __shared__ float red[8][16];
  ST_INIT();
  const int b = blockIdx.x, t = threadIdx.x, l = t & 63;
  if (b >= 128) { const int p = b - 128; u32x4 d[7];
    const char* xb = x + (size_t)((p & 7) * 16 + (p >> 3)) * 32768;
#pragma unroll
    for (int i = 0; i < 4; ++i) d[i] = gload<0>(t * 16, xb + i * 8192);
    d[4] = gload<0>(t * 16, wq + p * 16384); d[5] = gload<0>(t * 16, wq + p * 16384 + 8192);
    d[6] = gload<0>((t & 63) * 16, (p & 64 ? wv : wk) + (p & 63) * 1024);
    unsigned e = 0;
    unsigned e2 = 0;
    if (p < 8 && t < 64) { e = gload1<0>((t & 31) << 20, ws); e2 = gload1<0>((t & 3) << 20, out); }
    asm volatile("s_waitcnt vmcnt(0)" : "+v"(e2)::"memory");
    asm volatile("s_waitcnt vmcnt(0)" : "+v"(d[0]), "+v"(d[1]), "+v"(d[2]), "+v"(d[3]), "+v"(d[4]), "+v"(d[5]), "+v"(d[6]), "+v"(e)::"memory");
    return; }
  const int cg = (b & 7) * 16 + (b >> 3);
  u32x4 w[4]; unsigned cv[4];
#pragma unroll
  for (int i = 0; i < 4; ++i) w[i] = gload<0>((t >> 1) * (NMOD * 2) + (t & 1) * 16, wmod + cg * 32 + (size_t)i * 256 * NMOD * 2);
  cv[0] = gloadh<0>(t & ~1, c); cv[1] = gloadh<512>(t & ~1, c); cv[2] = gloadh<1024>(t & ~1, c); cv[3] = gloadh<1536>(t & ~1, c);
  asm volatile("s_waitcnt vmcnt(0)" : "+v"(w[0]), "+v"(w[1]), "+v"(w[2]), "+v"(w[3]), "+v"(cv[0]), "+v"(cv[1]), "+v"(cv[2]), "+v"(cv[3])::"memory");
  const f32x2 v = mod_wave<4, true>(w, cv);
  if ((l & 14) == 0) *(f32x2*)&red[t >> 6][(l & 1) * 8 + (l >> 4) * 2] = v;
  __syncthreads();
  if (t < 16) { float a = 0.f;
#pragma unroll
    for (int u = 0; u < 8; ++u) a += red[u][t];
    mod_put(a, cg * 16 + t, modf, modh); }
  ST_END(0);
}

constexpr int K1_W = 0, K1_H = 65536, K1_MV = K1_H + 64 * 528, K1_RED = K1_MV + 8192, K1_MR = K1_RED + 32768, K1_LDS = K1_MR + 256;
__global__ __launch_bounds__(512, 2) void k_ln1kv(const char* __restrict__ x, const char* __restrict__ wk, const char* __restrict__ wv,
                                                  const char* __restrict__ cvec, const char* __restrict__ wmod, float* modf, uint16_t* __restrict__ modh,
                                                  char* __restrict__ hbuf, char* __restrict__ kv, const char* __restrict__ wq) {
  const lptr sm = (lptr)smem_raw;
  ST_INIT();
  const int b = blockIdx.x, t = threadIdx.x, l = t & 63, w = __builtin_amdgcn_readfirstlane(t >> 6);
  const int which = (b >> 3) & 1, rb = (b & 7) * 16 + (b >> 4), mcol = 3 * DM + ((b & 7) * 32 + (b >> 3)) * 8;
  u32x4 xv[4]; ModJob mj;
  { const char* xb = x + (size_t)(rb * 16 + 2 * w) * 2048; const unsigned vo = l * 32;
    xv[0] = gload<0>(vo, xb); xv[1] = gload<16>(vo, xb); xv[2] = gload<2048>(vo, xb); xv[3] = gload<2064>(vo, xb);
    SETM0(K1_MV + w * 1024); glds<0>(l * 64, (const char*)modf + (w >> 2) * 4096 + (w & 3) * 16); }
  asm volatile("s_barrier" ::: "memory");
  { const char* wsrc = (which ? wv : wk) + w * 8192;
#pragma unroll
    for (int g = 0; g < 2; ++g) { SETM0(K1_W + w * 8192 + g * 4096); const char* s = wsrc + g * 4096;
      glds<0>(l * 16, s); glds<1024>(l * 16, s); glds<2048>(l * 16, s); glds<3072>(l * 16, s); } }
  modjob_load(mj, cvec, wmod, mcol, t);
  asm volatile("s_waitcnt vmcnt(12)" : "+v"(xv[0]), "+v"(xv[1]), "+v"(xv[2]), "+v"(xv[3])::"memory");
  ST(1);
  f32x2 v[2][8]; float rs[2];
#pragma unroll
  for (int r = 0; r < 2; ++r) { f32x2 s = {0.f, 0.f};
#pragma unroll
    for (int e = 0; e < 8; ++e) { const unsigned u = xv[2 * r + (e >> 2)][e & 3]; v[r][e] = (f32x2){blo(u), bhi(u)}; s += v[r][e]; }
    const float m = wavesum(s[0] + s[1]) * (1.f / 1024.f); f32x2 q = {0.f, 0.f};
#pragma unroll
    for (int e = 0; e < 8; ++e) { v[r][e] -= m; q += v[r][e] * v[r][e]; }
    rs[r] = __builtin_amdgcn_rsqf(wavesum(q[0] + q[1]) * (1.f / 1024.f) + 1e-5f); }
  asm volatile("s_barrier" ::: "memory");
  { unsigned u[2][8];
#pragma unroll
    for (int e = 0; e < 4; ++e) { const f32x4 a = *(const LDS f32x4*)(sm + K1_MV + e * 1024 + l * 16), cc = *(const LDS f32x4*)(sm + K1_MV + 4096 + e * 1024 + l * 16);
#pragma unroll
      for (int r = 0; r < 2; ++r)
#pragma unroll
        for (int i = 0; i < 2; ++i) { const f32x2 h = v[r][2 * e + i] * rs[r] * (f32x2){cc[2 * i], cc[2 * i + 1]} + (f32x2){a[2 * i], a[2 * i + 1]}; u[r][2 * e + i] = pk(h[0], h[1]); } }
#pragma unroll
    for (int r = 0; r < 2; ++r) { const lptr d = sm + K1_H + l * 528 + (2 * w + r) * 16;
      *(LDS u32x4*)d = (u32x4){u[r][0], u[r][1], u[r][4], u[r][5]}; *(LDS u32x4*)(d + 256) = (u32x4){u[r][2], u[r][3], u[r][6], u[r][7]}; } }
  ST(2); waitbar<4>(); ST(3);
  f32x16 acc = {};
  { const int hi = l >> 5; const lptr hp = sm + K1_H + w * 8 * 528 + hi * 256 + (l & 15) * 16;
    const lptr wp = sm + K1_W + (w * 128 + 4 * hi + ((l >> 2) & 3)) * 64 + ((l >> 4) & 1) * 32 + (l & 3) * 8;
    bf16x8 fa[8], fb[8];
#pragma unroll
    for (int i = 0; i < 8; ++i) { fa[i] = ldsA(hp + i * 528); fb[i] = ldsT(wp + i * 1024, 512); }
#pragma unroll
    for (int i = 0; i < 2; ++i) { const int id = t + 512 * i, tok = id & 15, h2 = (id >> 4) & 1, kb = which * 32 + (id >> 5);
      gstore(hbuf + (size_t)((rb >> 1) * 64 + kb) * 1024 + (h2 * 32 + (rb & 1) * 16 + tok) * 16, *(const LDS u32x4*)(sm + K1_H + kb * 528 + h2 * 256 + tok * 16)); }
    if (which == 0) {
#pragma unroll
      for (int i = 0; i < 8; ++i) acc = mfma32(fb[i], fa[i], acc);
    } else {
#pragma unroll
      for (int i = 0; i < 8; ++i) acc = mfma32(fa[i], fb[i], acc);
    } }
  u32x4 dq[4];
  { const char* s = wq + (size_t)(b >> 3) * 65536 + (b & 1) * 1024; const unsigned vo = (t >> 6) * 2048 + l * 16;
#pragma unroll
    for (int i = 0; i < 4; ++i) dq[i] = gload<0>(vo, s + i * 16384); }
#pragma unroll
  for (int q = 0; q < 4; ++q) *(LDS f32x4*)(sm + K1_RED + w * 4096 + q * 1024 + l * 16) = (f32x4){acc[4 * q], acc[4 * q + 1], acc[4 * q + 2], acc[4 * q + 3]};
  asm volatile("s_waitcnt vmcnt(%0)" ::"i"(WQL2 ? 6 : 2) : "memory");
  modjob_sum(mj, sm + K1_MR, l, w);
  asm volatile("s_waitcnt lgkmcnt(0)\n\ts_barrier" ::: "memory");
  if (w == 0) {
    const lptr rp = sm + K1_RED + (which ? l * 16 : (l >> 5) * 2048 + (((l >> 4) & 1) * 32 + (l & 15)) * 16);
    f32x4 a = {}, c = {};
#pragma unroll
    for (int u = 0; u < 8; ++u) { a += *(const LDS f32x4*)(rp + u * 4096); c += *(const LDS f32x4*)(rp + u * 4096 + 1024); }
    char* base = kv + (rb >> 1) * 4096;
    char* d = which ? base + 2048 + (rb & 1) * 1024 + l * 16 : base + (l >> 5) * 1024 + (((l >> 4) & 1) * 32 + (rb & 1) * 16 + (l & 15)) * 16;
    *(u32x4*)d = (u32x4){pk(a[0], a[1]), pk(a[2], a[3]), pk(c[0], c[1]), pk(c[2], c[3])};
  } else if (w == 1) modjob_put(sm + K1_MR, mcol, l, modf, modh);
  asm volatile("s_waitcnt vmcnt(0)" : "+v"(dq[0]), "+v"(dq[1]), "+v"(dq[2]), "+v"(dq[3])::"memory");
  ST_END(1);
}

DEV void bar() { asm volatile("s_waitcnt lgkmcnt(0)\n\ts_barrier" ::: "memory"); }
template <int N, int G, int EXTRA> DEV void loader_tail() { waitbar<G * N + EXTRA>(); if constexpr (N > 0) loader_tail<N - 1, G, EXTRA>(); }
constexpr int issued(int t, int S) { return t == 0 ? 2 : (2 + 2 * t < S + t - 1 ? 2 + 2 * t : S + t - 1); }
template <int T, int S, int G, int STAGE, class I> DEV void loader_ramp(I&& issue) {
#pragma unroll
  for (int s = T == 0 ? 0 : issued(T - 1, S); s < issued(T, S); ++s) issue(s, (s % S) * STAGE);
  waitbar<G*(issued(T, S) - T - 1)>();
  if constexpr (issued(T, S) < S + T - 1 || T == 0) loader_ramp<T + 1, S, G, STAGE>(issue);
}
constexpr int ramp_end(int S) { int t = 1; while (issued(t, S) < S + t - 1) ++t; return t; }
template <int KT, int S, int G, int STAGE, int EXTRA, class I, class X>
DEV void loader(I&& issue, X&& extra) {
  loader_ramp<0, S, G, STAGE>(issue);
  constexpr int T0 = ramp_end(S);
  int slot = ((S + T0 - 1) % S) * STAGE;
  for (int t = T0; t <= KT - S; ++t) { issue(t - 1 + S, slot); slot = slot == (S - 1) * STAGE ? 0 : slot + STAGE; if (t < KT - S) waitbar<G*(S - 2)>(); }
  extra();
  loader_tail<S - 2, G, EXTRA>();
}
constexpr int GA_STAGE = 24576;
template <int KT, int S, int LDW, int EXTRA, class X>
DEV void gemmA_load(const char* act, const char* wgt, int l, int lw, X&& extra) {
  const char* ap = act + (size_t)lw * (KT * 4096);
  const char* wp = wgt + (size_t)lw * (16 * LDW * 2);
  const unsigned va = l * 16, vb = (l >> 3) * (LDW * 2) + (((l & 7) ^ (((l >> 4) & 1) << 2)) * 16);
  loader<KT, S, 6, GA_STAGE, EXTRA>([&](int t, int slot) {
    SETM0(slot + lw * 4096); const char* a = ap + (size_t)t * 4096; glds<0>(va, a); glds<1024>(va, a); glds<2048>(va, a); glds<3072>(va, a);
    SETM0(slot + 16384 + lw * 2048); const char* s = wp + (size_t)t * (64 * LDW * 2); glds<0>(vb, s); glds<1024>(vb, s + 8 * LDW * 2 - 1024); }, extra);
}
struct FragA { bf16x8 a0[4], a1[4], b[4]; };
template <int KT, int S>
DEV void gemmA_compute(const lptr sm, int l, int w, f32x16& c0, f32x16& c1, Stamps& S_) {
  const int q = (l >> 2) & 3, p = l & 3;
  const lptr ra = sm + (w & 1) * 8192 + l * 16;
  const lptr rb = sm + 16384 + (4 * (l >> 5) + q) * 128 + ((((w >> 1) * 4 + ((l >> 4) & 1) * 2 + (p >> 1)) ^ ((q >> 1) << 2)) * 16) + (p & 1) * 8;
  c0 = (f32x16){}; c1 = (f32x16){};
  FragA f, g; int slot = 0;
  auto rd = [&](FragA& d, int k) { d.a0[k] = ldsA(ra + slot + k * 1024); d.b[k] = ldsT(rb + slot + k * 2048, 1024); d.a1[k] = ldsA(ra + slot + 4096 + k * 1024); };
  auto step = [&](FragA& cur, FragA& nxt) { bar(); slot = slot == (S - 1) * GA_STAGE ? 0 : slot + GA_STAGE;
#pragma unroll
    for (int k = 0; k < 4; ++k) { if (k < 2) rd(nxt, 2 * k); c0 = mfma32(cur.b[k], cur.a0[k], c0); SB(); if (k < 2) rd(nxt, 2 * k + 1); c1 = mfma32(cur.b[k], cur.a1[k], c1); SB(); } };
  bar(); ST(2);
#pragma unroll
  for (int k = 0; k < 4; ++k) rd(f, k);
  for (int t = 0; t < KT - 2; t += 2) { step(f, g); step(g, f); }
  step(f, g);
#pragma unroll
  for (int k = 0; k < 4; ++k) { c0 = mfma32(g.b[k], g.a0[k], c0); c1 = mfma32(g.b[k], g.a1[k], c1); }
  ST(3);
}

constexpr int K2_KV0 = 98304, K2_KV1 = 131072, K2_KV2 = 0, K2_UW = 32768, K2_Q = 65536, K2_RED = 73728, K2_DUMP = 81920, K2_LDS = 163840;
DEV void pack16(const f32x16& s, u32x4& a, u32x4& b) {
  a = (u32x4){pk(s[0], s[1]), pk(s[2], s[3]), pk(s[4], s[5]), pk(s[6], s[7])};
  b = (u32x4){pk(s[8], s[9]), pk(s[10], s[11]), pk(s[12], s[13]), pk(s[14], s[15])}; }
DEV float max16(const f32x16& s) { float m = s[0];
#pragma unroll
  for (int i = 1; i < 16; ++i) m = __builtin_fmaxf(m, s[i]);
  return swap32max(m); }
#define EX2(v) __builtin_amdgcn_exp2f(v)
#define PIN(x) asm volatile("" : "+v"(x))

__global__ __launch_bounds__(512, 2) void k_qattn(const char* __restrict__ hbuf, const char* __restrict__ wq, const char* __restrict__ kv, char* __restrict__ obuf,
                                                  const char* __restrict__ w1, char* modh, float* __restrict__ utp,
                                                  const char* __restrict__ wo, const char* __restrict__ w2,
                                                  const char* __restrict__ cvec, const char* __restrict__ wmod, float* __restrict__ modf) {
  const lptr sm = (lptr)smem_raw;
  ST_INIT();
  const int b = blockIdx.x, l = threadIdx.x & 63, w = __builtin_amdgcn_readfirstlane(threadIdx.x >> 6);
  const int xcd = b & 7, j = b >> 3, tb = (xcd >> 1) * 4 + (j & 3), hp = (xcd & 1) * 8 + (j >> 2);
  const unsigned l16 = l * 16;
  u32x4 q0u, q1u;
  if (w >= 4) {
    gemmA_load<16, 4, DM, 8>(hbuf + (size_t)tb * 4 * 64 * 1024, wq + hp * 128, l, w - 4, [&] {
#pragma unroll
      for (int g = 0; g < 2; ++g) { SETM0(K2_KV0 + (w - 4) * 8192 + g * 4096); const char* s = kv + (w - 4) * 8192 + g * 4096;
        glds<0>(l16, s); glds<1024>(l16, s); glds<2048>(l16, s); glds<3072>(l16, s); } });
    waitbar<0>();
    q0u = *(const LDS u32x4*)(sm + K2_Q + (w - 4) * 2048 + l16); q1u = *(const LDS u32x4*)(sm + K2_Q + (w - 4) * 2048 + 1024 + l16);
  } else {
    f32x16 c0, c1; gemmA_compute<16, 4>(sm, l, w, c0, c1, S_);
    constexpr float QS = 0.17677669529663687f * 1.4426950408889634f;
    c0 *= QS; c1 *= QS;
    u32x4 a, c; pack16(c1, a, c); *(LDS u32x4*)(sm + K2_Q + w * 2048 + l16) = a; *(LDS u32x4*)(sm + K2_Q + w * 2048 + 1024 + l16) = c;
    pack16(c0, q0u, q1u);
    bar();
  }
  const bf16x8 q0 = asb(q0u), q1 = asb(q1u);
  const char* kvw = kv + w * 4096;
  auto kvload = [&](int st, int slot) { SETM0(slot + w * 4096); const char* s = kvw + st * 32768; glds<0>(l16, s); glds<1024>(l16, s); glds<2048>(l16, s); glds<3072>(l16, s); };
  kvload(1, K2_KV1);
  const unsigned one2 = ((l & 7) == ((l >> 4) & 1) * 4) ? 0x3f803f80u : 0u;
  const bf16x8 ones = asb((u32x4){one2, one2, one2, one2});
  const lptr kp0 = sm + K2_KV0 + l16, kp1 = sm + K2_KV1 + l16, kp2 = sm + K2_KV2 + l16;
  bf16x8 k0 = ldsA(kp0), k1 = ldsA(kp0 + 1024), v0 = ldsA(kp0 + 2048), v1 = ldsA(kp0 + 3072);
  f32x16 negm, sA, sB, o = {}; f32x4 ls = {};
  { f32x16 z = {}; z = mfma32(k0, q0, z); z = mfma32(k1, q1, z); const float m = -max16(z);
#pragma unroll
    for (int i = 0; i < 16; ++i) negm[i] = m; }
  PIN(negm);
  sA = mfma32(k0, q0, negm); sA = mfma32(k1, q1, sA);
  k0 = ldsA(kp0 + 4096); k1 = ldsA(kp0 + 4096 + 1024);
  u32x4 p0, p1 = {0, 0, 0, 0}; bf16x8 v1p = v1;
#define TILE(S, N, KP, VP)                                                                                            \
  { N = mfma32(k0, q0, negm); SB();                                                                                  \
    S[0] = EX2(S[0]); S[1] = EX2(S[1]); S[2] = EX2(S[2]); S[3] = EX2(S[3]); p0[0] = pk(S[0], S[1]); p0[1] = pk(S[2], S[3]); PIN(p0); SB(); \
    o = mfma32(v1p, asb(p1), o); k0 = ldsA(KP); SB();                                                                \
    S[4] = EX2(S[4]); S[5] = EX2(S[5]); S[6] = EX2(S[6]); S[7] = EX2(S[7]); p0[2] = pk(S[4], S[5]); p0[3] = pk(S[6], S[7]); PIN(p0); SB(); \
    ls = mfma16(ones, asb(p1), ls); v1p = v1; v1 = ldsA((VP) + 1024); SB();                                          \
    S[8] = EX2(S[8]); S[9] = EX2(S[9]); PIN(S); SB();                                                                \
    N = mfma32(k1, q1, N); SB();                                                                                     \
    S[10] = EX2(S[10]); S[11] = EX2(S[11]); S[12] = EX2(S[12]); S[13] = EX2(S[13]); p1[0] = pk(S[8], S[9]); p1[1] = pk(S[10], S[11]); PIN(p1); PIN(S); SB(); \
    o = mfma32(v0, asb(p0), o); k1 = ldsA((KP) + 1024); SB();                                                        \
    S[14] = EX2(S[14]); S[15] = EX2(S[15]); p1[2] = pk(S[12], S[13]); p1[3] = pk(S[14], S[15]); PIN(p1); SB();       \
    ls = mfma16(ones, asb(p0), ls); v0 = ldsA(VP); SB(); }
#define STAGE(P, Q, ...)                                                                                              \
  TILE(sA, sB, P + 2 * 4096, P + 1 * 4096 + 2048) TILE(sB, sA, P + 3 * 4096, P + 2 * 4096 + 2048)                     \
  TILE(sA, sB, P + 4 * 4096, P + 3 * 4096 + 2048) TILE(sB, sA, P + 5 * 4096, P + 4 * 4096 + 2048)                     \
  TILE(sA, sB, P + 6 * 4096, P + 5 * 4096 + 2048) TILE(sB, sA, P + 7 * 4096, P + 6 * 4096 + 2048)                     \
  { __VA_ARGS__ }                                                                                                     \
  TILE(sA, sB, Q, P + 7 * 4096 + 2048) TILE(sB, sA, Q + 4096, Q + 2048)
  const bool ut = b < 224; const int cg = b % 56, ks = b / 56;
  u32x2 vf[4] = {{0, 0}, {0, 0}, {0, 0}, {0, 0}};
  ModJob mj; const int mcol = (xcd < 4 ? 2 * DM : 4 * DM) + (xcd * 32 + j) * 8;
  ST(4);
  STAGE(kp0, kp1, waitbar<0>(); kvload(2, K2_KV2);
        if (ut) { SETM0(K2_UW + w * 4096); const char* s = w1 + (size_t)(ks * 256 + w * 32) * (FF * 2) + cg * 128;
          const unsigned vo = (l >> 3) * (FF * 2) + (((l & 7) ^ (((l >> 4) & 1) << 2)) * 16);
          glds<0>(vo, s); glds<1024>(vo, s + 8 * FF * 2 - 1024); glds<2048>(vo, s + 16 * FF * 2 - 2048); glds<3072>(vo, s + 24 * FF * 2 - 3072);
          if ((l & 31) < 2) { const char* mv = modh + ((4 - (l & 31)) * DM + ks * 256 + w * 32 + 4 * (l >> 5)) * 2;
            _Pragma("unroll") for (int i = 0; i < 4; ++i) vf[i] = *(const u32x2*)(mv + i * 16); } })
  STAGE(kp1, kp2, waitbar<8>(); kvload(3, K2_KV0);)
  STAGE(kp2, kp0, waitbar<0>();
        if (ut) { const int q = (l >> 2) & 3; const int p = l & 3; f32x16 d0 = {}; f32x16 d1 = {};
          const lptr rb = sm + K2_UW + (w * 32 + 4 * (l >> 5) + q) * 128 + (((((l >> 4) & 1) * 2 + (p >> 1)) ^ ((q >> 1) << 2)) * 16) + (p & 1) * 8;
          const lptr rc = sm + K2_UW + (w * 32 + 4 * (l >> 5) + q) * 128 + (((4 + ((l >> 4) & 1) * 2 + (p >> 1)) ^ ((q >> 1) << 2)) * 16) + (p & 1) * 8;
          _Pragma("unroll") for (int kk = 0; kk < 2; ++kk) { const bf16x8 vv = asb((u32x4){vf[2 * kk][0], vf[2 * kk][1], vf[2 * kk + 1][0], vf[2 * kk + 1][1]});
            d0 = mfma32(ldsT(rb + kk * 2048, 1024), vv, d0); d1 = mfma32(ldsT(rc + kk * 2048, 1024), vv, d1); }
          if ((l & 31) < 2) { const lptr d = sm + K2_RED + w * 512 + (l & 31) * 256 + (l >> 5) * 16;
            _Pragma("unroll") for (int i = 0; i < 4; ++i) { *(LDS f32x4*)(d + i * 32) = (f32x4){d0[4 * i], d0[4 * i + 1], d0[4 * i + 2], d0[4 * i + 3]};
              *(LDS f32x4*)(d + 128 + i * 32) = (f32x4){d1[4 * i], d1[4 * i + 1], d1[4 * i + 2], d1[4 * i + 3]}; } } }
        kvload(4, K2_KV1);
        if (PREFETCH) { SETM0(K2_DUMP + w * 1024); glds<0>(l16, wo + b * 8192 + w * 1024); glds<0>(l16, w2 + (size_t)b * 28672 + w * 1024); })
  STAGE(kp0, kp1, waitbar<2>();
        if (ut && w < 2) { float v = 0.f;
          _Pragma("unroll") for (int u = 0; u < 8; ++u) v += *(const LDS float*)(sm + K2_RED + u * 512 + w * 256 + l * 4);
          utp[(ks * 2 + w) * FF + cg * 64 + l] = v; }
        modjob_load(mj, cvec, wmod, mcol, threadIdx.x);
        kvload(5, K2_KV2);
        if (PREFETCH) { SETM0(K2_DUMP + w * 1024); glds<0>(l16, w2 + (size_t)b * 28672 + 8192 + w * 1024); glds<0>(l16, w2 + (size_t)b * 28672 + 16384 + w * 1024); })
  STAGE(kp1, kp2, waitbar<2>(); kvload(6, K2_KV0); modjob_sum(mj, sm + K2_RED + 4096, l, w);
        if (PREFETCH) { SETM0(K2_DUMP + w * 1024); glds<0>(l16, w2 + (size_t)b * 28672 + 24576 + (w & 3) * 1024); })
  STAGE(kp2, kp0, waitbar<1>(); kvload(7, K2_KV1); if (w == 0) modjob_put(sm + K2_RED + 4096, mcol, l, modf, (uint16_t*)modh);)
  STAGE(kp0, kp1, waitbar<0>();)
  STAGE(kp1, kp2, )
  ST(5);
  o = mfma32(v1p, asb(p1), o); ls = mfma16(ones, asb(p1), ls);
  float lsum = ls[0];
  { float chk = lsum;
#pragma unroll
    for (int i = 0; i < 16; ++i) chk = __builtin_fmaxf(chk, __builtin_fabsf(o[i]) * 0x1p-40f);
    bool bad = !(chk < 0x1p80f);
#pragma unroll
    for (int i = 0; i < 16; ++i) bad |= (o[i] != o[i]);
    bad |= (lsum != lsum);
    if (__any(bad)) {
      float m = -INFINITY; lsum = 0.f; o = (f32x16){};
      for (int t = 0; t < 64; ++t) { const bf16x8* g = (const bf16x8*)(kv + t * 4096) + l;
        f32x16 s = {}; s = mfma32(g[0], q0, s); s = mfma32(g[64], q1, s);
        const float mn = __builtin_fmaxf(m, max16(s)), al = EX2(m - mn); m = mn; float rsum = 0.f;
#pragma unroll
        for (int i = 0; i < 16; ++i) { s[i] = rbf(EX2(s[i] - mn)); rsum += s[i]; o[i] *= al; }
        lsum = lsum * al + swap32add(rsum);
        u32x4 a, c; pack16(s, a, c); o = mfma32(g[128], asb(a), o); o = mfma32(g[192], asb(c), o); } } }
  o *= 1.f / lsum;
  u32x4 o0, o1; pack16(o, o0, o1);
  char* d = obuf + (size_t)((tb * 4 + 2 * (w & 1) + (w >> 2)) * 64 + (hp * 2 + ((w >> 1) & 1)) * 2) * 1024 + l16;
  gstore(d, o0); gstore(d + 1024, o1);
  ST_END(2);
}

DEV u32x4 rowswap(u32x4 v) { auto a = __builtin_amdgcn_permlane32_swap(v[0], v[2], false, false); auto c = __builtin_amdgcn_permlane32_swap(v[1], v[3], false, false);
  return (u32x4){a[0], c[0], a[1], c[1]}; }

constexpr int K3_S = 6, K3_LDS = K3_S * GA_STAGE;
__global__ __launch_bounds__(512, 2) void k_oproj(const char* __restrict__ obuf, const char* __restrict__ wo, const char* __restrict__ x, const float* __restrict__ modf,
                                                  char* __restrict__ x1b, char* __restrict__ yb, f32x2* __restrict__ stat) {
  const lptr sm = (lptr)smem_raw;
  ST_INIT();
  const int b = blockIdx.x, l = threadIdx.x & 63, w = __builtin_amdgcn_readfirstlane(threadIdx.x >> 6);
  const int xcd = b & 7, j = b >> 3, tb = (xcd >> 1) * 4 + (j & 3), nt = (xcd & 1) * 8 + (j >> 2);
  if (w >= 4) { gemmA_load<16, K3_S, DM, 0>(obuf + (size_t)tb * 4 * 64 * 1024, wo + nt * 128, l, w - 4, [] {}); return; }
  const int hi = l >> 5, tb32 = tb * 4 + 2 * (w & 1), n0 = nt * 64 + (w >> 1) * 32;
  u32x4 xr[2][2]; f32x4 ga[4], cb[4];
#pragma unroll
  for (int i = 0; i < 2; ++i)
#pragma unroll
    for (int h = 0; h < 2; ++h) xr[i][h] = *(const u32x4*)(x + (size_t)((tb32 + i) * 32 + (l & 31)) * 2048 + (n0 + 16 * h + 8 * hi) * 2);
#pragma unroll
  for (int q = 0; q < 4; ++q) { ga[q] = *(const f32x4*)(modf + 2 * DM + n0 + 8 * q + 4 * hi); cb[q] = *(const f32x4*)(modf + 4 * DM + n0 + 8 * q + 4 * hi); }
  f32x16 acc[2]; gemmA_compute<16, K3_S>(sm, l, w, acc[0], acc[1], S_);
#pragma unroll
  for (int i = 0; i < 2; ++i) { unsigned xo[8], yo[8]; float x1[16], s = 0.f;
#pragma unroll
    for (int h = 0; h < 2; ++h) { const u32x4 xs = rowswap(xr[i][h]);
#pragma unroll
      for (int e = 0; e < 4; ++e) { const int r = 8 * h + 2 * e, q = r >> 2, c = r & 3;
        const unsigned u = pk(blo(xs[e]) + ga[q][c] * acc[i][r], bhi(xs[e]) + ga[q][c + 1] * acc[i][r + 1]);
        x1[r] = blo(u); x1[r + 1] = bhi(u); xo[4 * h + e] = u; yo[4 * h + e] = pk(x1[r] * cb[q][c], x1[r + 1] * cb[q][c + 1]); s += x1[r] + x1[r + 1]; } }
    const size_t off = (size_t)((tb32 + i) * 64 + (n0 >> 4)) * 1024 + l * 16;
    gstore(yb + off, (u32x4){yo[0], yo[1], yo[2], yo[3]}); gstore(yb + off + 1024, (u32x4){yo[4], yo[5], yo[6], yo[7]});
    gstore(x1b + off, (u32x4){xo[0], xo[1], xo[2], xo[3]}); gstore(x1b + off + 1024, (u32x4){xo[4], xo[5], xo[6], xo[7]});
    const float mean = swap32add(s) * (1.f / 32.f); float m2 = 0.f;
#pragma unroll
    for (int e = 0; e < 16; ++e) m2 += (x1[e] - mean) * (x1[e] - mean);
    m2 = swap32add(m2);
    if (hi == 0) stat[((tb32 + i) * 32 + (n0 >> 5)) * 32 + l] = (f32x2){mean, m2}; }
  ST_END(3);
}

constexpr int K4_STAGE = 45056, K4_UT = 3 * K4_STAGE, K4_LDS = K4_UT + 2048;
struct FragB { bf16x8 a, b[7]; };
__global__ __launch_bounds__(512, 2) void k_ffn1(const char* __restrict__ yb, const char* __restrict__ w1, const f32x2* __restrict__ stat,
                                                 const float* __restrict__ utp, char* __restrict__ gb) {
  const lptr sm = (lptr)smem_raw;
  ST_INIT();
  const int b = blockIdx.x, t = threadIdx.x, l = t & 63, w = __builtin_amdgcn_readfirstlane(t >> 6);
  const int xcd = b & 7, j = b >> 3, tb = (xcd >> 2) * 8 + (j & 7), nt = (xcd & 3) * 4 + (j >> 3);
  if (w >= 4) { const int lw = w - 4;
    const char* ap = yb + (size_t)(tb * 4 + lw) * 64 * 1024; const char* wp = w1 + nt * 448;
    unsigned vb[7];
#pragma unroll
    for (int i = 0; i < 7; ++i) { const int c = (lw * 7 + i) * 64 + l, r = c / 28; vb[i] = r * (FF * 2) + (c - r * 28) * 16 + 3072 - i * 1024; }
    const unsigned va = l * 16;
    loader<16, 3, 11, K4_STAGE, 0>([&](int tt, int slot) {
      SETM0(slot + lw * 4096); const char* a = ap + (size_t)tt * 4096;
      glds<0>(va, a); glds<1024>(va, a); glds<2048>(va, a); glds<3072>(va, a);
      SETM0(slot + 16384 + lw * 7168 + 3072); const char* s = wp + (size_t)tt * (64 * FF * 2);
      glds<-3072>(vb[0], s); glds<-2048>(vb[1], s); glds<-1024>(vb[2], s); glds<0>(vb[3], s); glds<1024>(vb[4], s); glds<2048>(vb[5], s); glds<3072>(vb[6], s); }, [] {});
    return; }
  const int hi = l >> 5, tb32 = tb * 4 + w;
  constexpr float NL2E = -1.4426950408889634f;
  float rs, nm;
  { f32x2 st[16]; float up[8];
#pragma unroll
    for (int i = 0; i < 16; ++i) st[i] = stat[(tb32 * 32 + 2 * i + hi) * 32 + (l & 31)];
    const int col = nt * 224 + (t < 224 ? t : 0);
#pragma unroll
    for (int i = 0; i < 8; ++i) up[i] = utp[i * FF + col];
    float mean = 0.f;
#pragma unroll
    for (int i = 0; i < 16; ++i) mean += st[i][0];
    mean = swap32add(mean) * (1.f / 32.f); float m2 = 0.f;
#pragma unroll
    for (int i = 0; i < 16; ++i) m2 += st[i][1] + 32.f * (st[i][0] - mean) * (st[i][0] - mean);
    rs = NL2E * __builtin_amdgcn_rsqf(swap32add(m2) * (1.f / 1024.f) + 1e-5f); nm = -rs * mean;
    if (t < 224) { *(LDS float*)(sm + K4_UT + t * 4) = (up[0] + up[2]) + (up[4] + up[6]); *(LDS float*)(sm + K4_UT + 1024 + t * 4) = NL2E * ((up[1] + up[3]) + (up[5] + up[7])); } }
  ST(1);
  const lptr ra = sm + w * 4096 + l * 16;
  const lptr rb = sm + 16384 + (4 * hi + ((l >> 2) & 3)) * 448 + ((l >> 4) & 1) * 32 + (l & 3) * 8;
  f32x16 acc[7];
#pragma unroll
  for (int n = 0; n < 7; ++n) acc[n] = (f32x16){};
  FragB f, g; int slot = 0;
  auto kstep = [&](FragB& cur, FragB& nxt, int k) {
    nxt.a = ldsA(ra + slot + k * 1024);
#pragma unroll
    for (int n = 0; n < 7; ++n) { acc[n] = mfma32(cur.b[n], cur.a, acc[n]); nxt.b[n] = ldsT(rb + slot + k * 7168 + n * 64, 3584); SB(); } };
  bar(); ST(2);
  f.a = ldsA(ra);
#pragma unroll
  for (int n = 0; n < 7; ++n) f.b[n] = ldsT(rb + n * 64, 3584);
  for (int tt = 0; tt < 15; ++tt) { kstep(f, g, 1); kstep(g, f, 2); kstep(f, g, 3); bar(); slot = slot == 2 * K4_STAGE ? 0 : slot + K4_STAGE; kstep(g, f, 0); }
  kstep(f, g, 1); kstep(g, f, 2); kstep(f, g, 3);
#pragma unroll
  for (int n = 0; n < 7; ++n) acc[n] = mfma32(g.b[n], g.a, acc[n]);
  ST(3);
  char* d = gb + (size_t)(tb32 * 224 + nt * 14) * 1024 + l * 16;
#pragma unroll
  for (int n = 0; n < 7; ++n) { unsigned o[8];
#pragma unroll
    for (int q = 0; q < 4; ++q) { const f32x4 u = *(const LDS f32x4*)(sm + K4_UT + (n * 32 + 8 * q + 4 * hi) * 4), tv = *(const LDS f32x4*)(sm + K4_UT + 1024 + (n * 32 + 8 * q + 4 * hi) * 4);
#pragma unroll
      for (int i = 0; i < 2; ++i) { const f32x2 z = rs * (f32x2){acc[n][4 * q + 2 * i], acc[n][4 * q + 2 * i + 1]} + (nm * (f32x2){u[2 * i], u[2 * i + 1]} + (f32x2){tv[2 * i], tv[2 * i + 1]});
        const f32x2 dn = NL2E * (f32x2){EX2(z[0]), EX2(z[1])} + NL2E;
        const f32x2 r = z * (f32x2){__builtin_amdgcn_rcpf(dn[0]), __builtin_amdgcn_rcpf(dn[1])}; o[2 * q + i] = pk(r[0], r[1]); } }
    gstore(d + n * 2048, (u32x4){o[0], o[1], o[2], o[3]}); gstore(d + n * 2048 + 1024, (u32x4){o[4], o[5], o[6], o[7]}); }
  ST_END(4);
}

constexpr int K5_S = 6, K5_LDS = K5_S * GA_STAGE;
__global__ __launch_bounds__(512, 2) void k_ffn2(const char* __restrict__ gb, const char* __restrict__ w2, const char* __restrict__ x1b,
                                                 const float* __restrict__ modf, char* __restrict__ out) {
  const lptr sm = (lptr)smem_raw;
  ST_INIT();
  const int b = blockIdx.x, l = threadIdx.x & 63, w = __builtin_amdgcn_readfirstlane(threadIdx.x >> 6);
  const int xcd = b & 7, j = b >> 3, tb = (xcd >> 1) * 4 + (j & 3), nt = (xcd & 1) * 8 + (j >> 2);
  if (w >= 4) { gemmA_load<56, K5_S, DM, 0>(gb + (size_t)tb * 4 * 224 * 1024, w2 + nt * 128, l, w - 4, [] {}); return; }
  const int hi = l >> 5, tb32 = tb * 4 + 2 * (w & 1), n0 = nt * 64 + (w >> 1) * 32;
  u32x4 xr[2][2]; f32x4 g[4];
#pragma unroll
  for (int i = 0; i < 2; ++i)
#pragma unroll
    for (int h = 0; h < 2; ++h) xr[i][h] = *(const u32x4*)(x1b + (size_t)((tb32 + i) * 64 + (n0 >> 4) + h) * 1024 + l * 16);
#pragma unroll
  for (int q = 0; q < 4; ++q) g[q] = *(const f32x4*)(modf + 5 * DM + n0 + 8 * q + 4 * hi);
  f32x16 acc[2]; gemmA_compute<56, K5_S>(sm, l, w, acc[0], acc[1], S_);
#pragma unroll
  for (int i = 0; i < 2; ++i)
#pragma unroll
    for (int h = 0; h < 2; ++h) { u32x4 o;
#pragma unroll
      for (int e = 0; e < 4; ++e) { const int r = 8 * h + 2 * e, q = r >> 2, c = r & 3; o[e] = pk(blo(xr[i][h][e]) + g[q][c] * acc[i][r], bhi(xr[i][h][e]) + g[q][c + 1] * acc[i][r + 1]); }
      gstore(out + (size_t)((tb32 + i) * 32 + (l & 31)) * 2048 + (n0 + 16 * h + 8 * hi) * 2, rowswap(o)); }
  ST_END(5);
}

extern "C" void kernel_launch(void* const* d_in, const int* in_sizes, int n_in, void* d_out, int out_size, void* d_ws, size_t ws_size, hipStream_t stream) {
  const char* x = (const char*)d_in[0]; const char* wq = (const char*)d_in[1]; const char* wk = (const char*)d_in[2]; const char* wv = (const char*)d_in[3];
  const char* wo = (const char*)d_in[4]; const char* w1 = (const char*)d_in[6]; const char* w2 = (const char*)d_in[7];
  const char* c = (const char*)d_in[8]; const char* wmod = (const char*)d_in[9];
  char* ws = (char*)d_ws;
  (void)hipFuncSetAttribute((const void*)k_ln1kv, hipFuncAttributeMaxDynamicSharedMemorySize, K1_LDS);
  (void)hipFuncSetAttribute((const void*)k_qattn, hipFuncAttributeMaxDynamicSharedMemorySize, K2_LDS);
  (void)hipFuncSetAttribute((const void*)k_oproj, hipFuncAttributeMaxDynamicSharedMemorySize, K3_LDS);
  (void)hipFuncSetAttribute((const void*)k_ffn1, hipFuncAttributeMaxDynamicSharedMemorySize, K4_LDS);
  (void)hipFuncSetAttribute((const void*)k_ffn2, hipFuncAttributeMaxDynamicSharedMemorySize, K5_LDS);
  float* modf = (float*)(ws + WS_MODF); uint16_t* modh = (uint16_t*)(ws + WS_MODH);
  k_mod<<<256, 512, 0, stream>>>(c, wmod, modf, modh, x, wq, wk, wv, ws, (const char*)d_out);
  k_ln1kv<<<256, 512, K1_LDS, stream>>>(x, wk, wv, c, wmod, modf, modh, ws + WS_H, ws + WS_KV, wq);
  k_qattn<<<256, 512, K2_LDS, stream>>>(ws + WS_H, wq, ws + WS_KV, ws + WS_O, w1, ws + WS_MODH, (float*)(ws + WS_UTP), wo, w2, c, wmod, modf);
  k_oproj<<<256, 512, K3_LDS, stream>>>(ws + WS_O, wo, x, modf, ws + WS_X1, ws + WS_Y, (f32x2*)(ws + WS_STAT));
  k_ffn1<<<256, 512, K4_LDS, stream>>>(ws + WS_Y, w1, (const f32x2*)(ws + WS_STAT), (const float*)(ws + WS_UTP), ws + WS_G);
  k_ffn2<<<256, 512, K5_LDS, stream>>>(ws + WS_G, w2, ws + WS_X1, modf, (char*)d_out);
}
```

```cpp
#include <hip/hip_runtime.h>
#include <hip/hip_bf16.h>
#include <stdint.h>

typedef __attribute__((ext_vector_type(2))) float f32x2;
typedef __attribute__((ext_vector_type(4))) float f32x4;
typedef __attribute__((ext_vector_type(16))) float f32x16;
typedef __attribute__((ext_vector_type(2))) __bf16 bf16x2;
typedef __attribute__((ext_vector_type(8))) __bf16 bf16x8;
typedef __attribute__((ext_vector_type(4))) short s16x4;
typedef __attribute__((ext_vector_type(2))) unsigned u32x2;
typedef __attribute__((ext_vector_type(4))) unsigned u32x4;
#define LDS __attribute__((address_space(3)))
typedef LDS char* lptr;
#define PREFETCH 1
#define PF0 768
#define DEV __device__ __forceinline__

constexpr int SEQ = 2048, DM = 1024, FF = 3584, NMOD = 6 * DM;
constexpr size_t WS_MODP = 0;
constexpr size_t WS_UTP = 98304;
constexpr size_t WS_MODF = 212992;
constexpr size_t WS_MODH = 237568;
constexpr size_t WS_STAT = 262144;
constexpr size_t WS_KV = 786432;
constexpr size_t WS_H = (size_t)1 << 20;
constexpr size_t WS_O = (size_t)5 << 20;
constexpr size_t WS_X1 = (size_t)9 << 20;
constexpr size_t WS_Y = (size_t)13 << 20;
constexpr size_t WS_G = (size_t)17 << 20;

DEV unsigned pk(float a, float b) { return __builtin_bit_cast(unsigned, __builtin_convertvector((f32x2){a, b}, bf16x2)); }
DEV float rbf(float a) { return __uint_as_float(pk(a, 0.f) << 16); }
DEV float blo(unsigned u) { return __uint_as_float(u << 16); }
DEV float bhi(unsigned u) { return __uint_as_float(u & 0xffff0000u); }
DEV float silu(float z) { return z * __builtin_amdgcn_rcpf(1.f + __builtin_amdgcn_exp2f(-1.4426950408889634f * z)); }
DEV f32x16 mfma32(bf16x8 a, bf16x8 b, f32x16 c) { return __builtin_amdgcn_mfma_f32_32x32x16_bf16(a, b, c, 0, 0, 0); }
DEV f32x4 mfma16(bf16x8 a, bf16x8 b, f32x4 c) { return __builtin_amdgcn_mfma_f32_16x16x32_bf16(a, b, c, 0, 0, 0); }
DEV bf16x8 asb(u32x4 v) { return __builtin_bit_cast(bf16x8, v); }
DEV bf16x8 ldsA(lptr p) { return *(const LDS bf16x8*)p; }
DEV bf16x8 ldsT(lptr p, int second) {
  s16x4 a = __builtin_amdgcn_ds_read_tr16_b64_v4i16((LDS s16x4*)p);
  s16x4 b = __builtin_amdgcn_ds_read_tr16_b64_v4i16((LDS s16x4*)(p + second));
  return __builtin_bit_cast(bf16x8, __builtin_shufflevector(a, b, 0, 1, 2, 3, 4, 5, 6, 7));
}
template <int C> DEV float dppadd(float v) { return v + __int_as_float(__builtin_amdgcn_update_dpp(0, __float_as_int(v), C, 0xf, 0xf, true)); }
DEV float swap32add(float v) { auto r = __builtin_amdgcn_permlane32_swap(__float_as_uint(v), __float_as_uint(v), false, false); return __uint_as_float(r[0]) + __uint_as_float(r[1]); }
DEV float swap16add(float v) { auto r = __builtin_amdgcn_permlane16_swap(__float_as_uint(v), __float_as_uint(v), false, false); return __uint_as_float(r[0]) + __uint_as_float(r[1]); }
DEV float swap32max(float v) { auto r = __builtin_amdgcn_permlane32_swap(__float_as_uint(v), __float_as_uint(v), false, false); return __builtin_fmaxf(__uint_as_float(r[0]), __uint_as_float(r[1])); }
DEV float wavesum(float v) { v = dppadd<0xB1>(v); v = dppadd<0x4E>(v); v = dppadd<0x141>(v); v = dppadd<0x140>(v); v = swap16add(v); return swap32add(v); }

#define SETM0(x) asm volatile("s_mov_b32 m0, %0\n\ts_nop 0" ::"s"(x) : "memory")
template <int OFF> DEV void glds(unsigned voff, const void* base) { asm volatile("global_load_lds_dwordx4 %0, %1 offset:%2" ::"v"(voff), "s"(base), "i"(OFF) : "memory"); }
template <int N> DEV void waitbar() { asm volatile("s_waitcnt vmcnt(%0) lgkmcnt(0)\n\ts_barrier" ::"i"(N) : "memory"); }
template <int OFF> DEV u32x4 gload(unsigned voff, const void* base) { u32x4 r; asm volatile("global_load_dwordx4 %0, %1, %2 offset:%3" : "=v"(r) : "v"(voff), "s"(base), "i"(OFF) : "memory"); return r; }
#define STPOL "sc1"
DEV void gstore(void* p, u32x4 v) { asm volatile("global_store_dwordx4 %0, %1, off " STPOL "\n\ts_nop 1" ::"v"(p), "v"(v) : "memory"); }
#define SB() __builtin_amdgcn_sched_barrier(0)
extern __shared__ __attribute__((aligned(16))) char smem_raw[];

struct Stamps {};
#define ST_INIT() Stamps S_
#define ST(i)
#define ST_END(k)

__global__ __launch_bounds__(256) void k_mod(const uint16_t* __restrict__ c, const char* __restrict__ wmod, float* __restrict__ modp,
                                             const char* __restrict__ x, const char* __restrict__ wq, const char* __restrict__ wk, const char* __restrict__ wv) {
  __shared__ float red[4][32];
  ST_INIT();
  const int t = threadIdx.x;
  if (blockIdx.x >= PF0 && blockIdx.x < PF0 + 256) { const int p = blockIdx.x - PF0; u32x4 d[7];
    const char* xb = x + (size_t)((p & 7) * 32 + (p >> 3)) * 16384;
#pragma unroll
    for (int i = 0; i < 4; ++i) d[i] = gload<0>(t * 16, xb + i * 4096);
    d[4] = gload<0>(t * 16, wq + p * 8192); d[5] = gload<0>(t * 16, wq + p * 8192 + 4096);
    d[6] = gload<0>((t & 31) * 16, (p & 128 ? wv : wk) + (p & 127) * 512);
    asm volatile("s_waitcnt vmcnt(0)" : "+v"(d[0]), "+v"(d[1]), "+v"(d[2]), "+v"(d[3]), "+v"(d[4]), "+v"(d[5]), "+v"(d[6])::"memory");
    return; }
  const int b = blockIdx.x < PF0 ? blockIdx.x : blockIdx.x - 256, xcd = b & 7, j = b >> 3;
  const int cg = (j & 1) + 2 * (xcd + 8 * ((j >> 1) % 12)), ks = j / 24;
  const int row0 = ks * 256 + (t >> 2);
  const char* p = wmod + (size_t)row0 * (NMOD * 2) + cg * 64 + (t & 3) * 16;
  u32x4 w[4]; float s[4];
#pragma unroll
  for (int i = 0; i < 4; ++i) { w[i] = *(const u32x4*)(p + (size_t)i * 64 * NMOD * 2); s[i] = __uint_as_float((unsigned)c[row0 + 64 * i] << 16); }
  float a[8] = {0, 0, 0, 0, 0, 0, 0, 0};
#pragma unroll
  for (int i = 0; i < 4; ++i) { const float f = rbf(silu(s[i]));
#pragma unroll
    for (int e = 0; e < 4; ++e) { a[2 * e] += f * blo(w[i][e]); a[2 * e + 1] += f * bhi(w[i][e]); } }
#pragma unroll
  for (int e = 0; e < 8; ++e) { float v = a[e]; v += __shfl_xor(v, 4); v += __shfl_xor(v, 8); v = swap16add(v); a[e] = swap32add(v); }
  if ((t & 63) < 4) {
#pragma unroll
    for (int e = 0; e < 8; ++e) red[t >> 6][(t & 3) * 8 + e] = a[e]; }
  __syncthreads();
  if (t < 32) modp[ks * NMOD + cg * 32 + t] = (red[0][t] + red[1][t]) + (red[2][t] + red[3][t]);
  ST_END(0);
}

constexpr int K1_W = 0, K1_H = 65536, K1_MV = K1_H + 64 * 528, K1_RED = K1_MV + 8192, K1_LDS = K1_RED + 32768;
__global__ __launch_bounds__(512, 2) void k_ln1kv(const char* __restrict__ x, const char* __restrict__ wk, const char* __restrict__ wv,
                                                  const char* __restrict__ modp, float* __restrict__ modf, uint16_t* __restrict__ modh,
                                                  char* __restrict__ hbuf, char* __restrict__ kv) {
  const lptr sm = (lptr)smem_raw;
  ST_INIT();
  const int b = blockIdx.x, t = threadIdx.x, l = t & 63, w = __builtin_amdgcn_readfirstlane(t >> 6);
  const int which = (b >> 3) & 1, rb = (b & 7) * 16 + (b >> 4);
  u32x4 xv[4], mp[4];
  { const char* xb = x + (size_t)(rb * 16 + 2 * w) * 2048; const unsigned vo = l * 32;
    xv[0] = gload<0>(vo, xb); xv[1] = gload<16>(vo, xb); xv[2] = gload<2048>(vo, xb); xv[3] = gload<2064>(vo, xb);
#pragma unroll
    for (int s = 0; s < 4; ++s) mp[s] = gload<0>(t * 16, modp + s * NMOD * 4); }
  { const char* wsrc = (which ? wv : wk) + w * 8192;
#pragma unroll
    for (int g = 0; g < 2; ++g) { SETM0(K1_W + w * 8192 + g * 4096); const char* s = wsrc + g * 4096;
      glds<0>(l * 16, s); glds<1024>(l * 16, s); glds<2048>(l * 16, s); glds<3072>(l * 16, s); } }
  asm volatile("s_waitcnt vmcnt(8)" : "+v"(xv[0]), "+v"(xv[1]), "+v"(xv[2]), "+v"(xv[3]), "+v"(mp[0]), "+v"(mp[1]), "+v"(mp[2]), "+v"(mp[3])::"memory");
  ST(1);
  { f32x4 o;
#pragma unroll
    for (int e = 0; e < 4; ++e) { const float v = rbf((__uint_as_float(mp[0][e]) + __uint_as_float(mp[1][e])) + (__uint_as_float(mp[2][e]) + __uint_as_float(mp[3][e])));
      o[e] = t >= 256 ? rbf(1.f + v) : v; }
    *(LDS f32x4*)(sm + K1_MV + (t & 256) * 16 + (t & 3) * 1024 + ((t & 255) >> 2) * 16) = o; }
  f32x2 v[2][8]; float rs[2];
#pragma unroll
  for (int r = 0; r < 2; ++r) { f32x2 s = {0.f, 0.f};
#pragma unroll
    for (int e = 0; e < 8; ++e) { const unsigned u = xv[2 * r + (e >> 2)][e & 3]; v[r][e] = (f32x2){blo(u), bhi(u)}; s += v[r][e]; }
    const float m = wavesum(s[0] + s[1]) * (1.f / 1024.f); f32x2 q = {0.f, 0.f};
#pragma unroll
    for (int e = 0; e < 8; ++e) { v[r][e] -= m; q += v[r][e] * v[r][e]; }
    rs[r] = __builtin_amdgcn_rsqf(wavesum(q[0] + q[1]) * (1.f / 1024.f) + 1e-5f); }
  asm volatile("s_waitcnt lgkmcnt(0)\n\ts_barrier" ::: "memory");
  { unsigned u[2][8];
#pragma unroll
    for (int e = 0; e < 4; ++e) { const f32x4 a = *(const LDS f32x4*)(sm + K1_MV + e * 1024 + l * 16), cc = *(const LDS f32x4*)(sm + K1_MV + 4096 + e * 1024 + l * 16);
#pragma unroll
      for (int r = 0; r < 2; ++r)
#pragma unroll
        for (int i = 0; i < 2; ++i) { const f32x2 h = v[r][2 * e + i] * rs[r] * (f32x2){cc[2 * i], cc[2 * i + 1]} + (f32x2){a[2 * i], a[2 * i + 1]}; u[r][2 * e + i] = pk(h[0], h[1]); } }
#pragma unroll
    for (int r = 0; r < 2; ++r) { const lptr d = sm + K1_H + l * 528 + (2 * w + r) * 16;
      *(LDS u32x4*)d = (u32x4){u[r][0], u[r][1], u[r][4], u[r][5]}; *(LDS u32x4*)(d + 256) = (u32x4){u[r][2], u[r][3], u[r][6], u[r][7]}; } }
  ST(2); waitbar<0>(); ST(3);
  f32x16 acc = {};
  { const int hi = l >> 5; const lptr hp = sm + K1_H + w * 8 * 528 + hi * 256 + (l & 15) * 16;
    const lptr wp = sm + K1_W + (w * 128 + 4 * hi + ((l >> 2) & 3)) * 64 + ((l >> 4) & 1) * 32 + (l & 3) * 8;
    bf16x8 fa[8], fb[8];
#pragma unroll
    for (int i = 0; i < 8; ++i) { fa[i] = ldsA(hp + i * 528); fb[i] = ldsT(wp + i * 1024, 512); }
#pragma unroll
    for (int i = 0; i < 2; ++i) { const int id = t + 512 * i, tok = id & 15, h2 = (id >> 4) & 1, kb = which * 32 + (id >> 5);
      gstore(hbuf + (size_t)((rb >> 1) * 64 + kb) * 1024 + (h2 * 32 + (rb & 1) * 16 + tok) * 16, *(const LDS u32x4*)(sm + K1_H + kb * 528 + h2 * 256 + tok * 16)); }
    if (which == 0) {
#pragma unroll
      for (int i = 0; i < 8; ++i) acc = mfma32(fb[i], fa[i], acc);
    } else {
#pragma unroll
      for (int i = 0; i < 8; ++i) acc = mfma32(fa[i], fb[i], acc);
    } }
#pragma unroll
  for (int q = 0; q < 4; ++q) *(LDS f32x4*)(sm + K1_RED + w * 4096 + q * 1024 + l * 16) = (f32x4){acc[4 * q], acc[4 * q + 1], acc[4 * q + 2], acc[4 * q + 3]};
  asm volatile("s_waitcnt lgkmcnt(0)\n\ts_barrier" ::: "memory");
  if (w == 0) {
    const lptr rp = sm + K1_RED + (which ? l * 16 : (l >> 5) * 2048 + (((l >> 4) & 1) * 32 + (l & 15)) * 16);
    f32x4 a = {}, c = {};
#pragma unroll
    for (int u = 0; u < 8; ++u) { a += *(const LDS f32x4*)(rp + u * 4096); c += *(const LDS f32x4*)(rp + u * 4096 + 1024); }
    char* base = kv + (rb >> 1) * 4096;
    char* d = which ? base + 2048 + (rb & 1) * 1024 + l * 16 : base + (l >> 5) * 1024 + (((l >> 4) & 1) * 32 + (rb & 1) * 16 + (l & 15)) * 16;
    *(u32x4*)d = (u32x4){pk(a[0], a[1]), pk(a[2], a[3]), pk(c[0], c[1]), pk(c[2], c[3])};
  } else if (b < 64 && w == 1) {
    const int col = 2048 + b * 64 + l; const float* mpf = (const float*)modp;
    float v2 = rbf((mpf[col] + mpf[NMOD + col]) + (mpf[2 * NMOD + col] + mpf[3 * NMOD + col]));
    if ((col >> 10) == 4) v2 = rbf(1.f + v2);
    modf[col] = v2; modh[col] = (uint16_t)(__float_as_uint(v2) >> 16);
  }
  ST_END(1);
}

DEV void bar() { asm volatile("s_waitcnt lgkmcnt(0)\n\ts_barrier" ::: "memory"); }
template <int N, int G, int EXTRA> DEV void loader_tail() { waitbar<G * N + EXTRA>(); if constexpr (N > 0) loader_tail<N - 1, G, EXTRA>(); }
constexpr int issued(int t, int S) { return t == 0 ? 2 : (2 + 2 * t < S + t - 1 ? 2 + 2 * t : S + t - 1); }
template <int T, int S, int G, int STAGE, class I> DEV void loader_ramp(I&& issue) {
#pragma unroll
  for (int s = T == 0 ? 0 : issued(T - 1, S); s < issued(T, S); ++s) issue(s, (s % S) * STAGE);
  waitbar<G*(issued(T, S) - T - 1)>();
  if constexpr (issued(T, S) < S + T - 1 || T == 0) loader_ramp<T + 1, S, G, STAGE>(issue);
}
constexpr int ramp_end(int S) { int t = 1; while (issued(t, S) < S + t - 1) ++t; return t; }
template <int KT, int S, int G, int STAGE, int EXTRA, class I, class X>
DEV void loader(I&& issue, X&& extra) {
  loader_ramp<0, S, G, STAGE>(issue);
  constexpr int T0 = ramp_end(S);
  int slot = ((S + T0 - 1) % S) * STAGE;
  for (int t = T0; t <= KT - S; ++t) { issue(t - 1 + S, slot); slot = slot == (S - 1) * STAGE ? 0 : slot + STAGE; if (t < KT - S) waitbar<G*(S - 2)>(); }
  extra();
  loader_tail<S - 2, G, EXTRA>();
}
constexpr int GA_STAGE = 24576;
template <int KT, int S, int LDW, int EXTRA, class X>
DEV void gemmA_load(const char* act, const char* wgt, int l, int lw, X&& extra) {
  const char* ap = act + (size_t)lw * (KT * 4096);
  const char* wp = wgt + (size_t)lw * (16 * LDW * 2);
  const unsigned va = l * 16, vb = (l >> 3) * (LDW * 2) + (((l & 7) ^ (((l >> 4) & 1) << 2)) * 16);
  loader<KT, S, 6, GA_STAGE, EXTRA>([&](int t, int slot) {
    SETM0(slot + lw * 4096); const char* a = ap + (size_t)t * 4096; glds<0>(va, a); glds<1024>(va, a); glds<2048>(va, a); glds<3072>(va, a);
    SETM0(slot + 16384 + lw * 2048); const char* s = wp + (size_t)t * (64 * LDW * 2); glds<0>(vb, s); glds<1024>(vb, s + 8 * LDW * 2 - 1024); }, extra);
}
struct FragA { bf16x8 a0[4], a1[4], b[4]; };
template <int KT, int S>
DEV void gemmA_compute(const lptr sm, int l, int w, f32x16& c0, f32x16& c1, Stamps& S_) {
  const int q = (l >> 2) & 3, p = l & 3;
  const lptr ra = sm + (w & 1) * 8192 + l * 16;
  const lptr rb = sm + 16384 + (4 * (l >> 5) + q) * 128 + ((((w >> 1) * 4 + ((l >> 4) & 1) * 2 + (p >> 1)) ^ ((q >> 1) << 2)) * 16) + (p & 1) * 8;
  c0 = (f32x16){}; c1 = (f32x16){};
  FragA f, g; int slot = 0;
  auto rd = [&](FragA& d, int k) { d.a0[k] = ldsA(ra + slot + k * 1024); d.b[k] = ldsT(rb + slot + k * 2048, 1024); d.a1[k] = ldsA(ra + slot + 4096 + k * 1024); };
  auto step = [&](FragA& cur, FragA& nxt) { bar(); slot = slot == (S - 1) * GA_STAGE ? 0 : slot + GA_STAGE;
#pragma unroll
    for (int k = 0; k < 4; ++k) { if (k < 2) rd(nxt, 2 * k); c0 = mfma32(cur.b[k], cur.a0[k], c0); SB(); if (k < 2) rd(nxt, 2 * k + 1); c1 = mfma32(cur.b[k], cur.a1[k], c1); SB(); } };
  bar(); ST(2);
#pragma unroll
  for (int k = 0; k < 4; ++k) rd(f, k);
  for (int t = 0; t < KT - 2; t += 2) { step(f, g); step(g, f); }
  step(f, g);
#pragma unroll
  for (int k = 0; k < 4; ++k) { c0 = mfma32(g.b[k], g.a0[k], c0); c1 = mfma32(g.b[k], g.a1[k], c1); }
  ST(3);
}

constexpr int K2_KV0 = 98304, K2_KV1 = 131072, K2_KV2 = 0, K2_UW = 32768, K2_Q = 65536, K2_RED = 73728, K2_DUMP = 81920, K2_LDS = 163840;
DEV void pack16(const f32x16& s, u32x4& a, u32x4& b) {
  a = (u32x4){pk(s[0], s[1]), pk(s[2], s[3]), pk(s[4], s[5]), pk(s[6], s[7])};
  b = (u32x4){pk(s[8], s[9]), pk(s[10], s[11]), pk(s[12], s[13]), pk(s[14], s[15])}; }
DEV float max16(const f32x16& s) { float m = s[0];
#pragma unroll
  for (int i = 1; i < 16; ++i) m = __builtin_fmaxf(m, s[i]);
  return swap32max(m); }
#define EX2(v) __builtin_amdgcn_exp2f(v)
#define PIN(x) asm volatile("" : "+v"(x))

__global__ __launch_bounds__(512, 2) void k_qattn(const char* __restrict__ hbuf, const char* __restrict__ wq, const char* __restrict__ kv, char* __restrict__ obuf,
                                                  const char* __restrict__ w1, const char* __restrict__ modh, float* __restrict__ utp,
                                                  const char* __restrict__ wo, const char* __restrict__ w2) {
  const lptr sm = (lptr)smem_raw;
  ST_INIT();
  const int b = blockIdx.x, l = threadIdx.x & 63, w = __builtin_amdgcn_readfirstlane(threadIdx.x >> 6);
  const int xcd = b & 7, j = b >> 3, tb = (xcd >> 1) * 4 + (j & 3), hp = (xcd & 1) * 8 + (j >> 2);
  const unsigned l16 = l * 16;
  u32x4 q0u, q1u;
  if (w >= 4) {
    gemmA_load<16, 4, DM, 8>(hbuf + (size_t)tb * 4 * 64 * 1024, wq + hp * 128, l, w - 4, [&] {
#pragma unroll
      for (int g = 0; g < 2; ++g) { SETM0(K2_KV0 + (w - 4) * 8192 + g * 4096); const char* s = kv + (w - 4) * 8192 + g * 4096;
        glds<0>(l16, s); glds<1024>(l16, s); glds<2048>(l16, s); glds<3072>(l16, s); } });
    waitbar<0>();
    q0u = *(const LDS u32x4*)(sm + K2_Q + (w - 4) * 2048 + l16); q1u = *(const LDS u32x4*)(sm + K2_Q + (w - 4) * 2048 + 1024 + l16);
  } else {
    f32x16 c0, c1; gemmA_compute<16, 4>(sm, l, w, c0, c1, S_);
    constexpr float QS = 0.17677669529663687f * 1.4426950408889634f;
    c0 *= QS; c1 *= QS;
    u32x4 a, c; pack16(c1, a, c); *(LDS u32x4*)(sm + K2_Q + w * 2048 + l16) = a; *(LDS u32x4*)(sm + K2_Q + w * 2048 + 1024 + l16) = c;
    pack16(c0, q0u, q1u);
    bar();
  }
  const bf16x8 q0 = asb(q0u), q1 = asb(q1u);
  const char* kvw = kv + w * 4096;
  auto kvload = [&](int st, int slot) { SETM0(slot + w * 4096); const char* s = kvw + st * 32768; glds<0>(l16, s); glds<1024>(l16, s); glds<2048>(l16, s); glds<3072>(l16, s); };
  kvload(1, K2_KV1);
  const unsigned one2 = ((l & 7) == ((l >> 4) & 1) * 4) ? 0x3f803f80u : 0u;
  const bf16x8 ones = asb((u32x4){one2, one2, one2, one2});
  const lptr kp0 = sm + K2_KV0 + l16, kp1 = sm + K2_KV1 + l16, kp2 = sm + K2_KV2 + l16;
  bf16x8 k0 = ldsA(kp0), k1 = ldsA(kp0 + 1024), v0 = ldsA(kp0 + 2048), v1 = ldsA(kp0 + 3072);
  f32x16 negm, sA, sB, o = {}; f32x4 ls = {};
  { f32x16 z = {}; z = mfma32(k0, q0, z); z = mfma32(k1, q1, z); const float m = -max16(z);
#pragma unroll
    for (int i = 0; i < 16; ++i) negm[i] = m; }
  PIN(negm);
  sA = mfma32(k0, q0, negm); sA = mfma32(k1, q1, sA);
  k0 = ldsA(kp0 + 4096); k1 = ldsA(kp0 + 4096 + 1024);
  u32x4 p0, p1 = {0, 0, 0, 0}; bf16x8 v1p = v1;
#define TILE(S, N, KP, VP)                                                                                            \
  { N = mfma32(k0, q0, negm); SB();                                                                                  \
    S[0] = EX2(S[0]); S[1] = EX2(S[1]); S[2] = EX2(S[2]); S[3] = EX2(S[3]); p0[0] = pk(S[0], S[1]); p0[1] = pk(S[2], S[3]); PIN(p0); SB(); \
    o = mfma32(v1p, asb(p1), o); k0 = ldsA(KP); SB();                                                                \
    S[4] = EX2(S[4]); S[5] = EX2(S[5]); S[6] = EX2(S[6]); S[7] = EX2(S[7]); p0[2] = pk(S[4], S[5]); p0[3] = pk(S[6], S[7]); PIN(p0); SB(); \
    ls = mfma16(ones, asb(p1), ls); v1p = v1; v1 = ldsA((VP) + 1024); SB();                                          \
    S[8] = EX2(S[8]); S[9] = EX2(S[9]); PIN(S); SB();                                                                \
    N = mfma32(k1, q1, N); SB();                                                                                     \
    S[10] = EX2(S[10]); S[11] = EX2(S[11]); S[12] = EX2(S[12]); S[13] = EX2(S[13]); p1[0] = pk(S[8], S[9]); p1[1] = pk(S[10], S[11]); PIN(p1); PIN(S); SB(); \
    o = mfma32(v0, asb(p0), o); k1 = ldsA((KP) + 1024); SB();                                                        \
    S[14] = EX2(S[14]); S[15] = EX2(S[15]); p1[2] = pk(S[12], S[13]); p1[3] = pk(S[14], S[15]); PIN(p1); SB();       \
    ls = mfma16(ones, asb(p0), ls); v0 = ldsA(VP); SB(); }
#define STAGE(P, Q, ...)                                                                                              \
  TILE(sA, sB, P + 2 * 4096, P + 1 * 4096 + 2048) TILE(sB, sA, P + 3 * 4096, P + 2 * 4096 + 2048)                     \
  TILE(sA, sB, P + 4 * 4096, P + 3 * 4096 + 2048) TILE(sB, sA, P + 5 * 4096, P + 4 * 4096 + 2048)                     \
  TILE(sA, sB, P + 6 * 4096, P + 5 * 4096 + 2048) TILE(sB, sA, P + 7 * 4096, P + 6 * 4096 + 2048)                     \
  { __VA_ARGS__ }                                                                                                     \
  TILE(sA, sB, Q, P + 7 * 4096 + 2048) TILE(sB, sA, Q + 4096, Q + 2048)
  const bool ut = b < 224; const int cg = b % 56, ks = b / 56;
  u32x2 vf[4] = {{0, 0}, {0, 0}, {0, 0}, {0, 0}};
  ST(4);
  STAGE(kp0, kp1, waitbar<0>(); kvload(2, K2_KV2);
        if (ut) { SETM0(K2_UW + w * 4096); const char* s = w1 + (size_t)(ks * 256 + w * 32) * (FF * 2) + cg * 128;
          const unsigned vo = (l >> 3) * (FF * 2) + (((l & 7) ^ (((l >> 4) & 1) << 2)) * 16);
          glds<0>(vo, s); glds<1024>(vo, s + 8 * FF * 2 - 1024); glds<2048>(vo, s + 16 * FF * 2 - 2048); glds<3072>(vo, s + 24 * FF * 2 - 3072);
          if ((l & 31) < 2) { const char* mv = modh + ((4 - (l & 31)) * DM + ks * 256 + w * 32 + 4 * (l >> 5)) * 2;
            _Pragma("unroll") for (int i = 0; i < 4; ++i) vf[i] = *(const u32x2*)(mv + i * 16); } })
  STAGE(kp1, kp2, waitbar<8>(); kvload(3, K2_KV0);)
  STAGE(kp2, kp0, waitbar<0>();
        if (ut) { const int q = (l >> 2) & 3; const int p = l & 3; f32x16 d0 = {}; f32x16 d1 = {};
          const lptr rb = sm + K2_UW + (w * 32 + 4 * (l >> 5) + q) * 128 + (((((l >> 4) & 1) * 2 + (p >> 1)) ^ ((q >> 1) << 2)) * 16) + (p & 1) * 8;
          const lptr rc = sm + K2_UW + (w * 32 + 4 * (l >> 5) + q) * 128 + (((4 + ((l >> 4) & 1) * 2 + (p >> 1)) ^ ((q >> 1) << 2)) * 16) + (p & 1) * 8;
          _Pragma("unroll") for (int kk = 0; kk < 2; ++kk) { const bf16x8 vv = asb((u32x4){vf[2 * kk][0], vf[2 * kk][1], vf[2 * kk + 1][0], vf[2 * kk + 1][1]});
            d0 = mfma32(ldsT(rb + kk * 2048, 1024), vv, d0); d1 = mfma32(ldsT(rc + kk * 2048, 1024), vv, d1); }
          if ((l & 31) < 2) { const lptr d = sm + K2_RED + w * 512 + (l & 31) * 256 + (l >> 5) * 16;
            _Pragma("unroll") for (int i = 0; i < 4; ++i) { *(LDS f32x4*)(d + i * 32) = (f32x4){d0[4 * i], d0[4 * i + 1], d0[4 * i + 2], d0[4 * i + 3]};
              *(LDS f32x4*)(d + 128 + i * 32) = (f32x4){d1[4 * i], d1[4 * i + 1], d1[4 * i + 2], d1[4 * i + 3]}; } } }
        kvload(4, K2_KV1);
        if (PREFETCH) { SETM0(K2_DUMP + w * 1024); glds<0>(l16, wo + b * 8192 + w * 1024); glds<0>(l16, w2 + (size_t)b * 28672 + w * 1024); })
  STAGE(kp0, kp1, waitbar<2>();
        if (ut && w < 2) { float v = 0.f;
          _Pragma("unroll") for (int u = 0; u < 8; ++u) v += *(const LDS float*)(sm + K2_RED + u * 512 + w * 256 + l * 4);
          utp[(ks * 2 + w) * FF + cg * 64 + l] = v; }
        kvload(5, K2_KV2);
        if (PREFETCH) { SETM0(K2_DUMP + w * 1024); glds<0>(l16, w2 + (size_t)b * 28672 + 8192 + w * 1024); glds<0>(l16, w2 + (size_t)b * 28672 + 16384 + w * 1024); })
  STAGE(kp1, kp2, waitbar<2>(); kvload(6, K2_KV0);
        if (PREFETCH) { SETM0(K2_DUMP + w * 1024); glds<0>(l16, w2 + (size_t)b * 28672 + 24576 + (w & 3) * 1024); })
  STAGE(kp2, kp0, waitbar<1>(); kvload(7, K2_KV1);)
  STAGE(kp0, kp1, waitbar<0>();)
  STAGE(kp1, kp2, )
  ST(5);
  o = mfma32(v1p, asb(p1), o); ls = mfma16(ones, asb(p1), ls);
  float lsum = ls[0];
  { float chk = lsum;
#pragma unroll
    for (int i = 0; i < 16; ++i) chk = __builtin_fmaxf(chk, __builtin_fabsf(o[i]) * 0x1p-40f);
    bool bad = !(chk < 0x1p80f);
#pragma unroll
    for (int i = 0; i < 16; ++i) bad |= (o[i] != o[i]);
    bad |= (lsum != lsum);
    if (__any(bad)) {
      float m = -INFINITY; lsum = 0.f; o = (f32x16){};
      for (int t = 0; t < 64; ++t) { const bf16x8* g = (const bf16x8*)(kv + t * 4096) + l;
        f32x16 s = {}; s = mfma32(g[0], q0, s); s = mfma32(g[64], q1, s);
        const float mn = __builtin_fmaxf(m, max16(s)), al = EX2(m - mn); m = mn; float rsum = 0.f;
#pragma unroll
        for (int i = 0; i < 16; ++i) { s[i] = rbf(EX2(s[i] - mn)); rsum += s[i]; o[i] *= al; }
        lsum = lsum * al + swap32add(rsum);
        u32x4 a, c; pack16(s, a, c); o = mfma32(g[128], asb(a), o); o = mfma32(g[192], asb(c), o); } } }
  o *= 1.f / lsum;
  u32x4 o0, o1; pack16(o, o0, o1);
  char* d = obuf + (size_t)((tb * 4 + 2 * (w & 1) + (w >> 2)) * 64 + (hp * 2 + ((w >> 1) & 1)) * 2) * 1024 + l16;
  gstore(d, o0); gstore(d + 1024, o1);
  ST_END(2);
}

DEV u32x4 rowswap(u32x4 v) { auto a = __builtin_amdgcn_permlane32_swap(v[0], v[2], false, false); auto c = __builtin_amdgcn_permlane32_swap(v[1], v[3], false, false);
  return (u32x4){a[0], c[0], a[1], c[1]}; }

constexpr int K3_S = 6, K3_LDS = K3_S * GA_STAGE;
__global__ __launch_bounds__(512, 2) void k_oproj(const char* __restrict__ obuf, const char* __restrict__ wo, const char* __restrict__ x, const float* __restrict__ modf,
                                                  char* __restrict__ x1b, char* __restrict__ yb, f32x2* __restrict__ stat) {
  const lptr sm = (lptr)smem_raw;
  ST_INIT();
  const int b = blockIdx.x, l = threadIdx.x & 63, w = __builtin_amdgcn_readfirstlane(threadIdx.x >> 6);
  const int xcd = b & 7, j = b >> 3, tb = (xcd >> 1) * 4 + (j & 3), nt = (xcd & 1) * 8 + (j >> 2);
  if (w >= 4) { gemmA_load<16, K3_S, DM, 0>(obuf + (size_t)tb * 4 * 64 * 1024, wo + nt * 128, l, w - 4, [] {}); return; }
  const int hi = l >> 5, tb32 = tb * 4 + 2 * (w & 1), n0 = nt * 64 + (w >> 1) * 32;
  u32x4 xr[2][2]; f32x4 ga[4], cb[4];
#pragma unroll
  for (int i = 0; i < 2; ++i)
#pragma unroll
    for (int h = 0; h < 2; ++h) xr[i][h] = *(const u32x4*)(x + (size_t)((tb32 + i) * 32 + (l & 31)) * 2048 + (n0 + 16 * h + 8 * hi) * 2);
#pragma unroll
  for (int q = 0; q < 4; ++q) { ga[q] = *(const f32x4*)(modf + 2 * DM + n0 + 8 * q + 4 * hi); cb[q] = *(const f32x4*)(modf + 4 * DM + n0 + 8 * q + 4 * hi); }
  f32x16 acc[2]; gemmA_compute<16, K3_S>(sm, l, w, acc[0], acc[1], S_);
#pragma unroll
  for (int i = 0; i < 2; ++i) { unsigned xo[8], yo[8]; float x1[16], s = 0.f;
#pragma unroll
    for (int h = 0; h < 2; ++h) { const u32x4 xs = rowswap(xr[i][h]);
#pragma unroll
      for (int e = 0; e < 4; ++e) { const int r = 8 * h + 2 * e, q = r >> 2, c = r & 3;
        const unsigned u = pk(blo(xs[e]) + ga[q][c] * acc[i][r], bhi(xs[e]) + ga[q][c + 1] * acc[i][r + 1]);
        x1[r] = blo(u); x1[r + 1] = bhi(u); xo[4 * h + e] = u; yo[4 * h + e] = pk(x1[r] * cb[q][c], x1[r + 1] * cb[q][c + 1]); s += x1[r] + x1[r + 1]; } }
    const size_t off = (size_t)((tb32 + i) * 64 + (n0 >> 4)) * 1024 + l * 16;
    gstore(yb + off, (u32x4){yo[0], yo[1], yo[2], yo[3]}); gstore(yb + off + 1024, (u32x4){yo[4], yo[5], yo[6], yo[7]});
    gstore(x1b + off, (u32x4){xo[0], xo[1], xo[2], xo[3]}); gstore(x1b + off + 1024, (u32x4){xo[4], xo[5], xo[6], xo[7]});
    const float mean = swap32add(s) * (1.f / 32.f); float m2 = 0.f;
#pragma unroll
    for (int e = 0; e < 16; ++e) m2 += (x1[e] - mean) * (x1[e] - mean);
    m2 = swap32add(m2);
    if (hi == 0) stat[((tb32 + i) * 32 + (n0 >> 5)) * 32 + l] = (f32x2){mean, m2}; }
  ST_END(3);
}

constexpr int K4_STAGE = 45056, K4_UT = 3 * K4_STAGE, K4_LDS = K4_UT + 2048;
struct FragB { bf16x8 a, b[7]; };
__global__ __launch_bounds__(512, 2) void k_ffn1(const char* __restrict__ yb, const char* __restrict__ w1, const f32x2* __restrict__ stat,
                                                 const float* __restrict__ utp, char* __restrict__ gb) {
  const lptr sm = (lptr)smem_raw;
  ST_INIT();
  const int b = blockIdx.x, t = threadIdx.x, l = t & 63, w = __builtin_amdgcn_readfirstlane(t >> 6);
  const int xcd = b & 7, j = b >> 3, tb = (xcd >> 2) * 8 + (j & 7), nt = (xcd & 3) * 4 + (j >> 3);
  if (w >= 4) { const int lw = w - 4;
    const char* ap = yb + (size_t)(tb * 4 + lw) * 64 * 1024; const char* wp = w1 + nt * 448;
    unsigned vb[7];
#pragma unroll
    for (int i = 0; i < 7; ++i) { const int c = (lw * 7 + i) * 64 + l, r = c / 28; vb[i] = r * (FF * 2) + (c - r * 28) * 16 + 3072 - i * 1024; }
    const unsigned va = l * 16;
    loader<16, 3, 11, K4_STAGE, 0>([&](int tt, int slot) {
      SETM0(slot + lw * 4096); const char* a = ap + (size_t)tt * 4096;
      glds<0>(va, a); glds<1024>(va, a); glds<2048>(va, a); glds<3072>(va, a);
      SETM0(slot + 16384 + lw * 7168 + 3072); const char* s = wp + (size_t)tt * (64 * FF * 2);
      glds<-3072>(vb[0], s); glds<-2048>(vb[1], s); glds<-1024>(vb[2], s); glds<0>(vb[3], s); glds<1024>(vb[4], s); glds<2048>(vb[5], s); glds<3072>(vb[6], s); }, [] {});
    return; }
  const int hi = l >> 5, tb32 = tb * 4 + w;
  constexpr float NL2E = -1.4426950408889634f;
  float rs, nm;
  { f32x2 st[16]; float up[8];
#pragma unroll
    for (int i = 0; i < 16; ++i) st[i] = stat[(tb32 * 32 + 2 * i + hi) * 32 + (l & 31)];
    const int col = nt * 224 + (t < 224 ? t : 0);
#pragma unroll
    for (int i = 0; i < 8; ++i) up[i] = utp[i * FF + col];
    float mean = 0.f;
#pragma unroll
    for (int i = 0; i < 16; ++i) mean += st[i][0];
    mean = swap32add(mean) * (1.f / 32.f); float m2 = 0.f;
#pragma unroll
    for (int i = 0; i < 16; ++i) m2 += st[i][1] + 32.f * (st[i][0] - mean) * (st[i][0] - mean);
    rs = NL2E * __builtin_amdgcn_rsqf(swap32add(m2) * (1.f / 1024.f) + 1e-5f); nm = -rs * mean;
    if (t < 224) { *(LDS float*)(sm + K4_UT + t * 4) = (up[0] + up[2]) + (up[4] + up[6]); *(LDS float*)(sm + K4_UT + 1024 + t * 4) = NL2E * ((up[1] + up[3]) + (up[5] + up[7])); } }
  ST(1);
  const lptr ra = sm + w * 4096 + l * 16;
  const lptr rb = sm + 16384 + (4 * hi + ((l >> 2) & 3)) * 448 + ((l >> 4) & 1) * 32 + (l & 3) * 8;
  f32x16 acc[7];
#pragma unroll
  for (int n = 0; n < 7; ++n) acc[n] = (f32x16){};
  FragB f, g; int slot = 0;
  auto kstep = [&](FragB& cur, FragB& nxt, int k) {
    nxt.a = ldsA(ra + slot + k * 1024);
#pragma unroll
    for (int n = 0; n < 7; ++n) { acc[n] = mfma32(cur.b[n], cur.a, acc[n]); nxt.b[n] = ldsT(rb + slot + k * 7168 + n * 64, 3584); SB(); } };
  bar(); ST(2);
  f.a = ldsA(ra);
#pragma unroll
  for (int n = 0; n < 7; ++n) f.b[n] = ldsT(rb + n * 64, 3584);
  for (int tt = 0; tt < 15; ++tt) { kstep(f, g, 1); kstep(g, f, 2); kstep(f, g, 3); bar(); slot = slot == 2 * K4_STAGE ? 0 : slot + K4_STAGE; kstep(g, f, 0); }
  kstep(f, g, 1); kstep(g, f, 2); kstep(f, g, 3);
#pragma unroll
  for (int n = 0; n < 7; ++n) acc[n] = mfma32(g.b[n], g.a, acc[n]);
  ST(3);
  char* d = gb + (size_t)(tb32 * 224 + nt * 14) * 1024 + l * 16;
#pragma unroll
  for (int n = 0; n < 7; ++n) { unsigned o[8];
#pragma unroll
    for (int q = 0; q < 4; ++q) { const f32x4 u = *(const LDS f32x4*)(sm + K4_UT + (n * 32 + 8 * q + 4 * hi) * 4), tv = *(const LDS f32x4*)(sm + K4_UT + 1024 + (n * 32 + 8 * q + 4 * hi) * 4);
#pragma unroll
      for (int i = 0; i < 2; ++i) { const f32x2 z = rs * (f32x2){acc[n][4 * q + 2 * i], acc[n][4 * q + 2 * i + 1]} + (nm * (f32x2){u[2 * i], u[2 * i + 1]} + (f32x2){tv[2 * i], tv[2 * i + 1]});
        const f32x2 dn = NL2E * (f32x2){EX2(z[0]), EX2(z[1])} + NL2E;
        const f32x2 r = z * (f32x2){__builtin_amdgcn_rcpf(dn[0]), __builtin_amdgcn_rcpf(dn[1])}; o[2 * q + i] = pk(r[0], r[1]); } }
    gstore(d + n * 2048, (u32x4){o[0], o[1], o[2], o[3]}); gstore(d + n * 2048 + 1024, (u32x4){o[4], o[5], o[6], o[7]}); }
  ST_END(4);
}

constexpr int K5_S = 6, K5_LDS = K5_S * GA_STAGE;
__global__ __launch_bounds__(512, 2) void k_ffn2(const char* __restrict__ gb, const char* __restrict__ w2, const char* __restrict__ x1b,
                                                 const float* __restrict__ modf, char* __restrict__ out) {
  const lptr sm = (lptr)smem_raw;
  ST_INIT();
  const int b = blockIdx.x, l = threadIdx.x & 63, w = __builtin_amdgcn_readfirstlane(threadIdx.x >> 6);
  const int xcd = b & 7, j = b >> 3, tb = (xcd >> 1) * 4 + (j & 3), nt = (xcd & 1) * 8 + (j >> 2);
  if (w >= 4) { gemmA_load<56, K5_S, DM, 0>(gb + (size_t)tb * 4 * 224 * 1024, w2 + nt * 128, l, w - 4, [] {}); return; }
  const int hi = l >> 5, tb32 = tb * 4 + 2 * (w & 1), n0 = nt * 64 + (w >> 1) * 32;
  u32x4 xr[2][2]; f32x4 g[4];
#pragma unroll
  for (int i = 0; i < 2; ++i)
#pragma unroll
    for (int h = 0; h < 2; ++h) xr[i][h] = *(const u32x4*)(x1b + (size_t)((tb32 + i) * 64 + (n0 >> 4) + h) * 1024 + l * 16);
#pragma unroll
  for (int q = 0; q < 4; ++q) g[q] = *(const f32x4*)(modf + 5 * DM + n0 + 8 * q + 4 * hi);
  f32x16 acc[2]; gemmA_compute<56, K5_S>(sm, l, w, acc[0], acc[1], S_);
#pragma unroll
  for (int i = 0; i < 2; ++i)
#pragma unroll
    for (int h = 0; h < 2; ++h) { u32x4 o;
#pragma unroll
      for (int e = 0; e < 4; ++e) { const int r = 8 * h + 2 * e, q = r >> 2, c = r & 3; o[e] = pk(blo(xr[i][h][e]) + g[q][c] * acc[i][r], bhi(xr[i][h][e]) + g[q][c + 1] * acc[i][r + 1]); }
      gstore(out + (size_t)((tb32 + i) * 32 + (l & 31)) * 2048 + (n0 + 16 * h + 8 * hi) * 2, rowswap(o)); }
  ST_END(5);
}

extern "C" void kernel_launch(void* const* d_in, const int* in_sizes, int n_in, void* d_out, int out_size, void* d_ws, size_t ws_size, hipStream_t stream) {
  const char* x = (const char*)d_in[0]; const char* wq = (const char*)d_in[1]; const char* wk = (const char*)d_in[2]; const char* wv = (const char*)d_in[3];
  const char* wo = (const char*)d_in[4]; const char* w1 = (const char*)d_in[6]; const char* w2 = (const char*)d_in[7];
  const uint16_t* c = (const uint16_t*)d_in[8]; const char* wmod = (const char*)d_in[9];
  char* ws = (char*)d_ws;
  (void)hipFuncSetAttribute((const void*)k_ln1kv, hipFuncAttributeMaxDynamicSharedMemorySize, K1_LDS);
  (void)hipFuncSetAttribute((const void*)k_qattn, hipFuncAttributeMaxDynamicSharedMemorySize, K2_LDS);
  (void)hipFuncSetAttribute((const void*)k_oproj, hipFuncAttributeMaxDynamicSharedMemorySize, K3_LDS);
  (void)hipFuncSetAttribute((const void*)k_ffn1, hipFuncAttributeMaxDynamicSharedMemorySize, K4_LDS);
  (void)hipFuncSetAttribute((const void*)k_ffn2, hipFuncAttributeMaxDynamicSharedMemorySize, K5_LDS);
  float* modp = (float*)(ws + WS_MODP); float* modf = (float*)(ws + WS_MODF);
  k_mod<<<1024, 256, 0, stream>>>(c, wmod, modp, x, wq, wk, wv);
  k_ln1kv<<<256, 512, K1_LDS, stream>>>(x, wk, wv, (const char*)modp, modf, (uint16_t*)(ws + WS_MODH), ws + WS_H, ws + WS_KV);
  k_qattn<<<256, 512, K2_LDS, stream>>>(ws + WS_H, wq, ws + WS_KV, ws + WS_O, w1, ws + WS_MODH, (float*)(ws + WS_UTP), wo, w2);
  k_oproj<<<256, 512, K3_LDS, stream>>>(ws + WS_O, wo, x, modf, ws + WS_X1, ws + WS_Y, (f32x2*)(ws + WS_STAT));
  k_ffn1<<<256, 512, K4_LDS, stream>>>(ws + WS_Y, w1, (const f32x2*)(ws + WS_STAT), (const float*)(ws + WS_UTP), ws + WS_G);
  k_ffn2<<<256, 512, K5_LDS, stream>>>(ws + WS_G, w2, ws + WS_X1, modf, (char*)d_out);
}
```

```cpp
#include <hip/hip_runtime.h>
#include <hip/hip_bf16.h>
#include <stdint.h>

typedef __attribute__((ext_vector_type(2))) float f32x2;
typedef __attribute__((ext_vector_type(4))) float f32x4;
typedef __attribute__((ext_vector_type(16))) float f32x16;
typedef __attribute__((ext_vector_type(2))) __bf16 bf16x2;
typedef __attribute__((ext_vector_type(8))) __bf16 bf16x8;
typedef __attribute__((ext_vector_type(4))) short s16x4;
typedef __attribute__((ext_vector_type(2))) unsigned u32x2;
typedef __attribute__((ext_vector_type(4))) unsigned u32x4;
#define LDS __attribute__((address_space(3)))
typedef LDS char* lptr;
#define PF0 768
#define CODEPF 1
#define DEV __device__ __forceinline__

constexpr int SEQ = 2048, DM = 1024, FF = 3584, NMOD = 6 * DM;
constexpr size_t WS_MODP = 0;
constexpr size_t WS_UTP = 98304;
constexpr size_t WS_MODF = 212992;
constexpr size_t WS_MODH = 237568;
constexpr size_t WS_STAT = 262144;
constexpr size_t WS_KV = 786432;
constexpr size_t WS_H = (size_t)1 << 20;
constexpr size_t WS_O = (size_t)5 << 20;
constexpr size_t WS_X1 = (size_t)9 << 20;
constexpr size_t WS_Y = (size_t)13 << 20;
constexpr size_t WS_G = (size_t)17 << 20;

DEV unsigned pk(float a, float b) { return __builtin_bit_cast(unsigned, __builtin_convertvector((f32x2){a, b}, bf16x2)); }
DEV float rbf(float a) { return __uint_as_float(pk(a, 0.f) << 16); }
DEV float blo(unsigned u) { return __uint_as_float(u << 16); }
DEV float bhi(unsigned u) { return __uint_as_float(u & 0xffff0000u); }
DEV float silu(float z) { return z * __builtin_amdgcn_rcpf(1.f + __builtin_amdgcn_exp2f(-1.4426950408889634f * z)); }
DEV f32x16 mfma32(bf16x8 a, bf16x8 b, f32x16 c) { return __builtin_amdgcn_mfma_f32_32x32x16_bf16(a, b, c, 0, 0, 0); }
DEV f32x4 mfma16(bf16x8 a, bf16x8 b, f32x4 c) { return __builtin_amdgcn_mfma_f32_16x16x32_bf16(a, b, c, 0, 0, 0); }
DEV bf16x8 asb(u32x4 v) { return __builtin_bit_cast(bf16x8, v); }
DEV bf16x8 ldsA(lptr p) { return *(const LDS bf16x8*)p; }
DEV bf16x8 ldsT(lptr p, int second) {
  s16x4 a = __builtin_amdgcn_ds_read_tr16_b64_v4i16((LDS s16x4*)p);
  s16x4 b = __builtin_amdgcn_ds_read_tr16_b64_v4i16((LDS s16x4*)(p + second));
  return __builtin_bit_cast(bf16x8, __builtin_shufflevector(a, b, 0, 1, 2, 3, 4, 5, 6, 7));
}
template <int C> DEV float dppadd(float v) { return v + __int_as_float(__builtin_amdgcn_update_dpp(0, __float_as_int(v), C, 0xf, 0xf, true)); }
DEV float swap32add(float v) { auto r = __builtin_amdgcn_permlane32_swap(__float_as_uint(v), __float_as_uint(v), false, false); return __uint_as_float(r[0]) + __uint_as_float(r[1]); }
DEV float swap16add(float v) { auto r = __builtin_amdgcn_permlane16_swap(__float_as_uint(v), __float_as_uint(v), false, false); return __uint_as_float(r[0]) + __uint_as_float(r[1]); }
DEV float swap32max(float v) { auto r = __builtin_amdgcn_permlane32_swap(__float_as_uint(v), __float_as_uint(v), false, false); return __builtin_fmaxf(__uint_as_float(r[0]), __uint_as_float(r[1])); }
DEV float wavesum(float v) { v = dppadd<0xB1>(v); v = dppadd<0x4E>(v); v = dppadd<0x141>(v); v = dppadd<0x140>(v); v = swap16add(v); return swap32add(v); }

#define SETM0(x) asm volatile("s_mov_b32 m0, %0\n\ts_nop 0" ::"s"(x) : "memory")
template <int OFF> DEV void glds(unsigned voff, const void* base) { asm volatile("global_load_lds_dwordx4 %0, %1 offset:%2" ::"v"(voff), "s"(base), "i"(OFF) : "memory"); }
template <int N> DEV void waitbar() { asm volatile("s_waitcnt vmcnt(%0) lgkmcnt(0)\n\ts_barrier" ::"i"(N) : "memory"); }
template <int OFF> DEV u32x4 gload(unsigned voff, const void* base) { u32x4 r; asm volatile("global_load_dwordx4 %0, %1, %2 offset:%3" : "=v"(r) : "v"(voff), "s"(base), "i"(OFF) : "memory"); return r; }
template <int OFF> DEV u32x2 gload2(unsigned voff, const void* base) { u32x2 r; asm volatile("global_load_dwordx2 %0, %1, %2 offset:%3" : "=v"(r) : "v"(voff), "s"(base), "i"(OFF) : "memory"); return r; }
#define STPOL "sc1"
DEV void gstore(void* p, u32x4 v) { asm volatile("global_store_dwordx4 %0, %1, off " STPOL "\n\ts_nop 1" ::"v"(p), "v"(v) : "memory"); }
#define SB() __builtin_amdgcn_sched_barrier(0)
extern __shared__ __attribute__((aligned(16))) char smem_raw[];

struct Stamps {};
#define ST_INIT() Stamps S_
#define ST(i)
#define ST_END(k)

extern "C" __global__ __launch_bounds__(256) void k_mod(const uint16_t* __restrict__ c, const char* __restrict__ wmod, float* __restrict__ modp,
                                             const char* __restrict__ x, const char* __restrict__ wq, const char* __restrict__ wk, const char* __restrict__ wv) {
  __shared__ float red[4][32];
  ST_INIT();
  const int t = threadIdx.x;
  if (blockIdx.x >= PF0 && blockIdx.x < PF0 + 256) { const int p = blockIdx.x - PF0; u32x4 d[7];
    const char* xb = x + (size_t)((p & 7) * 32 + (p >> 3)) * 16384;
#pragma unroll
    for (int i = 0; i < 4; ++i) d[i] = gload<0>(t * 16, xb + i * 4096);
    d[4] = gload<0>(t * 16, wq + p * 8192); d[5] = gload<0>(t * 16, wq + p * 8192 + 4096);
    d[6] = gload<0>((t & 31) * 16, (p & 128 ? wv : wk) + (p & 127) * 512);
    { unsigned long long pc; unsigned sz;
      asm volatile("s_getpc_b64 %0\n.Lpf%=:\n\ts_mov_b32 %1, k_ffn2-.Lpf%=+2048" : "=s"(pc), "=s"(sz)); pc &= ~15ull;
      const unsigned o = (p >> 3) * 2048 + t * 16; u32x4 e = gload<0>(t < 128 && o < sz ? o : 0, (const void*)pc);
      asm volatile("s_waitcnt vmcnt(0)" : "+v"(e)::"memory"); }
    asm volatile("s_waitcnt vmcnt(0)" : "+v"(d[0]), "+v"(d[1]), "+v"(d[2]), "+v"(d[3]), "+v"(d[4]), "+v"(d[5]), "+v"(d[6])::"memory");
    return; }
  const int b = blockIdx.x < PF0 ? blockIdx.x : blockIdx.x - 256, xcd = b & 7, j = b >> 3;
  const int cg = (j & 1) + 2 * (xcd + 8 * ((j >> 1) % 12)), ks = j / 24;
  const int row0 = ks * 256 + (t >> 2);
  const char* p = wmod + (size_t)row0 * (NMOD * 2) + cg * 64 + (t & 3) * 16;
  u32x4 w[4]; float s[4];
#pragma unroll
  for (int i = 0; i < 4; ++i) { w[i] = *(const u32x4*)(p + (size_t)i * 64 * NMOD * 2); s[i] = __uint_as_float((unsigned)c[row0 + 64 * i] << 16); }
  float a[8] = {0, 0, 0, 0, 0, 0, 0, 0};
#pragma unroll
  for (int i = 0; i < 4; ++i) { const float f = rbf(silu(s[i]));
#pragma unroll
    for (int e = 0; e < 4; ++e) { a[2 * e] += f * blo(w[i][e]); a[2 * e + 1] += f * bhi(w[i][e]); } }
#pragma unroll
  for (int e = 0; e < 8; ++e) { float v = a[e]; v += __shfl_xor(v, 4); v += __shfl_xor(v, 8); v = swap16add(v); a[e] = swap32add(v); }
  if ((t & 63) < 4) {
#pragma unroll
    for (int e = 0; e < 8; ++e) red[t >> 6][(t & 3) * 8 + e] = a[e]; }
  __syncthreads();
  if (t < 32) modp[ks * NMOD + cg * 32 + t] = (red[0][t] + red[1][t]) + (red[2][t] + red[3][t]);
  ST_END(0);
}

constexpr int K1_W = 0, K1_H = 65536, K1_MV = K1_H + 64 * 528, K1_RED = K1_MV + 8192, K1_LDS = K1_RED + 32768;
extern "C" __global__ __launch_bounds__(512, 2) void k_ln1kv(const char* __restrict__ x, const char* __restrict__ wk, const char* __restrict__ wv,
                                                  const char* __restrict__ modp, float* __restrict__ modf, uint16_t* __restrict__ modh,
                                                  char* __restrict__ hbuf, char* __restrict__ kv) {
  const lptr sm = (lptr)smem_raw;
  ST_INIT();
  const int b = blockIdx.x, t = threadIdx.x, l = t & 63, w = __builtin_amdgcn_readfirstlane(t >> 6);
  const int which = (b >> 3) & 1, rb = (b & 7) * 16 + (b >> 4);
  u32x4 xv[4], mp[4];
  { const char* xb = x + (size_t)(rb * 16 + 2 * w) * 2048; const unsigned vo = l * 32;
    xv[0] = gload<0>(vo, xb); xv[1] = gload<16>(vo, xb); xv[2] = gload<2048>(vo, xb); xv[3] = gload<2064>(vo, xb);
#pragma unroll
    for (int s = 0; s < 4; ++s) mp[s] = gload<0>(t * 16, modp + s * NMOD * 4); }
  { const char* wsrc = (which ? wv : wk) + w * 8192;
#pragma unroll
    for (int g = 0; g < 2; ++g) { SETM0(K1_W + w * 8192 + g * 4096); const char* s = wsrc + g * 4096;
      glds<0>(l * 16, s); glds<1024>(l * 16, s); glds<2048>(l * 16, s); glds<3072>(l * 16, s); } }
  asm volatile("s_waitcnt vmcnt(8)" : "+v"(xv[0]), "+v"(xv[1]), "+v"(xv[2]), "+v"(xv[3]), "+v"(mp[0]), "+v"(mp[1]), "+v"(mp[2]), "+v"(mp[3])::"memory");
  ST(1);
  { f32x4 o;
#pragma unroll
    for (int e = 0; e < 4; ++e) { const float v = rbf((__uint_as_float(mp[0][e]) + __uint_as_float(mp[1][e])) + (__uint_as_float(mp[2][e]) + __uint_as_float(mp[3][e])));
      o[e] = t >= 256 ? rbf(1.f + v) : v; }
    *(LDS f32x4*)(sm + K1_MV + (t & 256) * 16 + (t & 3) * 1024 + ((t & 255) >> 2) * 16) = o; }
  f32x2 v[2][8]; float rs[2];
#pragma unroll
  for (int r = 0; r < 2; ++r) { f32x2 s = {0.f, 0.f};
#pragma unroll
    for (int e = 0; e < 8; ++e) { const unsigned u = xv[2 * r + (e >> 2)][e & 3]; v[r][e] = (f32x2){blo(u), bhi(u)}; s += v[r][e]; }
    const float m = wavesum(s[0] + s[1]) * (1.f / 1024.f); f32x2 q = {0.f, 0.f};
#pragma unroll
    for (int e = 0; e < 8; ++e) { v[r][e] -= m; q += v[r][e] * v[r][e]; }
    rs[r] = __builtin_amdgcn_rsqf(wavesum(q[0] + q[1]) * (1.f / 1024.f) + 1e-5f); }
  asm volatile("s_waitcnt lgkmcnt(0)\n\ts_barrier" ::: "memory");
  { unsigned u[2][8];
#pragma unroll
    for (int e = 0; e < 4; ++e) { const f32x4 a = *(const LDS f32x4*)(sm + K1_MV + e * 1024 + l * 16), cc = *(const LDS f32x4*)(sm + K1_MV + 4096 + e * 1024 + l * 16);
#pragma unroll
      for (int r = 0; r < 2; ++r)
#pragma unroll
        for (int i = 0; i < 2; ++i) { const f32x2 h = v[r][2 * e + i] * rs[r] * (f32x2){cc[2 * i], cc[2 * i + 1]} + (f32x2){a[2 * i], a[2 * i + 1]}; u[r][2 * e + i] = pk(h[0], h[1]); } }
#pragma unroll
    for (int r = 0; r < 2; ++r) { const lptr d = sm + K1_H + l * 528 + (2 * w + r) * 16;
      *(LDS u32x4*)d = (u32x4){u[r][0], u[r][1], u[r][4], u[r][5]}; *(LDS u32x4*)(d + 256) = (u32x4){u[r][2], u[r][3], u[r][6], u[r][7]}; } }
  ST(2); waitbar<0>(); ST(3);
  f32x16 acc = {};
  { const int hi = l >> 5; const lptr hp = sm + K1_H + w * 8 * 528 + hi * 256 + (l & 15) * 16;
    const lptr wp = sm + K1_W + (w * 128 + 4 * hi + ((l >> 2) & 3)) * 64 + ((l >> 4) & 1) * 32 + (l & 3) * 8;
    bf16x8 fa[8], fb[8];
#pragma unroll
    for (int i = 0; i < 8; ++i) { fa[i] = ldsA(hp + i * 528); fb[i] = ldsT(wp + i * 1024, 512); }
#pragma unroll
    for (int i = 0; i < 2; ++i) { const int id = t + 512 * i, tok = id & 15, h2 = (id >> 4) & 1, kb = which * 32 + (id >> 5);
      gstore(hbuf + (size_t)((rb >> 1) * 64 + kb) * 1024 + (h2 * 32 + (rb & 1) * 16 + tok) * 16, *(const LDS u32x4*)(sm + K1_H + kb * 528 + h2 * 256 + tok * 16)); }
    if (which == 0) {
#pragma unroll
      for (int i = 0; i < 8; ++i) acc = mfma32(fb[i], fa[i], acc);
    } else {
#pragma unroll
      for (int i = 0; i < 8; ++i) acc = mfma32(fa[i], fb[i], acc);
    } }
#pragma unroll
  for (int q = 0; q < 4; ++q) *(LDS f32x4*)(sm + K1_RED + w * 4096 + q * 1024 + l * 16) = (f32x4){acc[4 * q], acc[4 * q + 1], acc[4 * q + 2], acc[4 * q + 3]};
  asm volatile("s_waitcnt lgkmcnt(0)\n\ts_barrier" ::: "memory");
  if (w == 0) {
    const lptr rp = sm + K1_RED + (which ? l * 16 : (l >> 5) * 2048 + (((l >> 4) & 1) * 32 + (l & 15)) * 16);
    f32x4 a = {}, c = {};
#pragma unroll
    for (int u = 0; u < 8; ++u) { a += *(const LDS f32x4*)(rp + u * 4096); c += *(const LDS f32x4*)(rp + u * 4096 + 1024); }
    char* base = kv + (rb >> 1) * 4096;
    char* d = which ? base + 2048 + (rb & 1) * 1024 + l * 16 : base + (l >> 5) * 1024 + (((l >> 4) & 1) * 32 + (rb & 1) * 16 + (l & 15)) * 16;
    *(u32x4*)d = (u32x4){pk(a[0], a[1]), pk(a[2], a[3]), pk(c[0], c[1]), pk(c[2], c[3])};
  } else if (b < 64 && w == 1) {
    const int col = 2048 + b * 64 + l; const float* mpf = (const float*)modp;
    float v2 = rbf((mpf[col] + mpf[NMOD + col]) + (mpf[2 * NMOD + col] + mpf[3 * NMOD + col]));
    if ((col >> 10) == 4) v2 = rbf(1.f + v2);
    modf[col] = v2; modh[col] = (uint16_t)(__float_as_uint(v2) >> 16);
  }
  ST_END(1);
}

DEV void bar() { asm volatile("s_waitcnt lgkmcnt(0)\n\ts_barrier" ::: "memory"); }
template <int N, int G, int EXTRA> DEV void loader_tail() { waitbar<G * N + EXTRA>(); if constexpr (N > 0) loader_tail<N - 1, G, EXTRA>(); }
constexpr int issued(int t, int S) { return t == 0 ? 2 : (2 + 2 * t < S + t - 1 ? 2 + 2 * t : S + t - 1); }
template <int T, int S, int G, int STAGE, class I> DEV void loader_ramp(I&& issue) {
#pragma unroll
  for (int s = T == 0 ? 0 : issued(T - 1, S); s < issued(T, S); ++s) issue(s, (s % S) * STAGE);
  waitbar<G*(issued(T, S) - T - 1)>();
  if constexpr (issued(T, S) < S + T - 1 || T == 0) loader_ramp<T + 1, S, G, STAGE>(issue);
}
constexpr int ramp_end(int S) { int t = 1; while (issued(t, S) < S + t - 1) ++t; return t; }
template <int KT, int S, int G, int STAGE, int EXTRA, class I, class X>
DEV void loader(I&& issue, X&& extra) {
  loader_ramp<0, S, G, STAGE>(issue);
  constexpr int T0 = ramp_end(S);
  int slot = ((S + T0 - 1) % S) * STAGE;
  for (int t = T0; t <= KT - S; ++t) { issue(t - 1 + S, slot); slot = slot == (S - 1) * STAGE ? 0 : slot + STAGE; if (t < KT - S) waitbar<G*(S - 2)>(); }
  extra();
  loader_tail<S - 2, G, EXTRA>();
}
constexpr int GA_STAGE = 24576;
template <int KT, int S, int LDW, int EXTRA, class X>
DEV void gemmA_load(const char* act, const char* wgt, int l, int lw, X&& extra) {
  const char* ap = act + (size_t)lw * (KT * 4096);
  const char* wp = wgt + (size_t)lw * (16 * LDW * 2);
  const unsigned va = l * 16, vb = (l >> 3) * (LDW * 2) + (((l & 7) ^ (((l >> 4) & 1) << 2)) * 16);
  loader<KT, S, 6, GA_STAGE, EXTRA>([&](int t, int slot) {
    SETM0(slot + lw * 4096); const char* a = ap + (size_t)t * 4096; glds<0>(va, a); glds<1024>(va, a); glds<2048>(va, a); glds<3072>(va, a);
    SETM0(slot + 16384 + lw * 2048); const char* s = wp + (size_t)t * (64 * LDW * 2); glds<0>(vb, s); glds<1024>(vb, s + 8 * LDW * 2 - 1024); }, extra);
}
struct FragA { bf16x8 a0[4], a1[4], b[4]; };
template <int KT, int S>
DEV void gemmA_compute(const lptr sm, int l, int w, f32x16& c0, f32x16& c1, Stamps& S_) {
  const int q = (l >> 2) & 3, p = l & 3;
  const lptr ra = sm + (w & 1) * 8192 + l * 16;
  const lptr rb = sm + 16384 + (4 * (l >> 5) + q) * 128 + ((((w >> 1) * 4 + ((l >> 4) & 1) * 2 + (p >> 1)) ^ ((q >> 1) << 2)) * 16) + (p & 1) * 8;
  c0 = (f32x16){}; c1 = (f32x16){};
  FragA f, g; int slot = 0;
  auto rd = [&](FragA& d, int k) { d.a0[k] = ldsA(ra + slot + k * 1024); d.b[k] = ldsT(rb + slot + k * 2048, 1024); d.a1[k] = ldsA(ra + slot + 4096 + k * 1024); };
  auto step = [&](FragA& cur, FragA& nxt) { bar(); slot = slot == (S - 1) * GA_STAGE ? 0 : slot + GA_STAGE;
#pragma unroll
    for (int k = 0; k < 4; ++k) { if (k < 2) rd(nxt, 2 * k); c0 = mfma32(cur.b[k], cur.a0[k], c0); SB(); if (k < 2) rd(nxt, 2 * k + 1); c1 = mfma32(cur.b[k], cur.a1[k], c1); SB(); } };
  bar(); ST(2);
#pragma unroll
  for (int k = 0; k < 4; ++k) rd(f, k);
  for (int t = 0; t < KT - 2; t += 2) { step(f, g); step(g, f); }
  step(f, g);
#pragma unroll
  for (int k = 0; k < 4; ++k) { c0 = mfma32(g.b[k], g.a0[k], c0); c1 = mfma32(g.b[k], g.a1[k], c1); }
  ST(3);
}

constexpr int K2_KV0 = 98304, K2_KV1 = 131072, K2_KV2 = 0, K2_UW = 32768, K2_Q = 65536, K2_RED = 73728, K2_DUMP = 81920, K2_LDS = 163840;
DEV void pack16(const f32x16& s, u32x4& a, u32x4& b) {
  a = (u32x4){pk(s[0], s[1]), pk(s[2], s[3]), pk(s[4], s[5]), pk(s[6], s[7])};
  b = (u32x4){pk(s[8], s[9]), pk(s[10], s[11]), pk(s[12], s[13]), pk(s[14], s[15])}; }
DEV float max16(const f32x16& s) { float m = s[0];
#pragma unroll
  for (int i = 1; i < 16; ++i) m = __builtin_fmaxf(m, s[i]);
  return swap32max(m); }
#define EX2(v) __builtin_amdgcn_exp2f(v)
#define PIN(x) asm volatile("" : "+v"(x))

extern "C" __global__ __launch_bounds__(512, 2) void k_qattn(const char* __restrict__ hbuf, const char* __restrict__ wq, const char* __restrict__ kv, char* __restrict__ obuf,
                                                  const char* __restrict__ w1, const char* __restrict__ modh, float* __restrict__ utp,
                                                  const char* __restrict__ wo, const char* __restrict__ w2) {
  const lptr sm = (lptr)smem_raw;
  ST_INIT();
  const int b = blockIdx.x, l = threadIdx.x & 63, w = __builtin_amdgcn_readfirstlane(threadIdx.x >> 6);
  const int xcd = b & 7, j = b >> 3, tb = (xcd >> 1) * 4 + (j & 3), hp = (xcd & 1) * 8 + (j >> 2);
  const unsigned l16 = l * 16;
  u32x4 q0u, q1u;
  if (w >= 4) {
    gemmA_load<16, 4, DM, 8>(hbuf + (size_t)tb * 4 * 64 * 1024, wq + hp * 128, l, w - 4, [&] {
#pragma unroll
      for (int g = 0; g < 2; ++g) { SETM0(K2_KV0 + (w - 4) * 8192 + g * 4096); const char* s = kv + (w - 4) * 8192 + g * 4096;
        glds<0>(l16, s); glds<1024>(l16, s); glds<2048>(l16, s); glds<3072>(l16, s); } });
    waitbar<0>();
    q0u = *(const LDS u32x4*)(sm + K2_Q + (w - 4) * 2048 + l16); q1u = *(const LDS u32x4*)(sm + K2_Q + (w - 4) * 2048 + 1024 + l16);
  } else {
    f32x16 c0, c1; gemmA_compute<16, 4>(sm, l, w, c0, c1, S_);
    constexpr float QS = 0.17677669529663687f * 1.4426950408889634f;
    c0 *= QS; c1 *= QS;
    u32x4 a, c; pack16(c1, a, c); *(LDS u32x4*)(sm + K2_Q + w * 2048 + l16) = a; *(LDS u32x4*)(sm + K2_Q + w * 2048 + 1024 + l16) = c;
    pack16(c0, q0u, q1u);
    bar();
  }
  const bf16x8 q0 = asb(q0u), q1 = asb(q1u);
  const char* kvw = kv + w * 4096;
  auto kvload = [&](int st, int slot) { SETM0(slot + w * 4096); const char* s = kvw + st * 32768; glds<0>(l16, s); glds<1024>(l16, s); glds<2048>(l16, s); glds<3072>(l16, s); };
  kvload(1, K2_KV1);
  const unsigned one2 = ((l & 7) == ((l >> 4) & 1) * 4) ? 0x3f803f80u : 0u;
  const bf16x8 ones = asb((u32x4){one2, one2, one2, one2});
  const lptr kp0 = sm + K2_KV0 + l16, kp1 = sm + K2_KV1 + l16, kp2 = sm + K2_KV2 + l16;
  bf16x8 k0 = ldsA(kp0), k1 = ldsA(kp0 + 1024), v0 = ldsA(kp0 + 2048), v1 = ldsA(kp0 + 3072);
  f32x16 negm, sA, sB, o = {}; f32x4 ls = {};
  { f32x16 z = {}; z = mfma32(k0, q0, z); z = mfma32(k1, q1, z); const float m = -max16(z);
#pragma unroll
    for (int i = 0; i < 16; ++i) negm[i] = m; }
  PIN(negm);
  sA = mfma32(k0, q0, negm); sA = mfma32(k1, q1, sA);
  k0 = ldsA(kp0 + 4096); k1 = ldsA(kp0 + 4096 + 1024);
  u32x4 p0, p1 = {0, 0, 0, 0}; bf16x8 v1p = v1;
#define TILE(S, N, KP, VP)                                                                                            \
  { N = mfma32(k0, q0, negm); SB();                                                                                  \
    S[0] = EX2(S[0]); S[1] = EX2(S[1]); S[2] = EX2(S[2]); S[3] = EX2(S[3]); p0[0] = pk(S[0], S[1]); p0[1] = pk(S[2], S[3]); PIN(p0); SB(); \
    o = mfma32(v1p, asb(p1), o); k0 = ldsA(KP); SB();                                                                \
    S[4] = EX2(S[4]); S[5] = EX2(S[5]); S[6] = EX2(S[6]); S[7] = EX2(S[7]); p0[2] = pk(S[4], S[5]); p0[3] = pk(S[6], S[7]); PIN(p0); SB(); \
    ls = mfma16(ones, asb(p1), ls); v1p = v1; v1 = ldsA((VP) + 1024); SB();                                          \
    S[8] = EX2(S[8]); S[9] = EX2(S[9]); PIN(S); SB();                                                                \
    N = mfma32(k1, q1, N); SB();                                                                                     \
    S[10] = EX2(S[10]); S[11] = EX2(S[11]); S[12] = EX2(S[12]); S[13] = EX2(S[13]); p1[0] = pk(S[8], S[9]); p1[1] = pk(S[10], S[11]); PIN(p1); PIN(S); SB(); \
    o = mfma32(v0, asb(p0), o); k1 = ldsA((KP) + 1024); SB();                                                        \
    S[14] = EX2(S[14]); S[15] = EX2(S[15]); p1[2] = pk(S[12], S[13]); p1[3] = pk(S[14], S[15]); PIN(p1); SB();       \
    ls = mfma16(ones, asb(p0), ls); v0 = ldsA(VP); SB(); }
#define STAGE(P, Q, ...)                                                                                              \
  TILE(sA, sB, P + 2 * 4096, P + 1 * 4096 + 2048) TILE(sB, sA, P + 3 * 4096, P + 2 * 4096 + 2048)                     \
  TILE(sA, sB, P + 4 * 4096, P + 3 * 4096 + 2048) TILE(sB, sA, P + 5 * 4096, P + 4 * 4096 + 2048)                     \
  TILE(sA, sB, P + 6 * 4096, P + 5 * 4096 + 2048) TILE(sB, sA, P + 7 * 4096, P + 6 * 4096 + 2048)                     \
  { __VA_ARGS__ }                                                                                                     \
  TILE(sA, sB, Q, P + 7 * 4096 + 2048) TILE(sB, sA, Q + 4096, Q + 2048)
  const int ub = b < 224 ? b : b - 224, cg = ub % 56, ks = ub / 56;
  u32x2 vf[4];
  ST(4);
  STAGE(kp0, kp1, waitbar<0>(); kvload(2, K2_KV2);
        { SETM0(K2_UW + w * 4096); const char* s = w1 + (size_t)(ks * 256 + w * 32) * (FF * 2) + cg * 128;
          const unsigned vo = (l >> 3) * (FF * 2) + (((l & 7) ^ (((l >> 4) & 1) << 2)) * 16);
          glds<0>(vo, s); glds<1024>(vo, s + 8 * FF * 2 - 1024); glds<2048>(vo, s + 16 * FF * 2 - 2048); glds<3072>(vo, s + 24 * FF * 2 - 3072);
          const char* mv = modh + (ks * 256 + w * 32) * 2; const unsigned vm = ((4 - (l & 1)) * DM + 4 * (l >> 5)) * 2;
          vf[0] = gload2<0>(vm, mv); vf[1] = gload2<16>(vm, mv); vf[2] = gload2<32>(vm, mv); vf[3] = gload2<48>(vm, mv); })
  STAGE(kp1, kp2, waitbar<8>(); kvload(3, K2_KV0);)
  STAGE(kp2, kp0, waitbar<0>();
        if (b < 224) { PIN(vf[0]); PIN(vf[1]); PIN(vf[2]); PIN(vf[3]); const int q = (l >> 2) & 3; const int p = l & 3; f32x16 d0 = {}; f32x16 d1 = {};
          const lptr rb = sm + K2_UW + (w * 32 + 4 * (l >> 5) + q) * 128 + (((((l >> 4) & 1) * 2 + (p >> 1)) ^ ((q >> 1) << 2)) * 16) + (p & 1) * 8;
          const lptr rc = sm + K2_UW + (w * 32 + 4 * (l >> 5) + q) * 128 + (((4 + ((l >> 4) & 1) * 2 + (p >> 1)) ^ ((q >> 1) << 2)) * 16) + (p & 1) * 8;
          _Pragma("unroll") for (int kk = 0; kk < 2; ++kk) { const bf16x8 vv = asb((u32x4){vf[2 * kk][0], vf[2 * kk][1], vf[2 * kk + 1][0], vf[2 * kk + 1][1]});
            d0 = mfma32(ldsT(rb + kk * 2048, 1024), vv, d0); d1 = mfma32(ldsT(rc + kk * 2048, 1024), vv, d1); }
          if ((l & 31) < 2) { const lptr d = sm + K2_RED + w * 512 + (l & 31) * 256 + (l >> 5) * 16;
            _Pragma("unroll") for (int i = 0; i < 4; ++i) { *(LDS f32x4*)(d + i * 32) = (f32x4){d0[4 * i], d0[4 * i + 1], d0[4 * i + 2], d0[4 * i + 3]};
              *(LDS f32x4*)(d + 128 + i * 32) = (f32x4){d1[4 * i], d1[4 * i + 1], d1[4 * i + 2], d1[4 * i + 3]}; } } }
        kvload(4, K2_KV1);
        { SETM0(K2_DUMP + w * 1024); glds<0>(l16, wo + b * 8192 + w * 1024); glds<0>(l16, w2 + (size_t)b * 28672 + w * 1024); })
  STAGE(kp0, kp1, waitbar<2>();
        if (b < 224 && w < 2) { float v = 0.f;
          _Pragma("unroll") for (int u = 0; u < 8; ++u) v += *(const LDS float*)(sm + K2_RED + u * 512 + w * 256 + l * 4);
          utp[(ks * 2 + w) * FF + cg * 64 + l] = v; }
        kvload(5, K2_KV2);
        { SETM0(K2_DUMP + w * 1024); glds<0>(l16, w2 + (size_t)b * 28672 + 8192 + w * 1024); glds<0>(l16, w2 + (size_t)b * 28672 + 16384 + w * 1024); })
  STAGE(kp1, kp2, waitbar<2>(); kvload(6, K2_KV0);
        { SETM0(K2_DUMP + w * 1024); glds<0>(l16, w2 + (size_t)b * 28672 + 24576 + (w & 3) * 1024); })
  STAGE(kp2, kp0, waitbar<1>(); kvload(7, K2_KV1);)
  STAGE(kp0, kp1, waitbar<0>();)
  STAGE(kp1, kp2, )
  ST(5);
  o = mfma32(v1p, asb(p1), o); ls = mfma16(ones, asb(p1), ls);
  float lsum = ls[0];
  { float chk = lsum;
#pragma unroll
    for (int i = 0; i < 16; ++i) chk = __builtin_fmaxf(chk, __builtin_fabsf(o[i]) * 0x1p-40f);
    bool bad = !(chk < 0x1p80f);
#pragma unroll
    for (int i = 0; i < 16; ++i) bad |= (o[i] != o[i]);
    bad |= (lsum != lsum);
    if (__any(bad)) {
      float m = -INFINITY; lsum = 0.f; o = (f32x16){};
      for (int t = 0; t < 64; ++t) { const bf16x8* g = (const bf16x8*)(kv + t * 4096) + l;
        f32x16 s = {}; s = mfma32(g[0], q0, s); s = mfma32(g[64], q1, s);
        const float mn = __builtin_fmaxf(m, max16(s)), al = EX2(m - mn); m = mn; float rsum = 0.f;
#pragma unroll
        for (int i = 0; i < 16; ++i) { s[i] = rbf(EX2(s[i] - mn)); rsum += s[i]; o[i] *= al; }
        lsum = lsum * al + swap32add(rsum);
        u32x4 a, c; pack16(s, a, c); o = mfma32(g[128], asb(a), o); o = mfma32(g[192], asb(c), o); } } }
  o *= 1.f / lsum;
  u32x4 o0, o1; pack16(o, o0, o1);
  char* d = obuf + (size_t)((tb * 4 + 2 * (w & 1) + (w >> 2)) * 64 + (hp * 2 + ((w >> 1) & 1)) * 2) * 1024 + l16;
  gstore(d, o0); gstore(d + 1024, o1);
  ST_END(2);
}

DEV u32x4 rowswap(u32x4 v) { auto a = __builtin_amdgcn_permlane32_swap(v[0], v[2], false, false); auto c = __builtin_amdgcn_permlane32_swap(v[1], v[3], false, false);
  return (u32x4){a[0], c[0], a[1], c[1]}; }

constexpr int K3_S = 6, K3_LDS = K3_S * GA_STAGE;
extern "C" __global__ __launch_bounds__(512, 2) void k_oproj(const char* __restrict__ obuf, const char* __restrict__ wo, const char* __restrict__ x, const float* __restrict__ modf,
                                                  char* __restrict__ x1b, char* __restrict__ yb, f32x2* __restrict__ stat) {
  const lptr sm = (lptr)smem_raw;
  ST_INIT();
  const int b = blockIdx.x, l = threadIdx.x & 63, w = __builtin_amdgcn_readfirstlane(threadIdx.x >> 6);
  const int xcd = b & 7, j = b >> 3, tb = (xcd >> 1) * 4 + (j & 3), nt = (xcd & 1) * 8 + (j >> 2);
  if (w >= 4) { gemmA_load<16, K3_S, DM, 0>(obuf + (size_t)tb * 4 * 64 * 1024, wo + nt * 128, l, w - 4, [] {}); return; }
  const int hi = l >> 5, tb32 = tb * 4 + 2 * (w & 1), n0 = nt * 64 + (w >> 1) * 32;
  u32x4 xr[2][2]; f32x4 ga[4], cb[4];
#pragma unroll
  for (int i = 0; i < 2; ++i)
#pragma unroll
    for (int h = 0; h < 2; ++h) xr[i][h] = *(const u32x4*)(x + (size_t)((tb32 + i) * 32 + (l & 31)) * 2048 + (n0 + 16 * h + 8 * hi) * 2);
#pragma unroll
  for (int q = 0; q < 4; ++q) { ga[q] = *(const f32x4*)(modf + 2 * DM + n0 + 8 * q + 4 * hi); cb[q] = *(const f32x4*)(modf + 4 * DM + n0 + 8 * q + 4 * hi); }
  f32x16 acc[2]; gemmA_compute<16, K3_S>(sm, l, w, acc[0], acc[1], S_);
#pragma unroll
  for (int i = 0; i < 2; ++i) { unsigned xo[8], yo[8]; float x1[16], s = 0.f;
#pragma unroll
    for (int h = 0; h < 2; ++h) { const u32x4 xs = rowswap(xr[i][h]);
#pragma unroll
      for (int e = 0; e < 4; ++e) { const int r = 8 * h + 2 * e, q = r >> 2, c = r & 3;
        const unsigned u = pk(blo(xs[e]) + ga[q][c] * acc[i][r], bhi(xs[e]) + ga[q][c + 1] * acc[i][r + 1]);
        x1[r] = blo(u); x1[r + 1] = bhi(u); xo[4 * h + e] = u; yo[4 * h + e] = pk(x1[r] * cb[q][c], x1[r + 1] * cb[q][c + 1]); s += x1[r] + x1[r + 1]; } }
    const size_t off = (size_t)((tb32 + i) * 64 + (n0 >> 4)) * 1024 + l * 16;
    gstore(yb + off, (u32x4){yo[0], yo[1], yo[2], yo[3]}); gstore(yb + off + 1024, (u32x4){yo[4], yo[5], yo[6], yo[7]});
    gstore(x1b + off, (u32x4){xo[0], xo[1], xo[2], xo[3]}); gstore(x1b + off + 1024, (u32x4){xo[4], xo[5], xo[6], xo[7]});
    const float mean = swap32add(s) * (1.f / 32.f); float m2 = 0.f;
#pragma unroll
    for (int e = 0; e < 16; ++e) m2 += (x1[e] - mean) * (x1[e] - mean);
    m2 = swap32add(m2);
    if (hi == 0) stat[((tb32 + i) * 32 + (n0 >> 5)) * 32 + l] = (f32x2){mean, m2}; }
  ST_END(3);
}

constexpr int K4_STAGE = 45056, K4_UT = 3 * K4_STAGE, K4_LDS = K4_UT + 2048;
struct FragB { bf16x8 a, b[7]; };
extern "C" __global__ __launch_bounds__(512, 2) void k_ffn1(const char* __restrict__ yb, const char* __restrict__ w1, const f32x2* __restrict__ stat,
                                                 const float* __restrict__ utp, char* __restrict__ gb) {
  const lptr sm = (lptr)smem_raw;
  ST_INIT();
  const int b = blockIdx.x, t = threadIdx.x, l = t & 63, w = __builtin_amdgcn_readfirstlane(t >> 6);
  const int xcd = b & 7, j = b >> 3, tb = (xcd >> 2) * 8 + (j & 7), nt = (xcd & 3) * 4 + (j >> 3);
  if (w >= 4) { const int lw = w - 4;
    const char* ap = yb + (size_t)(tb * 4 + lw) * 64 * 1024; const char* wp = w1 + nt * 448;
    unsigned vb[7];
#pragma unroll
    for (int i = 0; i < 7; ++i) { const int c = (lw * 7 + i) * 64 + l, r = c / 28; vb[i] = r * (FF * 2) + (c - r * 28) * 16 + 3072 - i * 1024; }
    const unsigned va = l * 16;
    loader<16, 3, 11, K4_STAGE, 0>([&](int tt, int slot) {
      SETM0(slot + lw * 4096); const char* a = ap + (size_t)tt * 4096;
      glds<0>(va, a); glds<1024>(va, a); glds<2048>(va, a); glds<3072>(va, a);
      SETM0(slot + 16384 + lw * 7168 + 3072); const char* s = wp + (size_t)tt * (64 * FF * 2);
      glds<-3072>(vb[0], s); glds<-2048>(vb[1], s); glds<-1024>(vb[2], s); glds<0>(vb[3], s); glds<1024>(vb[4], s); glds<2048>(vb[5], s); glds<3072>(vb[6], s); }, [] {});
    return; }
  const int hi = l >> 5, tb32 = tb * 4 + w;
  constexpr float NL2E = -1.4426950408889634f;
  float rs, nm;
  { f32x2 st[16]; float up[8];
#pragma unroll
    for (int i = 0; i < 16; ++i) st[i] = stat[(tb32 * 32 + 2 * i + hi) * 32 + (l & 31)];
    const int col = nt * 224 + (t < 224 ? t : 0);
#pragma unroll
    for (int i = 0; i < 8; ++i) up[i] = utp[i * FF + col];
    float mean = 0.f;
#pragma unroll
    for (int i = 0; i < 16; ++i) mean += st[i][0];
    mean = swap32add(mean) * (1.f / 32.f); float m2 = 0.f;
#pragma unroll
    for (int i = 0; i < 16; ++i) m2 += st[i][1] + 32.f * (st[i][0] - mean) * (st[i][0] - mean);
    rs = NL2E * __builtin_amdgcn_rsqf(swap32add(m2) * (1.f / 1024.f) + 1e-5f); nm = -rs * mean;
    if (t < 224) { *(LDS float*)(sm + K4_UT + t * 4) = (up[0] + up[2]) + (up[4] + up[6]); *(LDS float*)(sm + K4_UT + 1024 + t * 4) = NL2E * ((up[1] + up[3]) + (up[5] + up[7])); } }
  ST(1);
  const lptr ra = sm + w * 4096 + l * 16;
  const lptr rb = sm + 16384 + (4 * hi + ((l >> 2) & 3)) * 448 + ((l >> 4) & 1) * 32 + (l & 3) * 8;
  f32x16 acc[7];
#pragma unroll
  for (int n = 0; n < 7; ++n) acc[n] = (f32x16){};
  FragB f, g; int slot = 0;
  auto kstep = [&](FragB& cur, FragB& nxt, int k) {
    nxt.a = ldsA(ra + slot + k * 1024);
#pragma unroll
    for (int n = 0; n < 7; ++n) { acc[n] = mfma32(cur.b[n], cur.a, acc[n]); nxt.b[n] = ldsT(rb + slot + k * 7168 + n * 64, 3584); SB(); } };
  bar(); ST(2);
  f.a = ldsA(ra);
#pragma unroll
  for (int n = 0; n < 7; ++n) f.b[n] = ldsT(rb + n * 64, 3584);
  for (int tt = 0; tt < 15; ++tt) { kstep(f, g, 1); kstep(g, f, 2); kstep(f, g, 3); bar(); slot = slot == 2 * K4_STAGE ? 0 : slot + K4_STAGE; kstep(g, f, 0); }
  kstep(f, g, 1); kstep(g, f, 2); kstep(f, g, 3);
#pragma unroll
  for (int n = 0; n < 7; ++n) acc[n] = mfma32(g.b[n], g.a, acc[n]);
  ST(3);
  char* d = gb + (size_t)(tb32 * 224 + nt * 14) * 1024 + l * 16;
#pragma unroll
  for (int n = 0; n < 7; ++n) { unsigned o[8];
#pragma unroll
    for (int q = 0; q < 4; ++q) { const f32x4 u = *(const LDS f32x4*)(sm + K4_UT + (n * 32 + 8 * q + 4 * hi) * 4), tv = *(const LDS f32x4*)(sm + K4_UT + 1024 + (n * 32 + 8 * q + 4 * hi) * 4);
#pragma unroll
      for (int i = 0; i < 2; ++i) { const f32x2 z = rs * (f32x2){acc[n][4 * q + 2 * i], acc[n][4 * q + 2 * i + 1]} + (nm * (f32x2){u[2 * i], u[2 * i + 1]} + (f32x2){tv[2 * i], tv[2 * i + 1]});
        const f32x2 dn = NL2E * (f32x2){EX2(z[0]), EX2(z[1])} + NL2E;
        const f32x2 r = z * (f32x2){__builtin_amdgcn_rcpf(dn[0]), __builtin_amdgcn_rcpf(dn[1])}; o[2 * q + i] = pk(r[0], r[1]); } }
    gstore(d + n * 2048, (u32x4){o[0], o[1], o[2], o[3]}); gstore(d + n * 2048 + 1024, (u32x4){o[4], o[5], o[6], o[7]}); }
  ST_END(4);
}

constexpr int K5_S = 6, K5_LDS = K5_S * GA_STAGE;
extern "C" __global__ __launch_bounds__(512, 2) void k_ffn2(const char* __restrict__ gb, const char* __restrict__ w2, const char* __restrict__ x1b,
                                                 const float* __restrict__ modf, char* __restrict__ out) {
  const lptr sm = (lptr)smem_raw;
  ST_INIT();
  const int b = blockIdx.x, l = threadIdx.x & 63, w = __builtin_amdgcn_readfirstlane(threadIdx.x >> 6);
  const int xcd = b & 7, j = b >> 3, tb = (xcd >> 1) * 4 + (j & 3), nt = (xcd & 1) * 8 + (j >> 2);
  if (w >= 4) { gemmA_load<56, K5_S, DM, 0>(gb + (size_t)tb * 4 * 224 * 1024, w2 + nt * 128, l, w - 4, [] {}); return; }
  const int hi = l >> 5, tb32 = tb * 4 + 2 * (w & 1), n0 = nt * 64 + (w >> 1) * 32;
  u32x4 xr[2][2]; f32x4 g[4];
#pragma unroll
  for (int i = 0; i < 2; ++i)
#pragma unroll
    for (int h = 0; h < 2; ++h) xr[i][h] = *(const u32x4*)(x1b + (size_t)((tb32 + i) * 64 + (n0 >> 4) + h) * 1024 + l * 16);
#pragma unroll
  for (int q = 0; q < 4; ++q) g[q] = *(const f32x4*)(modf + 5 * DM + n0 + 8 * q + 4 * hi);
  f32x16 acc[2]; gemmA_compute<56, K5_S>(sm, l, w, acc[0], acc[1], S_);
#pragma unroll
  for (int i = 0; i < 2; ++i)
#pragma unroll
    for (int h = 0; h < 2; ++h) { u32x4 o;
#pragma unroll
      for (int e = 0; e < 4; ++e) { const int r = 8 * h + 2 * e, q = r >> 2, c = r & 3; o[e] = pk(blo(xr[i][h][e]) + g[q][c] * acc[i][r], bhi(xr[i][h][e]) + g[q][c + 1] * acc[i][r + 1]); }
      gstore(out + (size_t)((tb32 + i) * 32 + (l & 31)) * 2048 + (n0 + 16 * h + 8 * hi) * 2, rowswap(o)); }
  ST_END(5);
}

extern "C" void kernel_launch(void* const* d_in, const int* in_sizes, int n_in, void* d_out, int out_size, void* d_ws, size_t ws_size, hipStream_t stream) {
  const char* x = (const char*)d_in[0]; const char* wq = (const char*)d_in[1]; const char* wk = (const char*)d_in[2]; const char* wv = (const char*)d_in[3];
  const char* wo = (const char*)d_in[4]; const char* w1 = (const char*)d_in[6]; const char* w2 = (const char*)d_in[7];
  const uint16_t* c = (const uint16_t*)d_in[8]; const char* wmod = (const char*)d_in[9];
  char* ws = (char*)d_ws;
  (void)hipFuncSetAttribute((const void*)k_ln1kv, hipFuncAttributeMaxDynamicSharedMemorySize, K1_LDS);
  (void)hipFuncSetAttribute((const void*)k_qattn, hipFuncAttributeMaxDynamicSharedMemorySize, K2_LDS);
  (void)hipFuncSetAttribute((const void*)k_oproj, hipFuncAttributeMaxDynamicSharedMemorySize, K3_LDS);
  (void)hipFuncSetAttribute((const void*)k_ffn1, hipFuncAttributeMaxDynamicSharedMemorySize, K4_LDS);
  (void)hipFuncSetAttribute((const void*)k_ffn2, hipFuncAttributeMaxDynamicSharedMemorySize, K5_LDS);
  float* modp = (float*)(ws + WS_MODP); float* modf = (float*)(ws + WS_MODF);
  k_mod<<<1024, 256, 0, stream>>>(c, wmod, modp, x, wq, wk, wv);
  k_ln1kv<<<256, 512, K1_LDS, stream>>>(x, wk, wv, (const char*)modp, modf, (uint16_t*)(ws + WS_MODH), ws + WS_H, ws + WS_KV);
  k_qattn<<<256, 512, K2_LDS, stream>>>(ws + WS_H, wq, ws + WS_KV, ws + WS_O, w1, ws + WS_MODH, (float*)(ws + WS_UTP), wo, w2);
  k_oproj<<<256, 512, K3_LDS, stream>>>(ws + WS_O, wo, x, modf, ws + WS_X1, ws + WS_Y, (f32x2*)(ws + WS_STAT));
  k_ffn1<<<256, 512, K4_LDS, stream>>>(ws + WS_Y, w1, (const f32x2*)(ws + WS_STAT), (const float*)(ws + WS_UTP), ws + WS_G);
  k_ffn2<<<256, 512, K5_LDS, stream>>>(ws + WS_G, w2, ws + WS_X1, modf, (char*)d_out);
}
```

```cpp
#include <hip/hip_runtime.h>
#include <hip/hip_bf16.h>
#include <stdint.h>

typedef __attribute__((ext_vector_type(2))) float f32x2;
typedef __attribute__((ext_vector_type(4))) float f32x4;
typedef __attribute__((ext_vector_type(16))) float f32x16;
typedef __attribute__((ext_vector_type(2))) __bf16 bf16x2;
typedef __attribute__((ext_vector_type(8))) __bf16 bf16x8;
typedef __attribute__((ext_vector_type(4))) short s16x4;
typedef __attribute__((ext_vector_type(2))) unsigned u32x2;
typedef __attribute__((ext_vector_type(4))) unsigned u32x4;
#define LDS __attribute__((address_space(3)))
typedef LDS char* lptr;
#define PF0 768
#define NEXTPF 0
#define DEV __device__ __forceinline__

constexpr int SEQ = 2048, DM = 1024, FF = 3584, NMOD = 6 * DM;
constexpr size_t WS_MODP = 0;
constexpr size_t WS_UTP = 98304;
constexpr size_t WS_MODF = 212992;
constexpr size_t WS_MODH = 237568;
constexpr size_t WS_STAT = 262144;
constexpr size_t WS_KV = 786432;
constexpr size_t WS_H = (size_t)1 << 20;
constexpr size_t WS_O = (size_t)5 << 20;
constexpr size_t WS_X1 = (size_t)9 << 20;
constexpr size_t WS_Y = (size_t)13 << 20;
constexpr size_t WS_G = (size_t)17 << 20;

DEV unsigned pk(float a, float b) { return __builtin_bit_cast(unsigned, __builtin_convertvector((f32x2){a, b}, bf16x2)); }
DEV float rbf(float a) { return __uint_as_float(pk(a, 0.f) << 16); }
DEV float blo(unsigned u) { return __uint_as_float(u << 16); }
DEV float bhi(unsigned u) { return __uint_as_float(u & 0xffff0000u); }
DEV float silu(float z) { return z * __builtin_amdgcn_rcpf(1.f + __builtin_amdgcn_exp2f(-1.4426950408889634f * z)); }
DEV f32x16 mfma32(bf16x8 a, bf16x8 b, f32x16 c) { return __builtin_amdgcn_mfma_f32_32x32x16_bf16(a, b, c, 0, 0, 0); }
DEV f32x4 mfma16(bf16x8 a, bf16x8 b, f32x4 c) { return __builtin_amdgcn_mfma_f32_16x16x32_bf16(a, b, c, 0, 0, 0); }
DEV bf16x8 asb(u32x4 v) { return __builtin_bit_cast(bf16x8, v); }
DEV bf16x8 ldsA(lptr p) { return *(const LDS bf16x8*)p; }
DEV bf16x8 ldsT(lptr p, int second) {
  s16x4 a = __builtin_amdgcn_ds_read_tr16_b64_v4i16((LDS s16x4*)p);
  s16x4 b = __builtin_amdgcn_ds_read_tr16_b64_v4i16((LDS s16x4*)(p + second));
  return __builtin_bit_cast(bf16x8, __builtin_shufflevector(a, b, 0, 1, 2, 3, 4, 5, 6, 7));
}
template <int C> DEV float dppadd(float v) { return v + __int_as_float(__builtin_amdgcn_update_dpp(0, __float_as_int(v), C, 0xf, 0xf, true)); }
DEV float swap32add(float v) { auto r = __builtin_amdgcn_permlane32_swap(__float_as_uint(v), __float_as_uint(v), false, false); return __uint_as_float(r[0]) + __uint_as_float(r[1]); }
DEV float swap16add(float v) { auto r = __builtin_amdgcn_permlane16_swap(__float_as_uint(v), __float_as_uint(v), false, false); return __uint_as_float(r[0]) + __uint_as_float(r[1]); }
DEV float swap32max(float v) { auto r = __builtin_amdgcn_permlane32_swap(__float_as_uint(v), __float_as_uint(v), false, false); return __builtin_fmaxf(__uint_as_float(r[0]), __uint_as_float(r[1])); }
DEV float wavesum(float v) { v = dppadd<0xB1>(v); v = dppadd<0x4E>(v); v = dppadd<0x141>(v); v = dppadd<0x140>(v); v = swap16add(v); return swap32add(v); }

#define SETM0(x) asm volatile("s_mov_b32 m0, %0\n\ts_nop 0" ::"s"(x) : "memory")
template <int OFF> DEV void glds(unsigned voff, const void* base) { asm volatile("global_load_lds_dwordx4 %0, %1 offset:%2" ::"v"(voff), "s"(base), "i"(OFF) : "memory"); }
template <int N> DEV void waitbar() { asm volatile("s_waitcnt vmcnt(%0) lgkmcnt(0)\n\ts_barrier" ::"i"(N) : "memory"); }
template <int OFF> DEV u32x4 gload(unsigned voff, const void* base) { u32x4 r; asm volatile("global_load_dwordx4 %0, %1, %2 offset:%3" : "=v"(r) : "v"(voff), "s"(base), "i"(OFF) : "memory"); return r; }
template <int OFF> DEV u32x2 gload2(unsigned voff, const void* base) { u32x2 r; asm volatile("global_load_dwordx2 %0, %1, %2 offset:%3" : "=v"(r) : "v"(voff), "s"(base), "i"(OFF) : "memory"); return r; }
#define STPOL "sc1"
DEV void gstore(void* p, u32x4 v) { asm volatile("global_store_dwordx4 %0, %1, off " STPOL "\n\ts_nop 1" ::"v"(p), "v"(v) : "memory"); }
#define CODE_RANGE(A, B, base, size) const char* base; unsigned size; { unsigned long long pc_; int o_; \
  asm volatile("s_getpc_b64 %0\n.Lcr%=:\n\ts_mov_b32 %1, " #A "-.Lcr%=\n\ts_mov_b32 %2, " #B "-" #A : "=s"(pc_), "=s"(o_), "=s"(size)); base = (const char*)(pc_ + o_); }
#define SB() __builtin_amdgcn_sched_barrier(0)
extern __shared__ __attribute__((aligned(16))) char smem_raw[];

struct Stamps {};
#define ST_INIT() Stamps S_
#define ST(i)
#define ST_END(k)

constexpr int K1_W = 0, K1_H = 65536, K1_MV = K1_H + 64 * 528, K1_RED = K1_MV + 8192, K1_LDS = K1_RED + 32768;
extern "C" __global__ __launch_bounds__(512, 2) void k_ln1kv(const char* __restrict__ x, const char* __restrict__ wk, const char* __restrict__ wv,
                                                  const char* __restrict__ modp, float* __restrict__ modf, uint16_t* __restrict__ modh,
                                                  char* __restrict__ hbuf, char* __restrict__ kv) {
  const lptr sm = (lptr)smem_raw;
  ST_INIT();
  const int b = blockIdx.x, t = threadIdx.x, l = t & 63, w = __builtin_amdgcn_readfirstlane(t >> 6);
  const int which = (b >> 3) & 1, rb = (b & 7) * 16 + (b >> 4);
  u32x4 xv[4], mp[4];
  { const char* xb = x + (size_t)(rb * 16 + 2 * w) * 2048; const unsigned vo = l * 32;
    xv[0] = gload<0>(vo, xb); xv[1] = gload<16>(vo, xb); xv[2] = gload<2048>(vo, xb); xv[3] = gload<2064>(vo, xb);
#pragma unroll
    for (int s = 0; s < 4; ++s) mp[s] = gload<0>(t * 16, modp + s * NMOD * 4); }
  { const char* wsrc = (which ? wv : wk) + w * 8192;
#pragma unroll
    for (int g = 0; g < 2; ++g) { SETM0(K1_W + w * 8192 + g * 4096); const char* s = wsrc + g * 4096;
      glds<0>(l * 16, s); glds<1024>(l * 16, s); glds<2048>(l * 16, s); glds<3072>(l * 16, s); } }
  asm volatile("s_waitcnt vmcnt(8)" : "+v"(xv[0]), "+v"(xv[1]), "+v"(xv[2]), "+v"(xv[3]), "+v"(mp[0]), "+v"(mp[1]), "+v"(mp[2]), "+v"(mp[3])::"memory");
  ST(1);
  { f32x4 o;
#pragma unroll
    for (int e = 0; e < 4; ++e) { const float v = rbf((__uint_as_float(mp[0][e]) + __uint_as_float(mp[1][e])) + (__uint_as_float(mp[2][e]) + __uint_as_float(mp[3][e])));
      o[e] = t >= 256 ? rbf(1.f + v) : v; }
    *(LDS f32x4*)(sm + K1_MV + (t & 256) * 16 + (t & 3) * 1024 + ((t & 255) >> 2) * 16) = o; }
  f32x2 v[2][8]; float rs[2];
#pragma unroll
  for (int r = 0; r < 2; ++r) { f32x2 s = {0.f, 0.f};
#pragma unroll
    for (int e = 0; e < 8; ++e) { const unsigned u = xv[2 * r + (e >> 2)][e & 3]; v[r][e] = (f32x2){blo(u), bhi(u)}; s += v[r][e]; }
    const float m = wavesum(s[0] + s[1]) * (1.f / 1024.f); f32x2 q = {0.f, 0.f};
#pragma unroll
    for (int e = 0; e < 8; ++e) { v[r][e] -= m; q += v[r][e] * v[r][e]; }
    rs[r] = __builtin_amdgcn_rsqf(wavesum(q[0] + q[1]) * (1.f / 1024.f) + 1e-5f); }
  asm volatile("s_waitcnt lgkmcnt(0)\n\ts_barrier" ::: "memory");
  { unsigned u[2][8];
#pragma unroll
    for (int e = 0; e < 4; ++e) { const f32x4 a = *(const LDS f32x4*)(sm + K1_MV + e * 1024 + l * 16), cc = *(const LDS f32x4*)(sm + K1_MV + 4096 + e * 1024 + l * 16);
#pragma unroll
      for (int r = 0; r < 2; ++r)
#pragma unroll
        for (int i = 0; i < 2; ++i) { const f32x2 h = v[r][2 * e + i] * rs[r] * (f32x2){cc[2 * i], cc[2 * i + 1]} + (f32x2){a[2 * i], a[2 * i + 1]}; u[r][2 * e + i] = pk(h[0], h[1]); } }
#pragma unroll
    for (int r = 0; r < 2; ++r) { const lptr d = sm + K1_H + l * 528 + (2 * w + r) * 16;
      *(LDS u32x4*)d = (u32x4){u[r][0], u[r][1], u[r][4], u[r][5]}; *(LDS u32x4*)(d + 256) = (u32x4){u[r][2], u[r][3], u[r][6], u[r][7]}; } }
  ST(2); waitbar<0>(); ST(3);
  f32x16 acc = {};
  { const int hi = l >> 5; const lptr hp = sm + K1_H + w * 8 * 528 + hi * 256 + (l & 15) * 16;
    const lptr wp = sm + K1_W + (w * 128 + 4 * hi + ((l >> 2) & 3)) * 64 + ((l >> 4) & 1) * 32 + (l & 3) * 8;
    bf16x8 fa[8], fb[8];
#pragma unroll
    for (int i = 0; i < 8; ++i) { fa[i] = ldsA(hp + i * 528); fb[i] = ldsT(wp + i * 1024, 512); }
#pragma unroll
    for (int i = 0; i < 2; ++i) { const int id = t + 512 * i, tok = id & 15, h2 = (id >> 4) & 1, kb = which * 32 + (id >> 5);
      gstore(hbuf + (size_t)((rb >> 1) * 64 + kb) * 1024 + (h2 * 32 + (rb & 1) * 16 + tok) * 16, *(const LDS u32x4*)(sm + K1_H + kb * 528 + h2 * 256 + tok * 16)); }
    if (which == 0) {
#pragma unroll
      for (int i = 0; i < 8; ++i) acc = mfma32(fb[i], fa[i], acc);
    } else {
#pragma unroll
      for (int i = 0; i < 8; ++i) acc = mfma32(fa[i], fb[i], acc);
    } }
#pragma unroll
  for (int q = 0; q < 4; ++q) *(LDS f32x4*)(sm + K1_RED + w * 4096 + q * 1024 + l * 16) = (f32x4){acc[4 * q], acc[4 * q + 1], acc[4 * q + 2], acc[4 * q + 3]};
  asm volatile("s_waitcnt lgkmcnt(0)\n\ts_barrier" ::: "memory");
  if (w == 0) {
    const lptr rp = sm + K1_RED + (which ? l * 16 : (l >> 5) * 2048 + (((l >> 4) & 1) * 32 + (l & 15)) * 16);
    f32x4 a = {}, c = {};
#pragma unroll
    for (int u = 0; u < 8; ++u) { a += *(const LDS f32x4*)(rp + u * 4096); c += *(const LDS f32x4*)(rp + u * 4096 + 1024); }
    char* base = kv + (rb >> 1) * 4096;
    char* d = which ? base + 2048 + (rb & 1) * 1024 + l * 16 : base + (l >> 5) * 1024 + (((l >> 4) & 1) * 32 + (rb & 1) * 16 + (l & 15)) * 16;
    *(u32x4*)d = (u32x4){pk(a[0], a[1]), pk(a[2], a[3]), pk(c[0], c[1]), pk(c[2], c[3])};
  } else if (b < 64 && w == 1) {
    const int col = 2048 + b * 64 + l; const float* mpf = (const float*)modp;
    float v2 = rbf((mpf[col] + mpf[NMOD + col]) + (mpf[2 * NMOD + col] + mpf[3 * NMOD + col]));
    if ((col >> 10) == 4) v2 = rbf(1.f + v2);
    modf[col] = v2; modh[col] = (uint16_t)(__float_as_uint(v2) >> 16);
  }
  ST_END(1);
}

DEV void bar() { asm volatile("s_waitcnt lgkmcnt(0)\n\ts_barrier" ::: "memory"); }
template <int N, int G, int EXTRA> DEV void loader_tail() { waitbar<G * N + EXTRA>(); if constexpr (N > 0) loader_tail<N - 1, G, EXTRA>(); }
constexpr int issued(int t, int S) { return t == 0 ? 2 : (2 + 2 * t < S + t - 1 ? 2 + 2 * t : S + t - 1); }
template <int T, int S, int G, int STAGE, class I> DEV void loader_ramp(I&& issue) {
#pragma unroll
  for (int s = T == 0 ? 0 : issued(T - 1, S); s < issued(T, S); ++s) issue(s, (s % S) * STAGE);
  waitbar<G*(issued(T, S) - T - 1)>();
  if constexpr (issued(T, S) < S + T - 1 || T == 0) loader_ramp<T + 1, S, G, STAGE>(issue);
}
constexpr int ramp_end(int S) { int t = 1; while (issued(t, S) < S + t - 1) ++t; return t; }
template <int KT, int S, int G, int STAGE, int EXTRA, class I, class X>
DEV void loader(I&& issue, X&& extra) {
  loader_ramp<0, S, G, STAGE>(issue);
  constexpr int T0 = ramp_end(S);
  int slot = ((S + T0 - 1) % S) * STAGE;
  for (int t = T0; t <= KT - S; ++t) { issue(t - 1 + S, slot); slot = slot == (S - 1) * STAGE ? 0 : slot + STAGE; if (t < KT - S) waitbar<G*(S - 2)>(); }
  extra();
  loader_tail<S - 2, G, EXTRA>();
}
constexpr int GA_STAGE = 24576;
template <int KT, int S, int LDW, int EXTRA, class X>
DEV void gemmA_load(const char* act, const char* wgt, int l, int lw, X&& extra) {
  const char* ap = act + (size_t)lw * (KT * 4096);
  const char* wp = wgt + (size_t)lw * (16 * LDW * 2);
  const unsigned va = l * 16, vb = (l >> 3) * (LDW * 2) + (((l & 7) ^ (((l >> 4) & 1) << 2)) * 16);
  loader<KT, S, 6, GA_STAGE, EXTRA>([&](int t, int slot) {
    SETM0(slot + lw * 4096); const char* a = ap + (size_t)t * 4096; glds<0>(va, a); glds<1024>(va, a); glds<2048>(va, a); glds<3072>(va, a);
    SETM0(slot + 16384 + lw * 2048); const char* s = wp + (size_t)t * (64 * LDW * 2); glds<0>(vb, s); glds<1024>(vb, s + 8 * LDW * 2 - 1024); }, extra);
}
struct FragA { bf16x8 a0[4], a1[4], b[4]; };
template <int KT, int S>
DEV void gemmA_compute(const lptr sm, int l, int w, f32x16& c0, f32x16& c1, Stamps& S_) {
  const int q = (l >> 2) & 3, p = l & 3;
  const lptr ra = sm + (w & 1) * 8192 + l * 16;
  const lptr rb = sm + 16384 + (4 * (l >> 5) + q) * 128 + ((((w >> 1) * 4 + ((l >> 4) & 1) * 2 + (p >> 1)) ^ ((q >> 1) << 2)) * 16) + (p & 1) * 8;
  c0 = (f32x16){}; c1 = (f32x16){};
  FragA f, g; int slot = 0;
  auto rd = [&](FragA& d, int k) { d.a0[k] = ldsA(ra + slot + k * 1024); d.b[k] = ldsT(rb + slot + k * 2048, 1024); d.a1[k] = ldsA(ra + slot + 4096 + k * 1024); };
  auto step = [&](FragA& cur, FragA& nxt) { bar(); slot = slot == (S - 1) * GA_STAGE ? 0 : slot + GA_STAGE;
#pragma unroll
    for (int k = 0; k < 4; ++k) { if (k < 2) rd(nxt, 2 * k); c0 = mfma32(cur.b[k], cur.a0[k], c0); SB(); if (k < 2) rd(nxt, 2 * k + 1); c1 = mfma32(cur.b[k], cur.a1[k], c1); SB(); } };
  bar(); ST(2);
#pragma unroll
  for (int k = 0; k < 4; ++k) rd(f, k);
  for (int t = 0; t < KT - 2; t += 2) { step(f, g); step(g, f); }
  step(f, g);
#pragma unroll
  for (int k = 0; k < 4; ++k) { c0 = mfma32(g.b[k], g.a0[k], c0); c1 = mfma32(g.b[k], g.a1[k], c1); }
  ST(3);
}

constexpr int K2_KV0 = 98304, K2_KV1 = 131072, K2_KV2 = 0, K2_UW = 32768, K2_Q = 65536, K2_RED = 73728, K2_DUMP = 81920, K2_LDS = 163840;
DEV void pack16(const f32x16& s, u32x4& a, u32x4& b) {
  a = (u32x4){pk(s[0], s[1]), pk(s[2], s[3]), pk(s[4], s[5]), pk(s[6], s[7])};
  b = (u32x4){pk(s[8], s[9]), pk(s[10], s[11]), pk(s[12], s[13]), pk(s[14], s[15])}; }
DEV float max16(const f32x16& s) { float m = s[0];
#pragma unroll
  for (int i = 1; i < 16; ++i) m = __builtin_fmaxf(m, s[i]);
  return swap32max(m); }
#define EX2(v) __builtin_amdgcn_exp2f(v)
#define PIN(x) asm volatile("" : "+v"(x))

extern "C" __global__ __launch_bounds__(512, 2) void k_qattn(const char* __restrict__ hbuf, const char* __restrict__ wq, const char* __restrict__ kv, char* __restrict__ obuf,
                                                  const char* __restrict__ w1, const char* __restrict__ modh, float* __restrict__ utp,
                                                  const char* __restrict__ wo, const char* __restrict__ w2) {
  const lptr sm = (lptr)smem_raw;
  ST_INIT();
  const int b = blockIdx.x, l = threadIdx.x & 63, w = __builtin_amdgcn_readfirstlane(threadIdx.x >> 6);
  const int xcd = b & 7, j = b >> 3, tb = (xcd >> 1) * 4 + (j & 3), hp = (xcd & 1) * 8 + (j >> 2);
  const unsigned l16 = l * 16;
  u32x4 q0u, q1u;
  if (w >= 4) {
    gemmA_load<16, 4, DM, 8>(hbuf + (size_t)tb * 4 * 64 * 1024, wq + hp * 128, l, w - 4, [&] {
#pragma unroll
      for (int g = 0; g < 2; ++g) { SETM0(K2_KV0 + (w - 4) * 8192 + g * 4096); const char* s = kv + (w - 4) * 8192 + g * 4096;
        glds<0>(l16, s); glds<1024>(l16, s); glds<2048>(l16, s); glds<3072>(l16, s); } });
    waitbar<0>();
    q0u = *(const LDS u32x4*)(sm + K2_Q + (w - 4) * 2048 + l16); q1u = *(const LDS u32x4*)(sm + K2_Q + (w - 4) * 2048 + 1024 + l16);
  } else {
    f32x16 c0, c1; gemmA_compute<16, 4>(sm, l, w, c0, c1, S_);
    constexpr float QS = 0.17677669529663687f * 1.4426950408889634f;
    c0 *= QS; c1 *= QS;
    u32x4 a, c; pack16(c1, a, c); *(LDS u32x4*)(sm + K2_Q + w * 2048 + l16) = a; *(LDS u32x4*)(sm + K2_Q + w * 2048 + 1024 + l16) = c;
    pack16(c0, q0u, q1u);
    bar();
  }
  const bf16x8 q0 = asb(q0u), q1 = asb(q1u);
  const char* kvw = kv + w * 4096;
  auto kvload = [&](int st, int slot) { SETM0(slot + w * 4096); const char* s = kvw + st * 32768; glds<0>(l16, s); glds<1024>(l16, s); glds<2048>(l16, s); glds<3072>(l16, s); };
  kvload(1, K2_KV1);
  const unsigned one2 = ((l & 7) == ((l >> 4) & 1) * 4) ? 0x3f803f80u : 0u;
  const bf16x8 ones = asb((u32x4){one2, one2, one2, one2});
  const lptr kp0 = sm + K2_KV0 + l16, kp1 = sm + K2_KV1 + l16, kp2 = sm + K2_KV2 + l16;
  bf16x8 k0 = ldsA(kp0), k1 = ldsA(kp0 + 1024), v0 = ldsA(kp0 + 2048), v1 = ldsA(kp0 + 3072);
  f32x16 negm, sA, sB, o = {}; f32x4 ls = {};
  { f32x16 z = {}; z = mfma32(k0, q0, z); z = mfma32(k1, q1, z); const float m = -max16(z);
#pragma unroll
    for (int i = 0; i < 16; ++i) negm[i] = m; }
  PIN(negm);
  sA = mfma32(k0, q0, negm); sA = mfma32(k1, q1, sA);
  k0 = ldsA(kp0 + 4096); k1 = ldsA(kp0 + 4096 + 1024);
  u32x4 p0, p1 = {0, 0, 0, 0}; bf16x8 v1p = v1;
#define TILE(S, N, KP, VP)                                                                                            \
  { N = mfma32(k0, q0, negm); SB();                                                                                  \
    S[0] = EX2(S[0]); S[1] = EX2(S[1]); S[2] = EX2(S[2]); S[3] = EX2(S[3]); p0[0] = pk(S[0], S[1]); p0[1] = pk(S[2], S[3]); PIN(p0); SB(); \
    o = mfma32(v1p, asb(p1), o); k0 = ldsA(KP); SB();                                                                \
    S[4] = EX2(S[4]); S[5] = EX2(S[5]); S[6] = EX2(S[6]); S[7] = EX2(S[7]); p0[2] = pk(S[4], S[5]); p0[3] = pk(S[6], S[7]); PIN(p0); SB(); \
    ls = mfma16(ones, asb(p1), ls); v1p = v1; v1 = ldsA((VP) + 1024); SB();                                          \
    S[8] = EX2(S[8]); S[9] = EX2(S[9]); PIN(S); SB();                                                                \
    N = mfma32(k1, q1, N); SB();                                                                                     \
    S[10] = EX2(S[10]); S[11] = EX2(S[11]); S[12] = EX2(S[12]); S[13] = EX2(S[13]); p1[0] = pk(S[8], S[9]); p1[1] = pk(S[10], S[11]); PIN(p1); PIN(S); SB(); \
    o = mfma32(v0, asb(p0), o); k1 = ldsA((KP) + 1024); SB();                                                        \
    S[14] = EX2(S[14]); S[15] = EX2(S[15]); p1[2] = pk(S[12], S[13]); p1[3] = pk(S[14], S[15]); PIN(p1); SB();       \
    ls = mfma16(ones, asb(p0), ls); v0 = ldsA(VP); SB(); }
#define STAGE(P, Q, ...)                                                                                              \
  TILE(sA, sB, P + 2 * 4096, P + 1 * 4096 + 2048) TILE(sB, sA, P + 3 * 4096, P + 2 * 4096 + 2048)                     \
  TILE(sA, sB, P + 4 * 4096, P + 3 * 4096 + 2048) TILE(sB, sA, P + 5 * 4096, P + 4 * 4096 + 2048)                     \
  TILE(sA, sB, P + 6 * 4096, P + 5 * 4096 + 2048) TILE(sB, sA, P + 7 * 4096, P + 6 * 4096 + 2048)                     \
  { __VA_ARGS__ }                                                                                                     \
  TILE(sA, sB, Q, P + 7 * 4096 + 2048) TILE(sB, sA, Q + 4096, Q + 2048)
  const int ub = b < 224 ? b : b - 224, cg = ub % 56, ks = ub / 56;
  u32x2 vf[4];
  ST(4);
  STAGE(kp0, kp1, waitbar<0>(); kvload(2, K2_KV2);
        { SETM0(K2_UW + w * 4096); const char* s = w1 + (size_t)(ks * 256 + w * 32) * (FF * 2) + cg * 128;
          const unsigned vo = (l >> 3) * (FF * 2) + (((l & 7) ^ (((l >> 4) & 1) << 2)) * 16);
          glds<0>(vo, s); glds<1024>(vo, s + 8 * FF * 2 - 1024); glds<2048>(vo, s + 16 * FF * 2 - 2048); glds<3072>(vo, s + 24 * FF * 2 - 3072);
          const char* mv = modh + (ks * 256 + w * 32) * 2; const unsigned vm = ((4 - (l & 1)) * DM + 4 * (l >> 5)) * 2;
          vf[0] = gload2<0>(vm, mv); vf[1] = gload2<16>(vm, mv); vf[2] = gload2<32>(vm, mv); vf[3] = gload2<48>(vm, mv); })
  STAGE(kp1, kp2, waitbar<8>(); kvload(3, K2_KV0);)
  STAGE(kp2, kp0, waitbar<0>();
        if (b < 224) { PIN(vf[0]); PIN(vf[1]); PIN(vf[2]); PIN(vf[3]); const int q = (l >> 2) & 3; const int p = l & 3; f32x16 d0 = {}; f32x16 d1 = {};
          const lptr rb = sm + K2_UW + (w * 32 + 4 * (l >> 5) + q) * 128 + (((((l >> 4) & 1) * 2 + (p >> 1)) ^ ((q >> 1) << 2)) * 16) + (p & 1) * 8;
          const lptr rc = sm + K2_UW + (w * 32 + 4 * (l >> 5) + q) * 128 + (((4 + ((l >> 4) & 1) * 2 + (p >> 1)) ^ ((q >> 1) << 2)) * 16) + (p & 1) * 8;
          _Pragma("unroll") for (int kk = 0; kk < 2; ++kk) { const bf16x8 vv = asb((u32x4){vf[2 * kk][0], vf[2 * kk][1], vf[2 * kk + 1][0], vf[2 * kk + 1][1]});
            d0 = mfma32(ldsT(rb + kk * 2048, 1024), vv, d0); d1 = mfma32(ldsT(rc + kk * 2048, 1024), vv, d1); }
          if ((l & 31) < 2) { const lptr d = sm + K2_RED + w * 512 + (l & 31) * 256 + (l >> 5) * 16;
            _Pragma("unroll") for (int i = 0; i < 4; ++i) { *(LDS f32x4*)(d + i * 32) = (f32x4){d0[4 * i], d0[4 * i + 1], d0[4 * i + 2], d0[4 * i + 3]};
              *(LDS f32x4*)(d + 128 + i * 32) = (f32x4){d1[4 * i], d1[4 * i + 1], d1[4 * i + 2], d1[4 * i + 3]}; } } }
        kvload(4, K2_KV1);
        { SETM0(K2_DUMP + w * 1024); glds<0>(l16, wo + b * 8192 + w * 1024); glds<0>(l16, w2 + (size_t)b * 28672 + w * 1024); })
  STAGE(kp0, kp1, waitbar<2>();
        if (b < 224 && w < 2) { float v = 0.f;
          _Pragma("unroll") for (int u = 0; u < 8; ++u) v += *(const LDS float*)(sm + K2_RED + u * 512 + w * 256 + l * 4);
          utp[(ks * 2 + w) * FF + cg * 64 + l] = v; }
        kvload(5, K2_KV2);
        { SETM0(K2_DUMP + w * 1024); glds<0>(l16, w2 + (size_t)b * 28672 + 8192 + w * 1024); glds<0>(l16, w2 + (size_t)b * 28672 + 16384 + w * 1024); })
  STAGE(kp1, kp2, waitbar<2>(); kvload(6, K2_KV0);
        { SETM0(K2_DUMP + w * 1024); glds<0>(l16, w2 + (size_t)b * 28672 + 24576 + (w & 3) * 1024); })
  STAGE(kp2, kp0, waitbar<1>(); kvload(7, K2_KV1);
        if (NEXTPF && w == 0) { CODE_RANGE(k_oproj, k_mod, cb, cs) const unsigned o = j * 1024 + l16; SETM0(K2_DUMP); glds<0>(o < cs ? o : 0, cb); })
  STAGE(kp0, kp1, waitbar<0>();)
  STAGE(kp1, kp2, )
  ST(5);
  o = mfma32(v1p, asb(p1), o); ls = mfma16(ones, asb(p1), ls);
  float lsum = ls[0];
  { float chk = lsum;
#pragma unroll
    for (int i = 0; i < 16; ++i) chk = __builtin_fmaxf(chk, __builtin_fabsf(o[i]) * 0x1p-40f);
    bool bad = !(chk < 0x1p80f);
#pragma unroll
    for (int i = 0; i < 16; ++i) bad |= (o[i] != o[i]);
    bad |= (lsum != lsum);
    if (__any(bad)) {
      float m = -INFINITY; lsum = 0.f; o = (f32x16){};
      for (int t = 0; t < 64; ++t) { const bf16x8* g = (const bf16x8*)(kv + t * 4096) + l;
        f32x16 s = {}; s = mfma32(g[0], q0, s); s = mfma32(g[64], q1, s);
        const float mn = __builtin_fmaxf(m, max16(s)), al = EX2(m - mn); m = mn; float rsum = 0.f;
#pragma unroll
        for (int i = 0; i < 16; ++i) { s[i] = rbf(EX2(s[i] - mn)); rsum += s[i]; o[i] *= al; }
        lsum = lsum * al + swap32add(rsum);
        u32x4 a, c; pack16(s, a, c); o = mfma32(g[128], asb(a), o); o = mfma32(g[192], asb(c), o); } } }
  o *= 1.f / lsum;
  u32x4 o0, o1; pack16(o, o0, o1);
  char* d = obuf + (size_t)((tb * 4 + 2 * (w & 1) + (w >> 2)) * 64 + (hp * 2 + ((w >> 1) & 1)) * 2) * 1024 + l16;
  gstore(d, o0); gstore(d + 1024, o1);
  ST_END(2);
}

DEV u32x4 rowswap(u32x4 v) { auto a = __builtin_amdgcn_permlane32_swap(v[0], v[2], false, false); auto c = __builtin_amdgcn_permlane32_swap(v[1], v[3], false, false);
  return (u32x4){a[0], c[0], a[1], c[1]}; }

constexpr int K3_S = 6, K3_LDS = K3_S * GA_STAGE;
extern "C" __global__ __launch_bounds__(512, 2) void k_oproj(const char* __restrict__ obuf, const char* __restrict__ wo, const char* __restrict__ x, const float* __restrict__ modf,
                                                  char* __restrict__ x1b, char* __restrict__ yb, f32x2* __restrict__ stat) {
  const lptr sm = (lptr)smem_raw;
  ST_INIT();
  const int b = blockIdx.x, l = threadIdx.x & 63, w = __builtin_amdgcn_readfirstlane(threadIdx.x >> 6);
  const int xcd = b & 7, j = b >> 3, tb = (xcd >> 1) * 4 + (j & 3), nt = (xcd & 1) * 8 + (j >> 2);
  if (w >= 4) { u32x4 e; gemmA_load<16, K3_S, DM, NEXTPF>(obuf + (size_t)tb * 4 * 64 * 1024, wo + nt * 128, l, w - 4, [&] {
      if (NEXTPF) { CODE_RANGE(k_ffn1, k_ffn2, cb, cs) const unsigned o = (j & 3) * 4096 + (w - 4) * 1024 + l * 16; e = gload<0>(o < cs ? o : 0, cb); } });
    if (NEXTPF) asm volatile("s_waitcnt vmcnt(0)" : "+v"(e)::"memory"); return; }
  const int hi = l >> 5, tb32 = tb * 4 + 2 * (w & 1), n0 = nt * 64 + (w >> 1) * 32;
  u32x4 xr[2][2]; f32x4 ga[4], cb[4];
#pragma unroll
  for (int i = 0; i < 2; ++i)
#pragma unroll
    for (int h = 0; h < 2; ++h) xr[i][h] = *(const u32x4*)(x + (size_t)((tb32 + i) * 32 + (l & 31)) * 2048 + (n0 + 16 * h + 8 * hi) * 2);
#pragma unroll
  for (int q = 0; q < 4; ++q) { ga[q] = *(const f32x4*)(modf + 2 * DM + n0 + 8 * q + 4 * hi); cb[q] = *(const f32x4*)(modf + 4 * DM + n0 + 8 * q + 4 * hi); }
  f32x16 acc[2]; gemmA_compute<16, K3_S>(sm, l, w, acc[0], acc[1], S_);
#pragma unroll
  for (int i = 0; i < 2; ++i) { unsigned xo[8], yo[8]; float x1[16], s = 0.f;
#pragma unroll
    for (int h = 0; h < 2; ++h) { const u32x4 xs = rowswap(xr[i][h]);
#pragma unroll
      for (int e = 0; e < 4; ++e) { const int r = 8 * h + 2 * e, q = r >> 2, c = r & 3;
        const unsigned u = pk(blo(xs[e]) + ga[q][c] * acc[i][r], bhi(xs[e]) + ga[q][c + 1] * acc[i][r + 1]);
        x1[r] = blo(u); x1[r + 1] = bhi(u); xo[4 * h + e] = u; yo[4 * h + e] = pk(x1[r] * cb[q][c], x1[r + 1] * cb[q][c + 1]); s += x1[r] + x1[r + 1]; } }
    const size_t off = (size_t)((tb32 + i) * 64 + (n0 >> 4)) * 1024 + l * 16;
    gstore(yb + off, (u32x4){yo[0], yo[1], yo[2], yo[3]}); gstore(yb + off + 1024, (u32x4){yo[4], yo[5], yo[6], yo[7]});
    gstore(x1b + off, (u32x4){xo[0], xo[1], xo[2], xo[3]}); gstore(x1b + off + 1024, (u32x4){xo[4], xo[5], xo[6], xo[7]});
    const float mean = swap32add(s) * (1.f / 32.f); float m2 = 0.f;
#pragma unroll
    for (int e = 0; e < 16; ++e) m2 += (x1[e] - mean) * (x1[e] - mean);
    m2 = swap32add(m2);
    if (hi == 0) stat[((tb32 + i) * 32 + (n0 >> 5)) * 32 + l] = (f32x2){mean, m2}; }
  ST_END(3);
}

constexpr int K4_STAGE = 45056, K4_UT = 3 * K4_STAGE, K4_LDS = K4_UT + 2048;
struct FragB { bf16x8 a, b[7]; };
extern "C" __global__ __launch_bounds__(512, 2) void k_ffn1(const char* __restrict__ yb, const char* __restrict__ w1, const f32x2* __restrict__ stat,
                                                 const float* __restrict__ utp, char* __restrict__ gb) {
  const lptr sm = (lptr)smem_raw;
  ST_INIT();
  const int b = blockIdx.x, t = threadIdx.x, l = t & 63, w = __builtin_amdgcn_readfirstlane(t >> 6);
  const int xcd = b & 7, j = b >> 3, tb = (xcd >> 2) * 8 + (j & 7), nt = (xcd & 3) * 4 + (j >> 3);
  if (w >= 4) { const int lw = w - 4;
    const char* ap = yb + (size_t)(tb * 4 + lw) * 64 * 1024; const char* wp = w1 + nt * 448;
    unsigned vb[7];
#pragma unroll
    for (int i = 0; i < 7; ++i) { const int c = (lw * 7 + i) * 64 + l, r = c / 28; vb[i] = r * (FF * 2) + (c - r * 28) * 16 + 3072 - i * 1024; }
    const unsigned va = l * 16;
    u32x4 e; loader<16, 3, 11, K4_STAGE, NEXTPF>([&](int tt, int slot) {
      SETM0(slot + lw * 4096); const char* a = ap + (size_t)tt * 4096;
      glds<0>(va, a); glds<1024>(va, a); glds<2048>(va, a); glds<3072>(va, a);
      SETM0(slot + 16384 + lw * 7168 + 3072); const char* s = wp + (size_t)tt * (64 * FF * 2);
      glds<-3072>(vb[0], s); glds<-2048>(vb[1], s); glds<-1024>(vb[2], s); glds<0>(vb[3], s); glds<1024>(vb[4], s); glds<2048>(vb[5], s); glds<3072>(vb[6], s); }, [&] {
      if (NEXTPF) { CODE_RANGE(k_ffn2, k_mod, cb, cs) const unsigned o = lw * 1024 + l * 16; e = gload<0>(o < cs ? o : 0, cb); } });
    if (NEXTPF) asm volatile("s_waitcnt vmcnt(0)" : "+v"(e)::"memory");
    return; }
  const int hi = l >> 5, tb32 = tb * 4 + w;
  constexpr float NL2E = -1.4426950408889634f;
  float rs, nm;
  { f32x2 st[16]; float up[8];
#pragma unroll
    for (int i = 0; i < 16; ++i) st[i] = stat[(tb32 * 32 + 2 * i + hi) * 32 + (l & 31)];
    const int col = nt * 224 + (t < 224 ? t : 0);
#pragma unroll
    for (int i = 0; i < 8; ++i) up[i] = utp[i * FF + col];
    float mean = 0.f;
#pragma unroll
    for (int i = 0; i < 16; ++i) mean += st[i][0];
    mean = swap32add(mean) * (1.f / 32.f); float m2 = 0.f;
#pragma unroll
    for (int i = 0; i < 16; ++i) m2 += st[i][1] + 32.f * (st[i][0] - mean) * (st[i][0] - mean);
    rs = NL2E * __builtin_amdgcn_rsqf(swap32add(m2) * (1.f / 1024.f) + 1e-5f); nm = -rs * mean;
    if (t < 224) { *(LDS float*)(sm + K4_UT + t * 4) = (up[0] + up[2]) + (up[4] + up[6]); *(LDS float*)(sm + K4_UT + 1024 + t * 4) = NL2E * ((up[1] + up[3]) + (up[5] + up[7])); } }
  ST(1);
  const lptr ra = sm + w * 4096 + l * 16;
  const lptr rb = sm + 16384 + (4 * hi + ((l >> 2) & 3)) * 448 + ((l >> 4) & 1) * 32 + (l & 3) * 8;
  f32x16 acc[7];
#pragma unroll
  for (int n = 0; n < 7; ++n) acc[n] = (f32x16){};
  FragB f, g; int slot = 0;
  auto kstep = [&](FragB& cur, FragB& nxt, int k) {
    nxt.a = ldsA(ra + slot + k * 1024);
#pragma unroll
    for (int n = 0; n < 7; ++n) { acc[n] = mfma32(cur.b[n], cur.a, acc[n]); nxt.b[n] = ldsT(rb + slot + k * 7168 + n * 64, 3584); SB(); } };
  bar(); ST(2);
  f.a = ldsA(ra);
#pragma unroll
  for (int n = 0; n < 7; ++n) f.b[n] = ldsT(rb + n * 64, 3584);
  for (int tt = 0; tt < 15; ++tt) { kstep(f, g, 1); kstep(g, f, 2); kstep(f, g, 3); bar(); slot = slot == 2 * K4_STAGE ? 0 : slot + K4_STAGE; kstep(g, f, 0); }
  kstep(f, g, 1); kstep(g, f, 2); kstep(f, g, 3);
#pragma unroll
  for (int n = 0; n < 7; ++n) acc[n] = mfma32(g.b[n], g.a, acc[n]);
  ST(3);
  char* d = gb + (size_t)(tb32 * 224 + nt * 14) * 1024 + l * 16;
#pragma unroll
  for (int n = 0; n < 7; ++n) { unsigned o[8];
#pragma unroll
    for (int q = 0; q < 4; ++q) { const f32x4 u = *(const LDS f32x4*)(sm + K4_UT + (n * 32 + 8 * q + 4 * hi) * 4), tv = *(const LDS f32x4*)(sm + K4_UT + 1024 + (n * 32 + 8 * q + 4 * hi) * 4);
#pragma unroll
      for (int i = 0; i < 2; ++i) { const f32x2 z = rs * (f32x2){acc[n][4 * q + 2 * i], acc[n][4 * q + 2 * i + 1]} + (nm * (f32x2){u[2 * i], u[2 * i + 1]} + (f32x2){tv[2 * i], tv[2 * i + 1]});
        const f32x2 dn = NL2E * (f32x2){EX2(z[0]), EX2(z[1])} + NL2E;
        const f32x2 r = z * (f32x2){__builtin_amdgcn_rcpf(dn[0]), __builtin_amdgcn_rcpf(dn[1])}; o[2 * q + i] = pk(r[0], r[1]); } }
    gstore(d + n * 2048, (u32x4){o[0], o[1], o[2], o[3]}); gstore(d + n * 2048 + 1024, (u32x4){o[4], o[5], o[6], o[7]}); }
  ST_END(4);
}

constexpr int K5_S = 6, K5_LDS = K5_S * GA_STAGE;
extern "C" __global__ __launch_bounds__(512, 2) void k_ffn2(const char* __restrict__ gb, const char* __restrict__ w2, const char* __restrict__ x1b,
                                                 const float* __restrict__ modf, char* __restrict__ out) {
  const lptr sm = (lptr)smem_raw;
  ST_INIT();
  const int b = blockIdx.x, l = threadIdx.x & 63, w = __builtin_amdgcn_readfirstlane(threadIdx.x >> 6);
  const int xcd = b & 7, j = b >> 3, tb = (xcd >> 1) * 4 + (j & 3), nt = (xcd & 1) * 8 + (j >> 2);
  if (w >= 4) { gemmA_load<56, K5_S, DM, 0>(gb + (size_t)tb * 4 * 224 * 1024, w2 + nt * 128, l, w - 4, [] {}); return; }
  const int hi = l >> 5, tb32 = tb * 4 + 2 * (w & 1), n0 = nt * 64 + (w >> 1) * 32;
  u32x4 xr[2][2]; f32x4 g[4];
#pragma unroll
  for (int i = 0; i < 2; ++i)
#pragma unroll
    for (int h = 0; h < 2; ++h) xr[i][h] = *(const u32x4*)(x1b + (size_t)((tb32 + i) * 64 + (n0 >> 4) + h) * 1024 + l * 16);
#pragma unroll
  for (int q = 0; q < 4; ++q) g[q] = *(const f32x4*)(modf + 5 * DM + n0 + 8 * q + 4 * hi);
  f32x16 acc[2]; gemmA_compute<56, K5_S>(sm, l, w, acc[0], acc[1], S_);
#pragma unroll
  for (int i = 0; i < 2; ++i)
#pragma unroll
    for (int h = 0; h < 2; ++h) { u32x4 o;
#pragma unroll
      for (int e = 0; e < 4; ++e) { const int r = 8 * h + 2 * e, q = r >> 2, c = r & 3; o[e] = pk(blo(xr[i][h][e]) + g[q][c] * acc[i][r], bhi(xr[i][h][e]) + g[q][c + 1] * acc[i][r + 1]); }
      gstore(out + (size_t)((tb32 + i) * 32 + (l & 31)) * 2048 + (n0 + 16 * h + 8 * hi) * 2, rowswap(o)); }
  ST_END(5);
}

extern "C" __global__ __launch_bounds__(256) void k_mod(const uint16_t* __restrict__ c, const char* __restrict__ wmod, float* __restrict__ modp,
                                             const char* __restrict__ x, const char* __restrict__ wq, const char* __restrict__ wk, const char* __restrict__ wv) {
  __shared__ float red[4][32];
  ST_INIT();
  const int t = threadIdx.x;
  if (blockIdx.x >= PF0 && blockIdx.x < PF0 + 256) { const int p = blockIdx.x - PF0; u32x4 d[7];
    const char* xb = x + (size_t)((p & 7) * 32 + (p >> 3)) * 16384;
#pragma unroll
    for (int i = 0; i < 4; ++i) d[i] = gload<0>(t * 16, xb + i * 4096);
    d[4] = gload<0>(t * 16, wq + p * 8192); d[5] = gload<0>(t * 16, wq + p * 8192 + 4096);
    d[6] = gload<0>((t & 31) * 16, (p & 128 ? wv : wk) + (p & 127) * 512);
    { CODE_RANGE(k_ln1kv, k_mod, cb, cs) const unsigned o = (p >> 3) * 2048 + t * 16; u32x4 e = gload<0>(t < 128 && o < cs ? o : 0, cb);
      asm volatile("s_waitcnt vmcnt(0)" : "+v"(e)::"memory"); }
    asm volatile("s_waitcnt vmcnt(0)" : "+v"(d[0]), "+v"(d[1]), "+v"(d[2]), "+v"(d[3]), "+v"(d[4]), "+v"(d[5]), "+v"(d[6])::"memory");
    return; }
  const int b = blockIdx.x < PF0 ? blockIdx.x : blockIdx.x - 256, xcd = b & 7, j = b >> 3;
  const int cg = (j & 1) + 2 * (xcd + 8 * ((j >> 1) % 12)), ks = j / 24;
  const int row0 = ks * 256 + (t >> 2);
  const char* p = wmod + (size_t)row0 * (NMOD * 2) + cg * 64 + (t & 3) * 16;
  u32x4 w[4]; float s[4];
#pragma unroll
  for (int i = 0; i < 4; ++i) { w[i] = *(const u32x4*)(p + (size_t)i * 64 * NMOD * 2); s[i] = __uint_as_float((unsigned)c[row0 + 64 * i] << 16); }
  float a[8] = {0, 0, 0, 0, 0, 0, 0, 0};
#pragma unroll
  for (int i = 0; i < 4; ++i) { const float f = rbf(silu(s[i]));
#pragma unroll
    for (int e = 0; e < 4; ++e) { a[2 * e] += f * blo(w[i][e]); a[2 * e + 1] += f * bhi(w[i][e]); } }
#pragma unroll
  for (int e = 0; e < 8; ++e) { float v = a[e]; v += __shfl_xor(v, 4); v += __shfl_xor(v, 8); v = swap16add(v); a[e] = swap32add(v); }
  if ((t & 63) < 4) {
#pragma unroll
    for (int e = 0; e < 8; ++e) red[t >> 6][(t & 3) * 8 + e] = a[e]; }
  __syncthreads();
  if (t < 32) modp[ks * NMOD + cg * 32 + t] = (red[0][t] + red[1][t]) + (red[2][t] + red[3][t]);
  ST_END(0);
}

extern "C" void kernel_launch(void* const* d_in, const int* in_sizes, int n_in, void* d_out, int out_size, void* d_ws, size_t ws_size, hipStream_t stream) {
  const char* x = (const char*)d_in[0]; const char* wq = (const char*)d_in[1]; const char* wk = (const char*)d_in[2]; const char* wv = (const char*)d_in[3];
  const char* wo = (const char*)d_in[4]; const char* w1 = (const char*)d_in[6]; const char* w2 = (const char*)d_in[7];
  const uint16_t* c = (const uint16_t*)d_in[8]; const char* wmod = (const char*)d_in[9];
  char* ws = (char*)d_ws;
  (void)hipFuncSetAttribute((const void*)k_ln1kv, hipFuncAttributeMaxDynamicSharedMemorySize, K1_LDS);
  (void)hipFuncSetAttribute((const void*)k_qattn, hipFuncAttributeMaxDynamicSharedMemorySize, K2_LDS);
  (void)hipFuncSetAttribute((const void*)k_oproj, hipFuncAttributeMaxDynamicSharedMemorySize, K3_LDS);
  (void)hipFuncSetAttribute((const void*)k_ffn1, hipFuncAttributeMaxDynamicSharedMemorySize, K4_LDS);
  (void)hipFuncSetAttribute((const void*)k_ffn2, hipFuncAttributeMaxDynamicSharedMemorySize, K5_LDS);
  float* modp = (float*)(ws + WS_MODP); float* modf = (float*)(ws + WS_MODF);
  k_mod<<<1024, 256, 0, stream>>>(c, wmod, modp, x, wq, wk, wv);
  k_ln1kv<<<256, 512, K1_LDS, stream>>>(x, wk, wv, (const char*)modp, modf, (uint16_t*)(ws + WS_MODH), ws + WS_H, ws + WS_KV);
  k_qattn<<<256, 512, K2_LDS, stream>>>(ws + WS_H, wq, ws + WS_KV, ws + WS_O, w1, ws + WS_MODH, (float*)(ws + WS_UTP), wo, w2);
  k_oproj<<<256, 512, K3_LDS, stream>>>(ws + WS_O, wo, x, modf, ws + WS_X1, ws + WS_Y, (f32x2*)(ws + WS_STAT));
  k_ffn1<<<256, 512, K4_LDS, stream>>>(ws + WS_Y, w1, (const f32x2*)(ws + WS_STAT), (const float*)(ws + WS_UTP), ws + WS_G);
  k_ffn2<<<256, 512, K5_LDS, stream>>>(ws + WS_G, w2, ws + WS_X1, modf, (char*)d_out);
}
```

```cpp
#include <hip/hip_runtime.h>
#include <hip/hip_bf16.h>
#include <stdint.h>
#include <type_traits>

typedef __attribute__((ext_vector_type(2))) float f32x2;
typedef __attribute__((ext_vector_type(4))) float f32x4;
typedef __attribute__((ext_vector_type(16))) float f32x16;
typedef __attribute__((ext_vector_type(2))) __bf16 bf16x2;
typedef __attribute__((ext_vector_type(8))) __bf16 bf16x8;
typedef __attribute__((ext_vector_type(4))) short s16x4;
typedef __attribute__((ext_vector_type(2))) unsigned u32x2;
typedef __attribute__((ext_vector_type(4))) unsigned u32x4;
#define LDS __attribute__((address_space(3)))
typedef LDS char* lptr;
#define PF0 768
#define NEXTPF 1
#define DEV __device__ __forceinline__

constexpr int SEQ = 2048, DM = 1024, FF = 3584, NMOD = 6 * DM;
constexpr size_t WS_MODP = 0;
constexpr size_t WS_UTP = 98304;
constexpr size_t WS_MODF = 212992;
constexpr size_t WS_MODH = 237568;
constexpr size_t WS_STAT = 262144;
constexpr size_t WS_KV = 786432;
constexpr size_t WS_H = (size_t)1 << 20;
constexpr size_t WS_O = (size_t)5 << 20;
constexpr size_t WS_X1 = (size_t)9 << 20;
constexpr size_t WS_Y = (size_t)13 << 20;
constexpr size_t WS_G = (size_t)17 << 20;
constexpr size_t WS_XCH = (size_t)32 << 20;
constexpr size_t WS_FLAG = (size_t)40 << 20;

DEV unsigned pk(float a, float b) { return __builtin_bit_cast(unsigned, __builtin_convertvector((f32x2){a, b}, bf16x2)); }
DEV float rbf(float a) { return __uint_as_float(pk(a, 0.f) << 16); }
DEV float blo(unsigned u) { return __uint_as_float(u << 16); }
DEV float bhi(unsigned u) { return __uint_as_float(u & 0xffff0000u); }
DEV float silu(float z) { return z * __builtin_amdgcn_rcpf(1.f + __builtin_amdgcn_exp2f(-1.4426950408889634f * z)); }
DEV f32x16 mfma32(bf16x8 a, bf16x8 b, f32x16 c) { return __builtin_amdgcn_mfma_f32_32x32x16_bf16(a, b, c, 0, 0, 0); }
DEV f32x4 mfma16(bf16x8 a, bf16x8 b, f32x4 c) { return __builtin_amdgcn_mfma_f32_16x16x32_bf16(a, b, c, 0, 0, 0); }
DEV bf16x8 asb(u32x4 v) { return __builtin_bit_cast(bf16x8, v); }
DEV bf16x8 ldsA(lptr p) { return *(const LDS bf16x8*)p; }
DEV bf16x8 ldsT(lptr p, int second) {
  s16x4 a = __builtin_amdgcn_ds_read_tr16_b64_v4i16((LDS s16x4*)p);
  s16x4 b = __builtin_amdgcn_ds_read_tr16_b64_v4i16((LDS s16x4*)(p + second));
  return __builtin_bit_cast(bf16x8, __builtin_shufflevector(a, b, 0, 1, 2, 3, 4, 5, 6, 7));
}
template <int C> DEV float dppadd(float v) { return v + __int_as_float(__builtin_amdgcn_update_dpp(0, __float_as_int(v), C, 0xf, 0xf, true)); }
DEV float swap32add(float v) { auto r = __builtin_amdgcn_permlane32_swap(__float_as_uint(v), __float_as_uint(v), false, false); return __uint_as_float(r[0]) + __uint_as_float(r[1]); }
DEV float swap16add(float v) { auto r = __builtin_amdgcn_permlane16_swap(__float_as_uint(v), __float_as_uint(v), false, false); return __uint_as_float(r[0]) + __uint_as_float(r[1]); }
DEV float swap32max(float v) { auto r = __builtin_amdgcn_permlane32_swap(__float_as_uint(v), __float_as_uint(v), false, false); return __builtin_fmaxf(__uint_as_float(r[0]), __uint_as_float(r[1])); }
DEV float wavesum(float v) { v = dppadd<0xB1>(v); v = dppadd<0x4E>(v); v = dppadd<0x141>(v); v = dppadd<0x140>(v); v = swap16add(v); return swap32add(v); }

#define SETM0(x) asm volatile("s_mov_b32 m0, %0\n\ts_nop 0" ::"s"(x) : "memory")
template <int OFF> DEV void glds(unsigned voff, const void* base) { asm volatile("global_load_lds_dwordx4 %0, %1 offset:%2" ::"v"(voff), "s"(base), "i"(OFF) : "memory"); }
template <int N> DEV void waitbar() { asm volatile("s_waitcnt vmcnt(%0) lgkmcnt(0)\n\ts_barrier" ::"i"(N) : "memory"); }
template <int OFF> DEV u32x4 gload(unsigned voff, const void* base) { u32x4 r; asm volatile("global_load_dwordx4 %0, %1, %2 offset:%3" : "=v"(r) : "v"(voff), "s"(base), "i"(OFF) : "memory"); return r; }
template <int OFF> DEV u32x2 gload2(unsigned voff, const void* base) { u32x2 r; asm volatile("global_load_dwordx2 %0, %1, %2 offset:%3" : "=v"(r) : "v"(voff), "s"(base), "i"(OFF) : "memory"); return r; }
template <int OFF> DEV unsigned gload1(unsigned voff, const void* base) { unsigned r; asm volatile("global_load_dword %0, %1, %2 offset:%3" : "=v"(r) : "v"(voff), "s"(base), "i"(OFF) : "memory"); return r; }
template <int OFF> DEV u32x4 gloadd(unsigned voff, const void* base) { u32x4 r; asm volatile("global_load_dwordx4 %0, %1, %2 offset:%3 sc1" : "=v"(r) : "v"(voff), "s"(base), "i"(OFF) : "memory"); return r; }
DEV void gstore2(void* p, u32x2 v) { asm volatile("global_store_dwordx2 %0, %1, off sc1" ::"v"(p), "v"(v) : "memory"); }
#define STPOL "sc1"
DEV void gstore(void* p, u32x4 v) { asm volatile("global_store_dwordx4 %0, %1, off " STPOL "\n\ts_nop 1" ::"v"(p), "v"(v) : "memory"); }
#define CODE_RANGE(A, B, base, size) const char* base; unsigned size; { unsigned long long pc_; int o_; \
  asm volatile("s_getpc_b64 %0\n.Lcr%=:\n\ts_mov_b32 %1, " #A "-.Lcr%=\n\ts_mov_b32 %2, " #B "-" #A : "=s"(pc_), "=s"(o_), "=s"(size)); base = (const char*)(pc_ + o_); }
#define SB() __builtin_amdgcn_sched_barrier(0)
extern __shared__ __attribute__((aligned(16))) char smem_raw[];

struct Stamps {};
#define ST_INIT() Stamps S_
#define ST(i)
#define ST_END(k)

constexpr int K1_W = 0, K1_H = 65536, K1_MV = K1_H + 64 * 528, K1_RED = K1_MV + 8192, K1_LDS = K1_RED + 32768;
extern "C" __global__ __launch_bounds__(512, 2) void k_ln1kv(const char* __restrict__ x, const char* __restrict__ wk, const char* __restrict__ wv,
                                                  const char* __restrict__ modp, float* __restrict__ modf, uint16_t* __restrict__ modh,
                                                  char* __restrict__ hbuf, char* __restrict__ kv) {
  const lptr sm = (lptr)smem_raw;
  ST_INIT();
  const int b = blockIdx.x, t = threadIdx.x, l = t & 63, w = __builtin_amdgcn_readfirstlane(t >> 6);
  const int which = (b >> 3) & 1, rb = (b & 7) * 16 + (b >> 4);
  u32x4 xv[4], mp[4];
  { const char* xb = x + (size_t)(rb * 16 + 2 * w) * 2048; const unsigned vo = l * 32;
    xv[0] = gload<0>(vo, xb); xv[1] = gload<16>(vo, xb); xv[2] = gload<2048>(vo, xb); xv[3] = gload<2064>(vo, xb);
#pragma unroll
    for (int s = 0; s < 4; ++s) mp[s] = gload<0>(t * 16, modp + s * NMOD * 4); }
  { const char* wsrc = (which ? wv : wk) + w * 8192;
#pragma unroll
    for (int g = 0; g < 2; ++g) { SETM0(K1_W + w * 8192 + g * 4096); const char* s = wsrc + g * 4096;
      glds<0>(l * 16, s); glds<1024>(l * 16, s); glds<2048>(l * 16, s); glds<3072>(l * 16, s); } }
  asm volatile("s_waitcnt vmcnt(8)" : "+v"(xv[0]), "+v"(xv[1]), "+v"(xv[2]), "+v"(xv[3]), "+v"(mp[0]), "+v"(mp[1]), "+v"(mp[2]), "+v"(mp[3])::"memory");
  ST(1);
  { f32x4 o;
#pragma unroll
    for (int e = 0; e < 4; ++e) { const float v = rbf((__uint_as_float(mp[0][e]) + __uint_as_float(mp[1][e])) + (__uint_as_float(mp[2][e]) + __uint_as_float(mp[3][e])));
      o[e] = t >= 256 ? rbf(1.f + v) : v; }
    *(LDS f32x4*)(sm + K1_MV + (t & 256) * 16 + (t & 3) * 1024 + ((t & 255) >> 2) * 16) = o; }
  f32x2 v[2][8]; float rs[2];
#pragma unroll
  for (int r = 0; r < 2; ++r) { f32x2 s = {0.f, 0.f};
#pragma unroll
    for (int e = 0; e < 8; ++e) { const unsigned u = xv[2 * r + (e >> 2)][e & 3]; v[r][e] = (f32x2){blo(u), bhi(u)}; s += v[r][e]; }
    const float m = wavesum(s[0] + s[1]) * (1.f / 1024.f); f32x2 q = {0.f, 0.f};
#pragma unroll
    for (int e = 0; e < 8; ++e) { v[r][e] -= m; q += v[r][e] * v[r][e]; }
    rs[r] = __builtin_amdgcn_rsqf(wavesum(q[0] + q[1]) * (1.f / 1024.f) + 1e-5f); }
  asm volatile("s_waitcnt lgkmcnt(0)\n\ts_barrier" ::: "memory");
  { unsigned u[2][8];
#pragma unroll
    for (int e = 0; e < 4; ++e) { const f32x4 a = *(const LDS f32x4*)(sm + K1_MV + e * 1024 + l * 16), cc = *(const LDS f32x4*)(sm + K1_MV + 4096 + e * 1024 + l * 16);
#pragma unroll
      for (int r = 0; r < 2; ++r)
#pragma unroll
        for (int i = 0; i < 2; ++i) { const f32x2 h = v[r][2 * e + i] * rs[r] * (f32x2){cc[2 * i], cc[2 * i + 1]} + (f32x2){a[2 * i], a[2 * i + 1]}; u[r][2 * e + i] = pk(h[0], h[1]); } }
#pragma unroll
    for (int r = 0; r < 2; ++r) { const lptr d = sm + K1_H + l * 528 + (2 * w + r) * 16;
      *(LDS u32x4*)d = (u32x4){u[r][0], u[r][1], u[r][4], u[r][5]}; *(LDS u32x4*)(d + 256) = (u32x4){u[r][2], u[r][3], u[r][6], u[r][7]}; } }
  ST(2); waitbar<0>(); ST(3);
  f32x16 acc = {};
  { const int hi = l >> 5; const lptr hp = sm + K1_H + w * 8 * 528 + hi * 256 + (l & 15) * 16;
    const lptr wp = sm + K1_W + (w * 128 + 4 * hi + ((l >> 2) & 3)) * 64 + ((l >> 4) & 1) * 32 + (l & 3) * 8;
    bf16x8 fa[8], fb[8];
#pragma unroll
    for (int i = 0; i < 8; ++i) { fa[i] = ldsA(hp + i * 528); fb[i] = ldsT(wp + i * 1024, 512); }
#pragma unroll
    for (int i = 0; i < 2; ++i) { const int id = t + 512 * i, tok = id & 15, h2 = (id >> 4) & 1, kb = which * 32 + (id >> 5);
      gstore(hbuf + (size_t)((rb >> 1) * 64 + kb) * 1024 + (h2 * 32 + (rb & 1) * 16 + tok) * 16, *(const LDS u32x4*)(sm + K1_H + kb * 528 + h2 * 256 + tok * 16)); }
    if (which == 0) {
#pragma unroll
      for (int i = 0; i < 8; ++i) acc = mfma32(fb[i], fa[i], acc);
    } else {
#pragma unroll
      for (int i = 0; i < 8; ++i) acc = mfma32(fa[i], fb[i], acc);
    } }
#pragma unroll
  for (int q = 0; q < 4; ++q) *(LDS f32x4*)(sm + K1_RED + w * 4096 + q * 1024 + l * 16) = (f32x4){acc[4 * q], acc[4 * q + 1], acc[4 * q + 2], acc[4 * q + 3]};
  asm volatile("s_waitcnt lgkmcnt(0)\n\ts_barrier" ::: "memory");
  if (w == 0) {
    const lptr rp = sm + K1_RED + (which ? l * 16 : (l >> 5) * 2048 + (((l >> 4) & 1) * 32 + (l & 15)) * 16);
    f32x4 a = {}, c = {};
#pragma unroll
    for (int u = 0; u < 8; ++u) { a += *(const LDS f32x4*)(rp + u * 4096); c += *(const LDS f32x4*)(rp + u * 4096 + 1024); }
    char* base = kv + (rb >> 1) * 4096;
    char* d = which ? base + 2048 + (rb & 1) * 1024 + l * 16 : base + (l >> 5) * 1024 + (((l >> 4) & 1) * 32 + (rb & 1) * 16 + (l & 15)) * 16;
    *(u32x4*)d = (u32x4){pk(a[0], a[1]), pk(a[2], a[3]), pk(c[0], c[1]), pk(c[2], c[3])};
  } else if (b < 64 && w == 1) {
    const int col = 2048 + b * 64 + l; const float* mpf = (const float*)modp;
    float v2 = rbf((mpf[col] + mpf[NMOD + col]) + (mpf[2 * NMOD + col] + mpf[3 * NMOD + col]));
    if ((col >> 10) == 4) v2 = rbf(1.f + v2);
    modf[col] = v2; modh[col] = (uint16_t)(__float_as_uint(v2) >> 16);
  }
  ST_END(1);
}

DEV void bar() { asm volatile("s_waitcnt lgkmcnt(0)\n\ts_barrier" ::: "memory"); }
template <int N, int G, int EXTRA> DEV void loader_tail() { waitbar<G * N + EXTRA>(); if constexpr (N > 0) loader_tail<N - 1, G, EXTRA>(); }
constexpr int issued(int t, int S) { return t == 0 ? 2 : (2 + 2 * t < S + t - 1 ? 2 + 2 * t : S + t - 1); }
template <int T, int S, int G, int STAGE, class I, class E> DEV void loader_ramp(I&& issue, E&& early) {
  if constexpr (T == 1) early();
#pragma unroll
  for (int s = T == 0 ? 0 : issued(T - 1, S); s < issued(T, S); ++s) issue(s, (s % S) * STAGE);
  waitbar<G*(issued(T, S) - T - 1)>();
  if constexpr (issued(T, S) < S + T - 1 || T == 0) loader_ramp<T + 1, S, G, STAGE>(issue, early);
}
constexpr int ramp_end(int S) { int t = 1; while (issued(t, S) < S + t - 1) ++t; return t; }
template <int KT, int S, int G, int STAGE, int EXTRA, class I, class X, class E>
DEV void loader(I&& issue, X&& extra, E&& early) {
  loader_ramp<0, S, G, STAGE>(issue, early);
  constexpr int T0 = ramp_end(S);
  int slot = ((S + T0 - 1) % S) * STAGE;
  for (int t = T0; t <= KT - S; ++t) { issue(t - 1 + S, slot); slot = slot == (S - 1) * STAGE ? 0 : slot + STAGE; if (t < KT - S) waitbar<G*(S - 2)>(); }
  extra();
  loader_tail<S - 2, G, EXTRA>();
}
constexpr int GA_STAGE = 24576;
template <int KT, int S, int LDW, int EXTRA, class X>
DEV void gemmA_load(const char* act, const char* wgt, int l, int lw, X&& extra) {
  const char* ap = act + (size_t)lw * (KT * 4096);
  const char* wp = wgt + (size_t)lw * (16 * LDW * 2);
  const unsigned va = l * 16, vb = (l >> 3) * (LDW * 2) + (((l & 7) ^ (((l >> 4) & 1) << 2)) * 16);
  loader<KT, S, 6, GA_STAGE, EXTRA>([&](int t, int slot) {
    SETM0(slot + lw * 4096); const char* a = ap + (size_t)t * 4096; glds<0>(va, a); glds<1024>(va, a); glds<2048>(va, a); glds<3072>(va, a);
    SETM0(slot + 16384 + lw * 2048); const char* s = wp + (size_t)t * (64 * LDW * 2); glds<0>(vb, s); glds<1024>(vb, s + 8 * LDW * 2 - 1024); }, extra, [] {});
}
struct FragA { bf16x8 a0[4], a1[4], b[4]; };
template <int KT, int S>
DEV void gemmA_compute(const lptr sm, int l, int w, f32x16& c0, f32x16& c1, Stamps& S_) {
  const int q = (l >> 2) & 3, p = l & 3;
  const lptr ra = sm + (w & 1) * 8192 + l * 16;
  const lptr rb = sm + 16384 + (4 * (l >> 5) + q) * 128 + ((((w >> 1) * 4 + ((l >> 4) & 1) * 2 + (p >> 1)) ^ ((q >> 1) << 2)) * 16) + (p & 1) * 8;
  c0 = (f32x16){}; c1 = (f32x16){};
  FragA f, g; int slot = 0;
  auto rd = [&](FragA& d, int k) { d.a0[k] = ldsA(ra + slot + k * 1024); d.b[k] = ldsT(rb + slot + k * 2048, 1024); d.a1[k] = ldsA(ra + slot + 4096 + k * 1024); };
  auto step = [&](FragA& cur, FragA& nxt) { bar(); slot = slot == (S - 1) * GA_STAGE ? 0 : slot + GA_STAGE;
#pragma unroll
    for (int k = 0; k < 4; ++k) { if (k < 2) rd(nxt, 2 * k); c0 = mfma32(cur.b[k], cur.a0[k], c0); SB(); if (k < 2) rd(nxt, 2 * k + 1); c1 = mfma32(cur.b[k], cur.a1[k], c1); SB(); } };
  bar(); ST(2);
#pragma unroll
  for (int k = 0; k < 4; ++k) rd(f, k);
  for (int t = 0; t < KT - 2; t += 2) { step(f, g); step(g, f); }
  step(f, g);
#pragma unroll
  for (int k = 0; k < 4; ++k) { c0 = mfma32(g.b[k], g.a0[k], c0); c1 = mfma32(g.b[k], g.a1[k], c1); }
  ST(3);
}

constexpr int K2_KV0 = 98304, K2_KV1 = 131072, K2_KV2 = 0, K2_UW = 32768, K2_Q = 65536, K2_RED = 73728, K2_DUMP = 81920, K2_LDS = 163840;
DEV void pack16(const f32x16& s, u32x4& a, u32x4& b) {
  a = (u32x4){pk(s[0], s[1]), pk(s[2], s[3]), pk(s[4], s[5]), pk(s[6], s[7])};
  b = (u32x4){pk(s[8], s[9]), pk(s[10], s[11]), pk(s[12], s[13]), pk(s[14], s[15])}; }
DEV float max16(const f32x16& s) { float m = s[0];
#pragma unroll
  for (int i = 1; i < 16; ++i) m = __builtin_fmaxf(m, s[i]);
  return swap32max(m); }
#define EX2(v) __builtin_amdgcn_exp2f(v)
#define PIN(x) asm volatile("" : "+v"(x))

extern "C" __global__ __launch_bounds__(512, 2) void k_qattn(const char* __restrict__ hbuf, const char* __restrict__ wq, const char* __restrict__ kv, char* __restrict__ obuf,
                                                  const char* __restrict__ w1, const char* __restrict__ modh, float* __restrict__ utp,
                                                  const char* __restrict__ wo, const char* __restrict__ w2) {
  const lptr sm = (lptr)smem_raw;
  ST_INIT();
  const int b = blockIdx.x, l = threadIdx.x & 63, w = __builtin_amdgcn_readfirstlane(threadIdx.x >> 6);
  const int xcd = b & 7, j = b >> 3, tb = (xcd >> 1) * 4 + (j & 3), hp = (xcd & 1) * 8 + (j >> 2);
  const unsigned l16 = l * 16;
  u32x4 q0u, q1u;
  if (w >= 4) {
    gemmA_load<16, 4, DM, 8>(hbuf + (size_t)tb * 4 * 64 * 1024, wq + hp * 128, l, w - 4, [&] {
#pragma unroll
      for (int g = 0; g < 2; ++g) { SETM0(K2_KV0 + (w - 4) * 8192 + g * 4096); const char* s = kv + (w - 4) * 8192 + g * 4096;
        glds<0>(l16, s); glds<1024>(l16, s); glds<2048>(l16, s); glds<3072>(l16, s); } });
    waitbar<0>();
    q0u = *(const LDS u32x4*)(sm + K2_Q + (w - 4) * 2048 + l16); q1u = *(const LDS u32x4*)(sm + K2_Q + (w - 4) * 2048 + 1024 + l16);
  } else {
    f32x16 c0, c1; gemmA_compute<16, 4>(sm, l, w, c0, c1, S_);
    constexpr float QS = 0.17677669529663687f * 1.4426950408889634f;
    c0 *= QS; c1 *= QS;
    u32x4 a, c; pack16(c1, a, c); *(LDS u32x4*)(sm + K2_Q + w * 2048 + l16) = a; *(LDS u32x4*)(sm + K2_Q + w * 2048 + 1024 + l16) = c;
    pack16(c0, q0u, q1u);
    bar();
  }
  const bf16x8 q0 = asb(q0u), q1 = asb(q1u);
  const char* kvw = kv + w * 4096;
  auto kvload = [&](int st, int slot) { SETM0(slot + w * 4096); const char* s = kvw + st * 32768; glds<0>(l16, s); glds<1024>(l16, s); glds<2048>(l16, s); glds<3072>(l16, s); };
  kvload(1, K2_KV1);
  const unsigned one2 = ((l & 7) == ((l >> 4) & 1) * 4) ? 0x3f803f80u : 0u;
  const bf16x8 ones = asb((u32x4){one2, one2, one2, one2});
  const lptr kp0 = sm + K2_KV0 + l16, kp1 = sm + K2_KV1 + l16, kp2 = sm + K2_KV2 + l16;
  bf16x8 k0 = ldsA(kp0), k1 = ldsA(kp0 + 1024), v0 = ldsA(kp0 + 2048), v1 = ldsA(kp0 + 3072);
  f32x16 negm, sA, sB, o = {}; f32x4 ls = {};
  { f32x16 z = {}; z = mfma32(k0, q0, z); z = mfma32(k1, q1, z); const float m = -max16(z);
#pragma unroll
    for (int i = 0; i < 16; ++i) negm[i] = m; }
  PIN(negm);
  sA = mfma32(k0, q0, negm); sA = mfma32(k1, q1, sA);
  k0 = ldsA(kp0 + 4096); k1 = ldsA(kp0 + 4096 + 1024);
  u32x4 p0, p1 = {0, 0, 0, 0}; bf16x8 v1p = v1;
#define TILE(S, N, KP, VP)                                                                                            \
  { N = mfma32(k0, q0, negm); SB();                                                                                  \
    S[0] = EX2(S[0]); S[1] = EX2(S[1]); S[2] = EX2(S[2]); S[3] = EX2(S[3]); p0[0] = pk(S[0], S[1]); p0[1] = pk(S[2], S[3]); PIN(p0); SB(); \
    o = mfma32(v1p, asb(p1), o); k0 = ldsA(KP); SB();                                                                \
    S[4] = EX2(S[4]); S[5] = EX2(S[5]); S[6] = EX2(S[6]); S[7] = EX2(S[7]); p0[2] = pk(S[4], S[5]); p0[3] = pk(S[6], S[7]); PIN(p0); SB(); \
    ls = mfma16(ones, asb(p1), ls); v1p = v1; v1 = ldsA((VP) + 1024); SB();                                          \
    S[8] = EX2(S[8]); S[9] = EX2(S[9]); PIN(S); SB();                                                                \
    N = mfma32(k1, q1, N); SB();                                                                                     \
    S[10] = EX2(S[10]); S[11] = EX2(S[11]); S[12] = EX2(S[12]); S[13] = EX2(S[13]); p1[0] = pk(S[8], S[9]); p1[1] = pk(S[10], S[11]); PIN(p1); PIN(S); SB(); \
    o = mfma32(v0, asb(p0), o); k1 = ldsA((KP) + 1024); SB();                                                        \
    S[14] = EX2(S[14]); S[15] = EX2(S[15]); p1[2] = pk(S[12], S[13]); p1[3] = pk(S[14], S[15]); PIN(p1); SB();       \
    ls = mfma16(ones, asb(p0), ls); v0 = ldsA(VP); SB(); }
#define STAGE(P, Q, ...)                                                                                              \
  TILE(sA, sB, P + 2 * 4096, P + 1 * 4096 + 2048) TILE(sB, sA, P + 3 * 4096, P + 2 * 4096 + 2048)                     \
  TILE(sA, sB, P + 4 * 4096, P + 3 * 4096 + 2048) TILE(sB, sA, P + 5 * 4096, P + 4 * 4096 + 2048)                     \
  TILE(sA, sB, P + 6 * 4096, P + 5 * 4096 + 2048) TILE(sB, sA, P + 7 * 4096, P + 6 * 4096 + 2048)                     \
  { __VA_ARGS__ }                                                                                                     \
  TILE(sA, sB, Q, P + 7 * 4096 + 2048) TILE(sB, sA, Q + 4096, Q + 2048)
  const int ub = b < 224 ? b : b - 224, cg = ub % 56, ks = ub / 56;
  u32x2 vf[4];
  ST(4);
  STAGE(kp0, kp1, waitbar<0>(); kvload(2, K2_KV2);
        { SETM0(K2_UW + w * 4096); const char* s = w1 + (size_t)(ks * 256 + w * 32) * (FF * 2) + cg * 128;
          const unsigned vo = (l >> 3) * (FF * 2) + (((l & 7) ^ (((l >> 4) & 1) << 2)) * 16);
          glds<0>(vo, s); glds<1024>(vo, s + 8 * FF * 2 - 1024); glds<2048>(vo, s + 16 * FF * 2 - 2048); glds<3072>(vo, s + 24 * FF * 2 - 3072);
          const char* mv = modh + (ks * 256 + w * 32) * 2; const unsigned vm = ((4 - (l & 1)) * DM + 4 * (l >> 5)) * 2;
          vf[0] = gload2<0>(vm, mv); vf[1] = gload2<16>(vm, mv); vf[2] = gload2<32>(vm, mv); vf[3] = gload2<48>(vm, mv); })
  STAGE(kp1, kp2, waitbar<8>(); kvload(3, K2_KV0);)
  STAGE(kp2, kp0, waitbar<0>();
        if (b < 224) { PIN(vf[0]); PIN(vf[1]); PIN(vf[2]); PIN(vf[3]); const int q = (l >> 2) & 3; const int p = l & 3; f32x16 d0 = {}; f32x16 d1 = {};
          const lptr rb = sm + K2_UW + (w * 32 + 4 * (l >> 5) + q) * 128 + (((((l >> 4) & 1) * 2 + (p >> 1)) ^ ((q >> 1) << 2)) * 16) + (p & 1) * 8;
          const lptr rc = sm + K2_UW + (w * 32 + 4 * (l >> 5) + q) * 128 + (((4 + ((l >> 4) & 1) * 2 + (p >> 1)) ^ ((q >> 1) << 2)) * 16) + (p & 1) * 8;
          _Pragma("unroll") for (int kk = 0; kk < 2; ++kk) { const bf16x8 vv = asb((u32x4){vf[2 * kk][0], vf[2 * kk][1], vf[2 * kk + 1][0], vf[2 * kk + 1][1]});
            d0 = mfma32(ldsT(rb + kk * 2048, 1024), vv, d0); d1 = mfma32(ldsT(rc + kk * 2048, 1024), vv, d1); }
          if ((l & 31) < 2) { const lptr d = sm + K2_RED + w * 512 + (l & 31) * 256 + (l >> 5) * 16;
            _Pragma("unroll") for (int i = 0; i < 4; ++i) { *(LDS f32x4*)(d + i * 32) = (f32x4){d0[4 * i], d0[4 * i + 1], d0[4 * i + 2], d0[4 * i + 3]};
              *(LDS f32x4*)(d + 128 + i * 32) = (f32x4){d1[4 * i], d1[4 * i + 1], d1[4 * i + 2], d1[4 * i + 3]}; } } }
        kvload(4, K2_KV1);
        { SETM0(K2_DUMP + w * 1024); glds<0>(l16, wo + b * 8192 + w * 1024); glds<0>(l16, w2 + (size_t)b * 28672 + w * 1024); })
  STAGE(kp0, kp1, waitbar<2>();
        if (b < 224 && w < 2) { float v = 0.f;
          _Pragma("unroll") for (int u = 0; u < 8; ++u) v += *(const LDS float*)(sm + K2_RED + u * 512 + w * 256 + l * 4);
          utp[(ks * 2 + w) * FF + cg * 64 + l] = v; }
        kvload(5, K2_KV2);
        { SETM0(K2_DUMP + w * 1024); glds<0>(l16, w2 + (size_t)b * 28672 + 8192 + w * 1024); glds<0>(l16, w2 + (size_t)b * 28672 + 16384 + w * 1024); })
  STAGE(kp1, kp2, waitbar<2>(); kvload(6, K2_KV0);
        { SETM0(K2_DUMP + w * 1024); glds<0>(l16, w2 + (size_t)b * 28672 + 24576 + (w & 3) * 1024); })
  STAGE(kp2, kp0, waitbar<1>(); kvload(7, K2_KV1);
        if (NEXTPF && w == 0) { CODE_RANGE(k_oproj, k_mod, cb, cs) const unsigned o = j * 1024 + l16; SETM0(K2_DUMP); glds<0>(o < cs ? o : 0, cb); })
  STAGE(kp0, kp1, waitbar<0>();)
  STAGE(kp1, kp2, )
  ST(5);
  o = mfma32(v1p, asb(p1), o); ls = mfma16(ones, asb(p1), ls);
  float lsum = ls[0];
  { float chk = lsum;
#pragma unroll
    for (int i = 0; i < 16; ++i) chk = __builtin_fmaxf(chk, __builtin_fabsf(o[i]) * 0x1p-40f);
    bool bad = !(chk < 0x1p80f);
#pragma unroll
    for (int i = 0; i < 16; ++i) bad |= (o[i] != o[i]);
    bad |= (lsum != lsum);
    if (__any(bad)) {
      float m = -INFINITY; lsum = 0.f; o = (f32x16){};
      for (int t = 0; t < 64; ++t) { const bf16x8* g = (const bf16x8*)(kv + t * 4096) + l;
        f32x16 s = {}; s = mfma32(g[0], q0, s); s = mfma32(g[64], q1, s);
        const float mn = __builtin_fmaxf(m, max16(s)), al = EX2(m - mn); m = mn; float rsum = 0.f;
#pragma unroll
        for (int i = 0; i < 16; ++i) { s[i] = rbf(EX2(s[i] - mn)); rsum += s[i]; o[i] *= al; }
        lsum = lsum * al + swap32add(rsum);
        u32x4 a, c; pack16(s, a, c); o = mfma32(g[128], asb(a), o); o = mfma32(g[192], asb(c), o); } } }
  o *= 1.f / lsum;
  u32x4 o0, o1; pack16(o, o0, o1);
  char* d = obuf + (size_t)((tb * 4 + 2 * (w & 1) + (w >> 2)) * 64 + (hp * 2 + ((w >> 1) & 1)) * 2) * 1024 + l16;
  gstore(d, o0); gstore(d + 1024, o1);
  ST_END(2);
}

DEV u32x4 rowswap(u32x4 v) { auto a = __builtin_amdgcn_permlane32_swap(v[0], v[2], false, false); auto c = __builtin_amdgcn_permlane32_swap(v[1], v[3], false, false);
  return (u32x4){a[0], c[0], a[1], c[1]}; }

constexpr int K3_S = 6, K3_LDS = K3_S * GA_STAGE;
extern "C" __global__ __launch_bounds__(512, 2) void k_oproj(const char* __restrict__ obuf, const char* __restrict__ wo, const char* __restrict__ x, const float* __restrict__ modf,
                                                  char* __restrict__ x1b, char* __restrict__ yb, f32x2* __restrict__ stat) {
  const lptr sm = (lptr)smem_raw;
  ST_INIT();
  const int b = blockIdx.x, l = threadIdx.x & 63, w = __builtin_amdgcn_readfirstlane(threadIdx.x >> 6);
  const int xcd = b & 7, j = b >> 3, tb = (xcd >> 1) * 4 + (j & 3), nt = (xcd & 1) * 8 + (j >> 2);
  if (w >= 4) { u32x4 e; gemmA_load<16, K3_S, DM, NEXTPF>(obuf + (size_t)tb * 4 * 64 * 1024, wo + nt * 128, l, w - 4, [&] {
      if (NEXTPF) { CODE_RANGE(k_ffn1, k_ffn2, cb, cs) const unsigned o = (j & 3) * 4096 + (w - 4) * 1024 + l * 16; e = gload<0>(o < cs ? o : 0, cb); } });
    if (NEXTPF) asm volatile("s_waitcnt vmcnt(0)" : "+v"(e)::"memory"); return; }
  const int hi = l >> 5, tb32 = tb * 4 + 2 * (w & 1), n0 = nt * 64 + (w >> 1) * 32;
  u32x4 xr[2][2]; f32x4 ga[4], cb[4];
#pragma unroll
  for (int i = 0; i < 2; ++i)
#pragma unroll
    for (int h = 0; h < 2; ++h) xr[i][h] = *(const u32x4*)(x + (size_t)((tb32 + i) * 32 + (l & 31)) * 2048 + (n0 + 16 * h + 8 * hi) * 2);
#pragma unroll
  for (int q = 0; q < 4; ++q) { ga[q] = *(const f32x4*)(modf + 2 * DM + n0 + 8 * q + 4 * hi); cb[q] = *(const f32x4*)(modf + 4 * DM + n0 + 8 * q + 4 * hi); }
  f32x16 acc[2]; gemmA_compute<16, K3_S>(sm, l, w, acc[0], acc[1], S_);
#pragma unroll
  for (int i = 0; i < 2; ++i) { unsigned xo[8], yo[8]; float x1[16], s = 0.f;
#pragma unroll
    for (int h = 0; h < 2; ++h) { const u32x4 xs = rowswap(xr[i][h]);
#pragma unroll
      for (int e = 0; e < 4; ++e) { const int r = 8 * h + 2 * e, q = r >> 2, c = r & 3;
        const unsigned u = pk(blo(xs[e]) + ga[q][c] * acc[i][r], bhi(xs[e]) + ga[q][c + 1] * acc[i][r + 1]);
        x1[r] = blo(u); x1[r + 1] = bhi(u); xo[4 * h + e] = u; yo[4 * h + e] = pk(x1[r] * cb[q][c], x1[r + 1] * cb[q][c + 1]); s += x1[r] + x1[r + 1]; } }
    const size_t off = (size_t)((tb32 + i) * 64 + (n0 >> 4)) * 1024 + l * 16;
    gstore(yb + off, (u32x4){yo[0], yo[1], yo[2], yo[3]}); gstore(yb + off + 1024, (u32x4){yo[4], yo[5], yo[6], yo[7]});
    gstore(x1b + off, (u32x4){xo[0], xo[1], xo[2], xo[3]}); gstore(x1b + off + 1024, (u32x4){xo[4], xo[5], xo[6], xo[7]});
    const float mean = swap32add(s) * (1.f / 32.f); float m2 = 0.f;
#pragma unroll
    for (int e = 0; e < 16; ++e) m2 += (x1[e] - mean) * (x1[e] - mean);
    m2 = swap32add(m2);
    if (hi == 0) stat[((tb32 + i) * 32 + (n0 >> 5)) * 32 + l] = (f32x2){mean, m2}; }
  ST_END(3);
}

constexpr int K4_STAGE = 45056, K4_UT = 3 * K4_STAGE, K4_RS = K4_UT + 2048, K4_LDS = K4_RS + 1024;
struct FragB { bf16x8 a[4], b[7]; };
extern "C" __global__ __launch_bounds__(512, 2) void k_ffn1(const char* __restrict__ yb, const char* __restrict__ w1, const char* __restrict__ stat,
                                                            const char* __restrict__ utp, char* __restrict__ gb) {
  const lptr sm = (lptr)smem_raw;
  ST_INIT();
  const int b = blockIdx.x, t = threadIdx.x, l = t & 63, w = __builtin_amdgcn_readfirstlane(t >> 6);
  const int xcd = b & 7, j = b >> 3, tb = (xcd >> 2) * 8 + (j & 7), nt = (xcd & 3) * 4 + (j >> 3);
  constexpr float NL2E = -1.4426950408889634f;
  if (w >= 4) { const int lw = w - 4;
    const char* ap = yb + (size_t)(tb * 4 + lw) * 64 * 1024; const char* wp = w1 + nt * 448;
    unsigned vb[7];
#pragma unroll
    for (int i = 0; i < 7; ++i) { const int c = (lw * 7 + i) * 64 + l, r = c / 28; vb[i] = r * (FF * 2) + ((c - r * 28) ^ ((r >> 1) & 2)) * 16 + 3072 - i * 1024; }
    const unsigned va = l * 16;
    u32x2 st[16]; unsigned up[8]; u32x4 e;
    loader<16, 3, 11, K4_STAGE, 1>([&](int tt, int slot) {
      SETM0(slot + lw * 4096); const char* a = ap + (size_t)tt * 4096;
      glds<0>(va, a); glds<1024>(va, a); glds<2048>(va, a); glds<3072>(va, a);
      SETM0(slot + 16384 + lw * 7168 + 3072); const char* s = wp + (size_t)tt * (64 * FF * 2);
      glds<-3072>(vb[0], s); glds<-2048>(vb[1], s); glds<-1024>(vb[2], s); glds<0>(vb[3], s); glds<1024>(vb[4], s); glds<2048>(vb[5], s); glds<3072>(vb[6], s); },
      [&] {
#pragma unroll
        for (int i = 0; i < 16; ++i) asm volatile("" : "+v"(st[i]));
#pragma unroll
        for (int i = 0; i < 8; ++i) asm volatile("" : "+v"(up[i]));
        float mean = 0.f;
#pragma unroll
        for (int i = 0; i < 16; ++i) mean += __uint_as_float(st[i][0]);
        mean = swap32add(mean) * (1.f / 32.f); float m2 = 0.f;
#pragma unroll
        for (int i = 0; i < 16; ++i) { const float d = __uint_as_float(st[i][0]) - mean; m2 += __uint_as_float(st[i][1]) + 32.f * d * d; }
        const float rs = NL2E * __builtin_amdgcn_rsqf(swap32add(m2) * (1.f / 1024.f) + 1e-5f);
        if (l < 32) *(LDS f32x2*)(sm + K4_RS + (lw * 32 + l) * 8) = (f32x2){rs, -rs * mean};
        auto f = [&](int i) { return __uint_as_float(up[i]); };
        *(LDS float*)(sm + K4_UT + (t - 256) * 4) = (f(0) + f(2)) + (f(4) + f(6)); *(LDS float*)(sm + K4_UT + 1024 + (t - 256) * 4) = NL2E * ((f(1) + f(3)) + (f(5) + f(7)));
        CODE_RANGE(k_ffn2, k_mod, cb, cs) const unsigned o = lw * 1024 + l * 16; e = gload<0>(o < cs ? o : 0, cb); },
      [&] { const char* sp = stat + (size_t)(tb * 4 + lw) * 8192; const unsigned vs = l * 8, vu = (nt * 224 + (t < 480 ? t - 256 : 0)) * 4;
#define S2(i) st[2 * i] = gload2<0>(vs, sp + i * 1024); st[2 * i + 1] = gload2<512>(vs, sp + i * 1024); up[i] = gload1<0>(vu, utp + i * FF * 4);
        S2(0) S2(1) S2(2) S2(3) S2(4) S2(5) S2(6) S2(7)
#undef S2
      });
    asm volatile("s_waitcnt vmcnt(0)" : "+v"(e)::"memory");
    return; }
  const int g = l >> 4, th = w & 1, nh = w >> 1, sw = (g & 1) * 32;
  const lptr ra = sm + th * 8192 + (g >> 1) * 1024 + (g & 1) * 512 + (l & 15) * 16;
  const lptr rb = sm + 16384 + (16 * (g >> 1) + 4 * (g & 1) + ((l >> 2) & 3)) * 448 + (l & 3) * 8;
  const lptr rbe = rb + nh * 256 + sw, rbo = rb + nh * 256 - sw, rb6 = rb + (((6 + nh) * 32) ^ sw);
  f32x4 acc[7][4];
  FragB f, h; int slot = 0;
  auto rd = [&](FragB& d, int i, int ks) {
    if (i < 4) d.a[i] = ldsA(ra + slot + (i >> 1) * 4096 + (i & 1) * 256 + ks * 2048);
    else { const int n = i - 4; d.b[n] = ldsT((n == 6 ? rb6 : (n & 1 ? rbo : rbe) + n * 32) + slot + ks * (32 * 448), 8 * 448); } };
  auto kstep = [&](FragB& cur, FragB& nxt, int ks, auto first) {
#pragma unroll
    for (int n = 0; n < 7; ++n)
#pragma unroll
      for (int i = 0; i < 4; ++i) { const int m = n * 4 + i;
        acc[n][i] = mfma16(cur.b[n], cur.a[i], first ? (f32x4){} : acc[n][i]);
        if (m < 4) rd(nxt, m, ks); else if (m < 18 && !(m & 1)) rd(nxt, 2 + m / 2, ks);
        SB(); } };
  bar(); ST(2);
#pragma unroll
  for (int i = 0; i < 11; ++i) rd(f, i, 0);
  kstep(f, h, 1, std::true_type{}); bar(); slot = K4_STAGE; kstep(h, f, 0, std::false_type{});
  for (int tt = 1; tt < 15; ++tt) { kstep(f, h, 1, std::false_type{}); bar(); slot = slot == 2 * K4_STAGE ? 0 : slot + K4_STAGE; kstep(h, f, 0, std::false_type{}); }
  kstep(f, h, 1, std::false_type{});
#pragma unroll
  for (int n = 0; n < 7; ++n)
#pragma unroll
    for (int i = 0; i < 4; ++i) acc[n][i] = mfma16(h.b[n], h.a[i], acc[n][i]);
  ST(3);
  f32x2 rn[4];
#pragma unroll
  for (int i = 0; i < 4; ++i) rn[i] = *(const LDS f32x2*)(sm + K4_RS + (th * 64 + i * 16 + (l & 15)) * 8);
  auto act = [&](int n, int i) { const int col = ((n < 6 ? 8 * nh + n : 6 + nh) * 16 + 4 * g) * 4;
    const f32x4 u = *(const LDS f32x4*)(sm + K4_UT + col), tv = *(const LDS f32x4*)(sm + K4_UT + 1024 + col); u32x2 o;
#pragma unroll
    for (int e = 0; e < 2; ++e) { const f32x2 z = rn[i][0] * (f32x2){acc[n][i][2 * e], acc[n][i][2 * e + 1]} + (rn[i][1] * (f32x2){u[2 * e], u[2 * e + 1]} + (f32x2){tv[2 * e], tv[2 * e + 1]});
      const f32x2 dn = NL2E * (f32x2){EX2(z[0]), EX2(z[1])} + NL2E;
      const f32x2 r = z * (f32x2){__builtin_amdgcn_rcpf(dn[0]), __builtin_amdgcn_rcpf(dn[1])}; o[e] = pk(r[0], r[1]); }
    return o; };
  auto dst = [&](int gi, int i) { const int kk = nt * 7 + gi; return gb + (size_t)(((tb * 56 + (kk >> 1)) * 8 + th * 4 + i) * 2 + (kk & 1)) * 1024 + l * 16; };
#pragma unroll
  for (int p = 0; p < 3; ++p)
#pragma unroll
    for (int i = 0; i < 4; ++i) { const u32x2 lo = act(2 * p, i), hi = act(2 * p + 1, i); gstore(dst(4 * nh + p, i), (u32x4){lo[0], lo[1], hi[0], hi[1]}); }
#pragma unroll
  for (int i = 0; i < 4; ++i) gstore2(dst(3, i) + nh * 8, act(6, i));
  ST_END(4);
}

constexpr int K5_STAGE = 32768, K5_S = 5, K5_LDS = K5_S * K5_STAGE;
struct FragC { bf16x8 a[4], b[4]; };
extern "C" __global__ __launch_bounds__(512, 2) void k_ffn2(const char* __restrict__ gb, const char* __restrict__ w2, const char* __restrict__ x1b,
                                                            const float* __restrict__ modf, char* __restrict__ out, char* xch, unsigned* flag) {
  const lptr sm = (lptr)smem_raw;
  ST_INIT();
  const int b = blockIdx.x, l = threadIdx.x & 63, w = __builtin_amdgcn_readfirstlane(threadIdx.x >> 6);
  const int kh = b & 1, j = b >> 3, tb = ((b & 7) >> 1) * 4 + (j & 3), nt = j >> 2;
  if (w >= 4) { const int lw = w - 4;
    const char* ap = gb + (size_t)(tb * 56 + kh * 28) * 16384 + lw * 4096; const char* wp = w2 + (size_t)(kh * 1792 + lw * 16) * 2048 + nt * 256;
    const unsigned va = l * 16, v0 = (l >> 4) * 2048 + ((l & 15) ^ ((l >> 4) * 2)) * 16, v1 = v0 ^ 128;
    loader<28, K5_S, 8, K5_STAGE, 0>([&](int t, int slot) {
      SETM0(slot + lw * 4096); const char* a = ap + (size_t)t * 16384; glds<0>(va, a); glds<1024>(va, a); glds<2048>(va, a); glds<3072>(va, a);
      SETM0(slot + 16384 + lw * 4096); const char* s = wp + (size_t)t * (64 * 2048);
      glds<0>(v0, s); glds<1024>(v1, s + 8192 - 1024); glds<2048>(v0, s + 16384 - 2048); glds<3072>(v1, s + 24576 - 3072); }, [] {}, [] {});
    return; }
  const int g = l >> 4, th = w & 1, nh = w >> 1, rx = 4 * (g & 1) + ((l >> 2) & 3);
  const int tok0 = tb * 128 + th * 64 + kh * 32, n0 = nt * 128 + nh * 64;
  u32x2 xr[2][4]; f32x4 gv[4];
#pragma unroll
  for (int n = 0; n < 4; ++n) { gv[n] = *(const f32x4*)(modf + 5 * DM + n0 + 16 * n + 4 * g);
#pragma unroll
    for (int i = 0; i < 2; ++i) xr[i][n] = *(const u32x2*)(x1b + (size_t)((tok0 >> 5) * 64 + (n0 >> 4) + n) * 1024 + (g & 1) * 512 + (i * 16 + (l & 15)) * 16 + (g >> 1) * 8); }
  const lptr rk = sm + th * 8192 + kh * 4096 + l * 16, rs = sm + th * 8192 + (kh ^ 1) * 4096 + l * 16;
  lptr rb[4];
#pragma unroll
  for (int n = 0; n < 4; ++n) rb[n] = sm + 16384 + (4 * g + ((l >> 2) & 3)) * 256 + ((4 * nh + n) ^ rx) * 32 + (l & 3) * 8;
  f32x4 acc[4][4];
  FragC f, h; int slot = 0;
  auto rd = [&](FragC& d, int i, int ks) {
    if (i < 4) d.a[i] = ldsA((i < 2 ? rk : rs) + slot + (i & 1) * 2048 + ks * 1024); else d.b[i - 4] = ldsT(rb[i - 4] + slot + ks * 8192, 4096); };
  auto kstep = [&](FragC& cur, FragC& nxt, int ks, auto first) {
#pragma unroll
    for (int n = 0; n < 4; ++n)
#pragma unroll
      for (int i = 0; i < 4; ++i) { const int m = n * 4 + i;
        acc[n][i] = mfma16(cur.b[n], cur.a[i], first ? (f32x4){} : acc[n][i]);
        if (m < 4) rd(nxt, m, ks); else if (m < 12 && !(m & 1)) rd(nxt, 2 + m / 2, ks);
        SB(); } };
  bar(); ST(2);
#pragma unroll
  for (int i = 0; i < 8; ++i) rd(f, i, 0);
  kstep(f, h, 1, std::true_type{}); bar(); slot = K5_STAGE; kstep(h, f, 0, std::false_type{});
  for (int tt = 1; tt < 27; ++tt) { kstep(f, h, 1, std::false_type{}); bar(); slot = slot == (K5_S - 1) * K5_STAGE ? 0 : slot + K5_STAGE; kstep(h, f, 0, std::false_type{}); }
  kstep(f, h, 1, std::false_type{});
#pragma unroll
  for (int i = 3; i >= 0; --i)
#pragma unroll
    for (int n = 0; n < 4; ++n) acc[n][i] = mfma16(h.b[n], h.a[i], acc[n][i]);
  ST(3);
  { char* d = xch + (size_t)(b * 4 + w) * 8192 + l * 16;
    asm volatile("s_nop 7\n\ts_nop 7" : "+v"(acc[0][2]), "+v"(acc[1][2]), "+v"(acc[2][2]), "+v"(acc[3][2]), "+v"(acc[0][3]), "+v"(acc[1][3]), "+v"(acc[2][3]), "+v"(acc[3][3]));
#pragma unroll
    for (int i = 0; i < 2; ++i)
#pragma unroll
      for (int n = 0; n < 4; ++n) gstore(d + (i * 4 + n) * 1024, __builtin_bit_cast(u32x4, acc[n][2 + i]));
    asm volatile("s_waitcnt vmcnt(0)" ::: "memory");
    if (l == 0) __hip_atomic_store(flag + (b * 4 + w) * 32, 1u, __ATOMIC_RELAXED, __HIP_MEMORY_SCOPE_AGENT); }
  ST(4);
  { const unsigned* fp = flag + ((b ^ 1) * 4 + w) * 32;
    for (int spin = 0; spin < (1 << 22); ++spin) { if (__builtin_amdgcn_readfirstlane(__hip_atomic_load(fp, __ATOMIC_RELAXED, __HIP_MEMORY_SCOPE_AGENT))) break; __builtin_amdgcn_s_sleep(1); } }
  ST(5);
  u32x4 pr[2][4];
  { const char* s = xch + (size_t)((b ^ 1) * 4 + w) * 8192; const unsigned vo = l * 16;
    pr[0][0] = gloadd<0>(vo, s); pr[0][1] = gloadd<1024>(vo, s); pr[0][2] = gloadd<2048>(vo, s); pr[0][3] = gloadd<3072>(vo, s); s += 4096;
    pr[1][0] = gloadd<0>(vo, s); pr[1][1] = gloadd<1024>(vo, s); pr[1][2] = gloadd<2048>(vo, s); pr[1][3] = gloadd<3072>(vo, s);
    asm volatile("s_waitcnt vmcnt(0)" : "+v"(pr[0][0]), "+v"(pr[0][1]), "+v"(pr[0][2]), "+v"(pr[0][3]), "+v"(pr[1][0]), "+v"(pr[1][1]), "+v"(pr[1][2]), "+v"(pr[1][3])::"memory"); }
  ST(6);
#pragma unroll
  for (int i = 0; i < 2; ++i)
#pragma unroll
    for (int p = 0; p < 2; ++p) { unsigned o[2][2];
#pragma unroll
      for (int q = 0; q < 2; ++q) { const int n = 2 * p + q; const f32x4 a = acc[n][i] + __builtin_bit_cast(f32x4, pr[i][n]);
        o[q][0] = pk(blo(xr[i][n][0]) + gv[n][0] * a[0], bhi(xr[i][n][0]) + gv[n][1] * a[1]); o[q][1] = pk(blo(xr[i][n][1]) + gv[n][2] * a[2], bhi(xr[i][n][1]) + gv[n][3] * a[3]); }
      auto r0 = __builtin_amdgcn_permlane16_swap(o[0][0], o[1][0], false, false); auto r1 = __builtin_amdgcn_permlane16_swap(o[0][1], o[1][1], false, false);
      gstore(out + (size_t)(tok0 + i * 16 + (l & 15)) * 2048 + (n0 + 32 * p + 16 * (g & 1) + 8 * (g >> 1)) * 2, (u32x4){r0[0], r1[0], r0[1], r1[1]}); }
  ST_END(5);
}

extern "C" __global__ __launch_bounds__(256) void k_mod(const uint16_t* __restrict__ c, const char* __restrict__ wmod, float* __restrict__ modp,
                                             const char* __restrict__ x, const char* __restrict__ wq, const char* __restrict__ wk, const char* __restrict__ wv, unsigned* __restrict__ flag) {
  __shared__ float red[4][32];
  ST_INIT();
  const int t = threadIdx.x;
  if (blockIdx.x >= PF0 && blockIdx.x < PF0 + 256) { const int p = blockIdx.x - PF0; u32x4 d[7];
    if (t < 4) flag[(p * 4 + t) * 32] = 0;
    const char* xb = x + (size_t)((p & 7) * 32 + (p >> 3)) * 16384;
#pragma unroll
    for (int i = 0; i < 4; ++i) d[i] = gload<0>(t * 16, xb + i * 4096);
    d[4] = gload<0>(t * 16, wq + p * 8192); d[5] = gload<0>(t * 16, wq + p * 8192 + 4096);
    d[6] = gload<0>((t & 31) * 16, (p & 128 ? wv : wk) + (p & 127) * 512);
    { CODE_RANGE(k_ln1kv, k_mod, cb, cs) const unsigned o = (p >> 3) * 2048 + t * 16; u32x4 e = gload<0>(t < 128 && o < cs ? o : 0, cb);
      asm volatile("s_waitcnt vmcnt(0)" : "+v"(e)::"memory"); }
    asm volatile("s_waitcnt vmcnt(0)" : "+v"(d[0]), "+v"(d[1]), "+v"(d[2]), "+v"(d[3]), "+v"(d[4]), "+v"(d[5]), "+v"(d[6])::"memory");
    return; }
  const int b = blockIdx.x < PF0 ? blockIdx.x : blockIdx.x - 256, xcd = b & 7, j = b >> 3;
  const int cg = (j & 1) + 2 * (xcd + 8 * ((j >> 1) % 12)), ks = j / 24;
  const int row0 = ks * 256 + (t >> 2);
  const char* p = wmod + (size_t)row0 * (NMOD * 2) + cg * 64 + (t & 3) * 16;
  u32x4 w[4]; float s[4];
#pragma unroll
  for (int i = 0; i < 4; ++i) { w[i] = *(const u32x4*)(p + (size_t)i * 64 * NMOD * 2); s[i] = __uint_as_float((unsigned)c[row0 + 64 * i] << 16); }
  float a[8] = {0, 0, 0, 0, 0, 0, 0, 0};
#pragma unroll
  for (int i = 0; i < 4; ++i) { const float f = rbf(silu(s[i]));
#pragma unroll
    for (int e = 0; e < 4; ++e) { a[2 * e] += f * blo(w[i][e]); a[2 * e + 1] += f * bhi(w[i][e]); } }
#pragma unroll
  for (int e = 0; e < 8; ++e) { float v = a[e]; v += __shfl_xor(v, 4); v += __shfl_xor(v, 8); v = swap16add(v); a[e] = swap32add(v); }
  if ((t & 63) < 4) {
#pragma unroll
    for (int e = 0; e < 8; ++e) red[t >> 6][(t & 3) * 8 + e] = a[e]; }
  __syncthreads();
  if (t < 32) modp[ks * NMOD + cg * 32 + t] = (red[0][t] + red[1][t]) + (red[2][t] + red[3][t]);
  ST_END(0);
}

extern "C" void kernel_launch(void* const* d_in, const int* in_sizes, int n_in, void* d_out, int out_size, void* d_ws, size_t ws_size, hipStream_t stream) {
  const char* x = (const char*)d_in[0]; const char* wq = (const char*)d_in[1]; const char* wk = (const char*)d_in[2]; const char* wv = (const char*)d_in[3];
  const char* wo = (const char*)d_in[4]; const char* w1 = (const char*)d_in[6]; const char* w2 = (const char*)d_in[7];
  const uint16_t* c = (const uint16_t*)d_in[8]; const char* wmod = (const char*)d_in[9];
  char* ws = (char*)d_ws;
  (void)hipFuncSetAttribute((const void*)k_ln1kv, hipFuncAttributeMaxDynamicSharedMemorySize, K1_LDS);
  (void)hipFuncSetAttribute((const void*)k_qattn, hipFuncAttributeMaxDynamicSharedMemorySize, K2_LDS);
  (void)hipFuncSetAttribute((const void*)k_oproj, hipFuncAttributeMaxDynamicSharedMemorySize, K3_LDS);
  (void)hipFuncSetAttribute((const void*)k_ffn1, hipFuncAttributeMaxDynamicSharedMemorySize, K4_LDS);
  (void)hipFuncSetAttribute((const void*)k_ffn2, hipFuncAttributeMaxDynamicSharedMemorySize, K5_LDS);
  float* modp = (float*)(ws + WS_MODP); float* modf = (float*)(ws + WS_MODF);
  k_mod<<<1024, 256, 0, stream>>>(c, wmod, modp, x, wq, wk, wv, (unsigned*)(ws + WS_FLAG));
  k_ln1kv<<<256, 512, K1_LDS, stream>>>(x, wk, wv, (const char*)modp, modf, (uint16_t*)(ws + WS_MODH), ws + WS_H, ws + WS_KV);
  k_qattn<<<256, 512, K2_LDS, stream>>>(ws + WS_H, wq, ws + WS_KV, ws + WS_O, w1, ws + WS_MODH, (float*)(ws + WS_UTP), wo, w2);
  k_oproj<<<256, 512, K3_LDS, stream>>>(ws + WS_O, wo, x, modf, ws + WS_X1, ws + WS_Y, (f32x2*)(ws + WS_STAT));
  k_ffn1<<<256, 512, K4_LDS, stream>>>(ws + WS_Y, w1, ws + WS_STAT, ws + WS_UTP, ws + WS_G);
  k_ffn2<<<256, 512, K5_LDS, stream>>>(ws + WS_G, w2, ws + WS_X1, modf, (char*)d_out, ws + WS_XCH, (unsigned*)(ws + WS_FLAG));
}
```

```cpp
#include <hip/hip_runtime.h>
#include <hip/hip_bf16.h>
#include <stdint.h>
#include <type_traits>

typedef __attribute__((ext_vector_type(2))) float f32x2;
typedef __attribute__((ext_vector_type(4))) float f32x4;
typedef __attribute__((ext_vector_type(16))) float f32x16;
typedef __attribute__((ext_vector_type(2))) __bf16 bf16x2;
typedef __attribute__((ext_vector_type(8))) __bf16 bf16x8;
typedef __attribute__((ext_vector_type(4))) short s16x4;
typedef __attribute__((ext_vector_type(2))) unsigned u32x2;
typedef __attribute__((ext_vector_type(4))) unsigned u32x4;
#define LDS __attribute__((address_space(3)))
typedef LDS char* lptr;
#define PF0 768
#define NEXTPF 1
#define DEV __device__ __forceinline__

constexpr int SEQ = 2048, DM = 1024, FF = 3584, NMOD = 6 * DM;
constexpr size_t WS_MODP = 0;
constexpr size_t WS_UTP = 98304;
constexpr size_t WS_MODF = 212992;
constexpr size_t WS_MODH = 237568;
constexpr size_t WS_STAT = 262144;
constexpr size_t WS_KV = 786432;
constexpr size_t WS_H = (size_t)1 << 20;
constexpr size_t WS_O = (size_t)5 << 20;
constexpr size_t WS_X1 = (size_t)9 << 20;
constexpr size_t WS_Y = (size_t)13 << 20;
constexpr size_t WS_G = (size_t)17 << 20;
constexpr size_t WS_XCH = (size_t)32 << 20;

DEV unsigned pk(float a, float b) { return __builtin_bit_cast(unsigned, __builtin_convertvector((f32x2){a, b}, bf16x2)); }
DEV float rbf(float a) { return __uint_as_float(pk(a, 0.f) << 16); }
DEV float blo(unsigned u) { return __uint_as_float(u << 16); }
DEV float bhi(unsigned u) { return __uint_as_float(u & 0xffff0000u); }
DEV float silu(float z) { return z * __builtin_amdgcn_rcpf(1.f + __builtin_amdgcn_exp2f(-1.4426950408889634f * z)); }
DEV f32x16 mfma32(bf16x8 a, bf16x8 b, f32x16 c) { return __builtin_amdgcn_mfma_f32_32x32x16_bf16(a, b, c, 0, 0, 0); }
DEV f32x4 mfma16(bf16x8 a, bf16x8 b, f32x4 c) { return __builtin_amdgcn_mfma_f32_16x16x32_bf16(a, b, c, 0, 0, 0); }
DEV bf16x8 asb(u32x4 v) { return __builtin_bit_cast(bf16x8, v); }
DEV bf16x8 ldsA(lptr p) { return *(const LDS bf16x8*)p; }
DEV bf16x8 ldsT(lptr p, int second) {
  s16x4 a = __builtin_amdgcn_ds_read_tr16_b64_v4i16((LDS s16x4*)p);
  s16x4 b = __builtin_amdgcn_ds_read_tr16_b64_v4i16((LDS s16x4*)(p + second));
  return __builtin_bit_cast(bf16x8, __builtin_shufflevector(a, b, 0, 1, 2, 3, 4, 5, 6, 7));
}
DEV s16x4 ldsT1(lptr p) { return __builtin_amdgcn_ds_read_tr16_b64_v4i16((LDS s16x4*)p); }
DEV bf16x8 cat(s16x4 a, s16x4 b) { return __builtin_bit_cast(bf16x8, __builtin_shufflevector(a, b, 0, 1, 2, 3, 4, 5, 6, 7)); }
template <int C> DEV float dppadd(float v) { return v + __int_as_float(__builtin_amdgcn_update_dpp(0, __float_as_int(v), C, 0xf, 0xf, true)); }
DEV float swap32add(float v) { auto r = __builtin_amdgcn_permlane32_swap(__float_as_uint(v), __float_as_uint(v), false, false); return __uint_as_float(r[0]) + __uint_as_float(r[1]); }
DEV float swap16add(float v) { auto r = __builtin_amdgcn_permlane16_swap(__float_as_uint(v), __float_as_uint(v), false, false); return __uint_as_float(r[0]) + __uint_as_float(r[1]); }
DEV float swap32max(float v) { auto r = __builtin_amdgcn_permlane32_swap(__float_as_uint(v), __float_as_uint(v), false, false); return __builtin_fmaxf(__uint_as_float(r[0]), __uint_as_float(r[1])); }
DEV float wavesum(float v) { v = dppadd<0xB1>(v); v = dppadd<0x4E>(v); v = dppadd<0x141>(v); v = dppadd<0x140>(v); v = swap16add(v); return swap32add(v); }

#define SETM0(x) asm volatile("s_mov_b32 m0, %0\n\ts_nop 0" ::"s"(x) : "memory")
template <int OFF> DEV void glds(unsigned voff, const void* base) { asm volatile("global_load_lds_dwordx4 %0, %1 offset:%2" ::"v"(voff), "s"(base), "i"(OFF) : "memory"); }
template <int N> DEV void waitbar() { asm volatile("s_waitcnt vmcnt(%0) lgkmcnt(0)\n\ts_barrier" ::"i"(N) : "memory"); }
template <int OFF> DEV u32x4 gload(unsigned voff, const void* base) { u32x4 r; asm volatile("global_load_dwordx4 %0, %1, %2 offset:%3" : "=v"(r) : "v"(voff), "s"(base), "i"(OFF) : "memory"); return r; }
template <int OFF> DEV u32x2 gload2(unsigned voff, const void* base) { u32x2 r; asm volatile("global_load_dwordx2 %0, %1, %2 offset:%3" : "=v"(r) : "v"(voff), "s"(base), "i"(OFF) : "memory"); return r; }
template <int OFF> DEV unsigned gload1(unsigned voff, const void* base) { unsigned r; asm volatile("global_load_dword %0, %1, %2 offset:%3" : "=v"(r) : "v"(voff), "s"(base), "i"(OFF) : "memory"); return r; }
template <int OFF> DEV u32x4 gloadd(unsigned voff, const void* base) { u32x4 r; asm volatile("global_load_dwordx4 %0, %1, %2 offset:%3 sc1" : "=v"(r) : "v"(voff), "s"(base), "i"(OFF) : "memory"); return r; }
DEV void gstore2(void* p, u32x2 v) { asm volatile("global_store_dwordx2 %0, %1, off sc1" ::"v"(p), "v"(v) : "memory"); }
#define STPOL "sc1"
DEV void gstore(void* p, u32x4 v) { asm volatile("global_store_dwordx4 %0, %1, off " STPOL "\n\ts_nop 1" ::"v"(p), "v"(v) : "memory"); }
#define CODE_RANGE(A, B, base, size) const char* base; unsigned size; { unsigned long long pc_; int o_; \
  asm volatile("s_getpc_b64 %0\n.Lcr%=:\n\ts_mov_b32 %1, " #A "-.Lcr%=\n\ts_mov_b32 %2, " #B "-" #A : "=s"(pc_), "=s"(o_), "=s"(size)); base = (const char*)(pc_ + o_); }
#define SB() __builtin_amdgcn_sched_barrier(0)
extern __shared__ __attribute__((aligned(16))) char smem_raw[];

struct Stamps {};
#define ST_INIT() Stamps S_
#define ST(i)
#define ST_END(k)

constexpr int K1_W = 0, K1_H = 65536, K1_MV = K1_H + 64 * 528, K1_RED = K1_MV + 8192, K1_LDS = K1_RED + 32768;
extern "C" __global__ __launch_bounds__(512, 2) void k_ln1kv(const char* __restrict__ x, const char* __restrict__ wk, const char* __restrict__ wv,
                                                  const char* __restrict__ modp, float* __restrict__ modf, uint16_t* __restrict__ modh,
                                                  char* __restrict__ hbuf, char* __restrict__ kv) {
  const lptr sm = (lptr)smem_raw;
  ST_INIT();
  const int b = blockIdx.x, t = threadIdx.x, l = t & 63, w = __builtin_amdgcn_readfirstlane(t >> 6);
  const int which = (b >> 3) & 1, rb = (b & 7) * 16 + (b >> 4);
  u32x4 xv[4], mp[4];
  { const char* xb = x + (size_t)(rb * 16 + 2 * w) * 2048; const unsigned vo = l * 32;
    xv[0] = gload<0>(vo, xb); xv[1] = gload<16>(vo, xb); xv[2] = gload<2048>(vo, xb); xv[3] = gload<2064>(vo, xb);
#pragma unroll
    for (int s = 0; s < 4; ++s) mp[s] = gload<0>(t * 16, modp + s * NMOD * 4); }
  { const char* wsrc = (which ? wv : wk) + w * 8192;
#pragma unroll
    for (int g = 0; g < 2; ++g) { SETM0(K1_W + w * 8192 + g * 4096); const char* s = wsrc + g * 4096;
      glds<0>(l * 16, s); glds<1024>(l * 16, s); glds<2048>(l * 16, s); glds<3072>(l * 16, s); } }
  asm volatile("s_waitcnt vmcnt(8)" : "+v"(xv[0]), "+v"(xv[1]), "+v"(xv[2]), "+v"(xv[3]), "+v"(mp[0]), "+v"(mp[1]), "+v"(mp[2]), "+v"(mp[3])::"memory");
  ST(1);
  { f32x4 o;
#pragma unroll
    for (int e = 0; e < 4; ++e) { const float v = rbf((__uint_as_float(mp[0][e]) + __uint_as_float(mp[1][e])) + (__uint_as_float(mp[2][e]) + __uint_as_float(mp[3][e])));
      o[e] = t >= 256 ? rbf(1.f + v) : v; }
    *(LDS f32x4*)(sm + K1_MV + (t & 256) * 16 + (t & 3) * 1024 + ((t & 255) >> 2) * 16) = o; }
  f32x2 v[2][8]; float rs[2];
#pragma unroll
  for (int r = 0; r < 2; ++r) { f32x2 s = {0.f, 0.f};
#pragma unroll
    for (int e = 0; e < 8; ++e) { const unsigned u = xv[2 * r + (e >> 2)][e & 3]; v[r][e] = (f32x2){blo(u), bhi(u)}; s += v[r][e]; }
    const float m = wavesum(s[0] + s[1]) * (1.f / 1024.f); f32x2 q = {0.f, 0.f};
#pragma unroll
    for (int e = 0; e < 8; ++e) { v[r][e] -= m; q += v[r][e] * v[r][e]; }
    rs[r] = __builtin_amdgcn_rsqf(wavesum(q[0] + q[1]) * (1.f / 1024.f) + 1e-5f); }
  asm volatile("s_waitcnt lgkmcnt(0)\n\ts_barrier" ::: "memory");
  { unsigned u[2][8];
#pragma unroll
    for (int e = 0; e < 4; ++e) { const f32x4 a = *(const LDS f32x4*)(sm + K1_MV + e * 1024 + l * 16), cc = *(const LDS f32x4*)(sm + K1_MV + 4096 + e * 1024 + l * 16);
#pragma unroll
      for (int r = 0; r < 2; ++r)
#pragma unroll
        for (int i = 0; i < 2; ++i) { const f32x2 h = v[r][2 * e + i] * rs[r] * (f32x2){cc[2 * i], cc[2 * i + 1]} + (f32x2){a[2 * i], a[2 * i + 1]}; u[r][2 * e + i] = pk(h[0], h[1]); } }
#pragma unroll
    for (int r = 0; r < 2; ++r) { const lptr d = sm + K1_H + l * 528 + (2 * w + r) * 16;
      *(LDS u32x4*)d = (u32x4){u[r][0], u[r][1], u[r][4], u[r][5]}; *(LDS u32x4*)(d + 256) = (u32x4){u[r][2], u[r][3], u[r][6], u[r][7]}; } }
  ST(2); waitbar<0>(); ST(3);
  f32x16 acc = {};
  { const int hi = l >> 5; const lptr hp = sm + K1_H + w * 8 * 528 + hi * 256 + (l & 15) * 16;
    const lptr wp = sm + K1_W + (w * 128 + 4 * hi + ((l >> 2) & 3)) * 64 + ((l >> 4) & 1) * 32 + (l & 3) * 8;
    bf16x8 fa[8], fb[8];
#pragma unroll
    for (int i = 0; i < 8; ++i) { fa[i] = ldsA(hp + i * 528); fb[i] = ldsT(wp + i * 1024, 512); }
#pragma unroll
    for (int i = 0; i < 2; ++i) { const int id = t + 512 * i, tok = id & 15, h2 = (id >> 4) & 1, kb = which * 32 + (id >> 5);
      gstore(hbuf + (size_t)((rb >> 1) * 64 + kb) * 1024 + (h2 * 32 + (rb & 1) * 16 + tok) * 16, *(const LDS u32x4*)(sm + K1_H + kb * 528 + h2 * 256 + tok * 16)); }
    if (which == 0) {
#pragma unroll
      for (int i = 0; i < 8; ++i) acc = mfma32(fb[i], fa[i], acc);
    } else {
#pragma unroll
      for (int i = 0; i < 8; ++i) acc = mfma32(fa[i], fb[i], acc);
    } }
#pragma unroll
  for (int q = 0; q < 4; ++q) *(LDS f32x4*)(sm + K1_RED + w * 4096 + q * 1024 + l * 16) = (f32x4){acc[4 * q], acc[4 * q + 1], acc[4 * q + 2], acc[4 * q + 3]};
  asm volatile("s_waitcnt lgkmcnt(0)\n\ts_barrier" ::: "memory");
  if (w == 0) {
    const lptr rp = sm + K1_RED + (which ? l * 16 : (l >> 5) * 2048 + (((l >> 4) & 1) * 32 + (l & 15)) * 16);
    f32x4 a = {}, c = {};
#pragma unroll
    for (int u = 0; u < 8; ++u) { a += *(const LDS f32x4*)(rp + u * 4096); c += *(const LDS f32x4*)(rp + u * 4096 + 1024); }
    char* base = kv + (rb >> 1) * 4096;
    char* d = which ? base + 2048 + (rb & 1) * 1024 + l * 16 : base + (l >> 5) * 1024 + (((l >> 4) & 1) * 32 + (rb & 1) * 16 + (l & 15)) * 16;
    *(u32x4*)d = (u32x4){pk(a[0], a[1]), pk(a[2], a[3]), pk(c[0], c[1]), pk(c[2], c[3])};
  } else if (b < 64 && w == 1) {
    const int col = 2048 + b * 64 + l; const float* mpf = (const float*)modp;
    float v2 = rbf((mpf[col] + mpf[NMOD + col]) + (mpf[2 * NMOD + col] + mpf[3 * NMOD + col]));
    if ((col >> 10) == 4) v2 = rbf(1.f + v2);
    modf[col] = v2; modh[col] = (uint16_t)(__float_as_uint(v2) >> 16);
  }
  ST_END(1);
}

DEV void bar() { asm volatile("s_waitcnt lgkmcnt(0)\n\ts_barrier" ::: "memory"); }
template <int N, int G, int EXTRA> DEV void loader_tail() { waitbar<G * N + EXTRA>(); if constexpr (N > 0) loader_tail<N - 1, G, EXTRA>(); }
constexpr int issued(int t, int S) { return t == 0 ? 2 : (2 + 2 * t < S + t - 1 ? 2 + 2 * t : S + t - 1); }
template <int T, int S, int G, int STAGE, class I, class E> DEV void loader_ramp(I&& issue, E&& early) {
  if constexpr (T == 1) early();
#pragma unroll
  for (int s = T == 0 ? 0 : issued(T - 1, S); s < issued(T, S); ++s) issue(s, (s % S) * STAGE);
  waitbar<G*(issued(T, S) - T - 1)>();
  if constexpr (issued(T, S) < S + T - 1 || T == 0) loader_ramp<T + 1, S, G, STAGE>(issue, early);
}
constexpr int ramp_end(int S) { int t = 1; while (issued(t, S) < S + t - 1) ++t; return t; }
template <int KT, int S, int G, int STAGE, int EXTRA, class I, class X, class E>
DEV void loader(I&& issue, X&& extra, E&& early) {
  loader_ramp<0, S, G, STAGE>(issue, early);
  constexpr int T0 = ramp_end(S);
  int slot = ((S + T0 - 1) % S) * STAGE;
  for (int t = T0; t <= KT - S; ++t) {
    issue(t - 1 + S, slot);
 slot = slot == (S - 1) * STAGE ? 0 : slot + STAGE; if (t < KT - S) waitbar<G*(S - 2)>(); }
  extra();
  loader_tail<S - 2, G, EXTRA>();
}
constexpr int GA_STAGE = 24576;
template <int KT, int S, int LDW, int EXTRA, class X>
DEV void gemmA_load(const char* act, const char* wgt, int l, int lw, X&& extra) {
  const char* ap = act + (size_t)lw * (KT * 4096);
  const char* wp = wgt + (size_t)lw * (16 * LDW * 2);
  const unsigned va = l * 16, vb = (l >> 3) * (LDW * 2) + (((l & 7) ^ (((l >> 4) & 1) << 2)) * 16);
  loader<KT, S, 6, GA_STAGE, EXTRA>([&](int t, int slot) {
    SETM0(slot + lw * 4096); const char* a = ap + (size_t)t * 4096; glds<0>(va, a); glds<1024>(va, a); glds<2048>(va, a); glds<3072>(va, a);
    SETM0(slot + 16384 + lw * 2048); const char* s = wp + (size_t)t * (64 * LDW * 2); glds<0>(vb, s); glds<1024>(vb, s + 8 * LDW * 2 - 1024); }, extra, [] {});
}
struct FragA { bf16x8 a0[4], a1[4], b[4]; };
template <int KT, int S>
DEV void gemmA_compute(const lptr sm, int l, int w, f32x16& c0, f32x16& c1, Stamps& S_) {
  const int q = (l >> 2) & 3, p = l & 3;
  const lptr ra = sm + (w & 1) * 8192 + l * 16;
  const lptr rb = sm + 16384 + (4 * (l >> 5) + q) * 128 + ((((w >> 1) * 4 + ((l >> 4) & 1) * 2 + (p >> 1)) ^ ((q >> 1) << 2)) * 16) + (p & 1) * 8;
  c0 = (f32x16){}; c1 = (f32x16){};
  FragA f, g; int slot = 0;
  auto rd = [&](FragA& d, int k) { d.a0[k] = ldsA(ra + slot + k * 1024); d.b[k] = ldsT(rb + slot + k * 2048, 1024); d.a1[k] = ldsA(ra + slot + 4096 + k * 1024); };
  auto step = [&](FragA& cur, FragA& nxt) { bar(); slot = slot == (S - 1) * GA_STAGE ? 0 : slot + GA_STAGE;
#pragma unroll
    for (int k = 0; k < 4; ++k) { if (k < 2) rd(nxt, 2 * k); c0 = mfma32(cur.b[k], cur.a0[k], c0); SB(); if (k < 2) rd(nxt, 2 * k + 1); c1 = mfma32(cur.b[k], cur.a1[k], c1); SB(); } };
  bar(); ST(2);
#pragma unroll
  for (int k = 0; k < 4; ++k) rd(f, k);
  for (int t = 0; t < KT - 2; t += 2) { step(f, g); step(g, f); }
  step(f, g);
#pragma unroll
  for (int k = 0; k < 4; ++k) { c0 = mfma32(g.b[k], g.a0[k], c0); c1 = mfma32(g.b[k], g.a1[k], c1); }
  ST(3);
}

constexpr int K2_KV0 = 98304, K2_KV1 = 131072, K2_KV2 = 0, K2_UW = 32768, K2_Q = 65536, K2_RED = 73728, K2_DUMP = 81920, K2_LDS = 163840;
DEV void pack16(const f32x16& s, u32x4& a, u32x4& b) {
  a = (u32x4){pk(s[0], s[1]), pk(s[2], s[3]), pk(s[4], s[5]), pk(s[6], s[7])};
  b = (u32x4){pk(s[8], s[9]), pk(s[10], s[11]), pk(s[12], s[13]), pk(s[14], s[15])}; }
DEV float max16(const f32x16& s) { float m = s[0];
#pragma unroll
  for (int i = 1; i < 16; ++i) m = __builtin_fmaxf(m, s[i]);
  return swap32max(m); }
#define EX2(v) __builtin_amdgcn_exp2f(v)
#define PIN(x) asm volatile("" : "+v"(x))

extern "C" __global__ __launch_bounds__(512, 2) void k_qattn(const char* __restrict__ hbuf, const char* __restrict__ wq, const char* __restrict__ kv, char* __restrict__ obuf,
                                                  const char* __restrict__ w1, const char* __restrict__ modh, float* __restrict__ utp,
                                                  const char* __restrict__ wo, const char* __restrict__ w2, char* __restrict__ xch) {
  const lptr sm = (lptr)smem_raw;
  ST_INIT();
  const int b = blockIdx.x, l = threadIdx.x & 63, w = __builtin_amdgcn_readfirstlane(threadIdx.x >> 6);
  const int xcd = b & 7, j = b >> 3, tb = (xcd >> 1) * 4 + (j & 3), hp = (xcd & 1) * 8 + (j >> 2);
  const unsigned l16 = l * 16;
  u32x4 q0u, q1u;
  if (w >= 4) {
    gemmA_load<16, 4, DM, 8>(hbuf + (size_t)tb * 4 * 64 * 1024, wq + hp * 128, l, w - 4, [&] {
#pragma unroll
      for (int g = 0; g < 2; ++g) { SETM0(K2_KV0 + (w - 4) * 8192 + g * 4096); const char* s = kv + (w - 4) * 8192 + g * 4096;
        glds<0>(l16, s); glds<1024>(l16, s); glds<2048>(l16, s); glds<3072>(l16, s); } });
    waitbar<0>();
    q0u = *(const LDS u32x4*)(sm + K2_Q + (w - 4) * 2048 + l16); q1u = *(const LDS u32x4*)(sm + K2_Q + (w - 4) * 2048 + 1024 + l16);
  } else {
    f32x16 c0, c1; gemmA_compute<16, 4>(sm, l, w, c0, c1, S_);
    constexpr float QS = 0.17677669529663687f * 1.4426950408889634f;
    c0 *= QS; c1 *= QS;
    u32x4 a, c; pack16(c1, a, c); *(LDS u32x4*)(sm + K2_Q + w * 2048 + l16) = a; *(LDS u32x4*)(sm + K2_Q + w * 2048 + 1024 + l16) = c;
    pack16(c0, q0u, q1u);
    bar();
  }
  const bf16x8 q0 = asb(q0u), q1 = asb(q1u);
  const char* kvw = kv + w * 4096;
  auto kvload = [&](int st, int slot) { SETM0(slot + w * 4096); const char* s = kvw + st * 32768; glds<0>(l16, s); glds<1024>(l16, s); glds<2048>(l16, s); glds<3072>(l16, s); };
  kvload(1, K2_KV1);
  const unsigned one2 = ((l & 7) == ((l >> 4) & 1) * 4) ? 0x3f803f80u : 0u;
  const bf16x8 ones = asb((u32x4){one2, one2, one2, one2});
  const lptr kp0 = sm + K2_KV0 + l16, kp1 = sm + K2_KV1 + l16, kp2 = sm + K2_KV2 + l16;
  bf16x8 k0 = ldsA(kp0), k1 = ldsA(kp0 + 1024), v0 = ldsA(kp0 + 2048), v1 = ldsA(kp0 + 3072);
  f32x16 negm, sA, sB, o = {}; f32x4 ls = {};
  { f32x16 z = {}; z = mfma32(k0, q0, z); z = mfma32(k1, q1, z); const float m = -max16(z);
#pragma unroll
    for (int i = 0; i < 16; ++i) negm[i] = m; }
  PIN(negm);
  sA = mfma32(k0, q0, negm); sA = mfma32(k1, q1, sA);
  k0 = ldsA(kp0 + 4096); k1 = ldsA(kp0 + 4096 + 1024);
  u32x4 p0, p1 = {0, 0, 0, 0}; bf16x8 v1p = v1;
#define TILE(S, N, KP, VP)                                                                                            \
  { N = mfma32(k0, q0, negm); SB();                                                                                  \
    S[0] = EX2(S[0]); S[1] = EX2(S[1]); S[2] = EX2(S[2]); S[3] = EX2(S[3]); p0[0] = pk(S[0], S[1]); p0[1] = pk(S[2], S[3]); PIN(p0); SB(); \
    o = mfma32(v1p, asb(p1), o); k0 = ldsA(KP); SB();                                                                \
    S[4] = EX2(S[4]); S[5] = EX2(S[5]); S[6] = EX2(S[6]); S[7] = EX2(S[7]); p0[2] = pk(S[4], S[5]); p0[3] = pk(S[6], S[7]); PIN(p0); SB(); \
    ls = mfma16(ones, asb(p1), ls); v1p = v1; v1 = ldsA((VP) + 1024); SB();                                          \
    S[8] = EX2(S[8]); S[9] = EX2(S[9]); PIN(S); SB();                                                                \
    N = mfma32(k1, q1, N); SB();                                                                                     \
    S[10] = EX2(S[10]); S[11] = EX2(S[11]); S[12] = EX2(S[12]); S[13] = EX2(S[13]); p1[0] = pk(S[8], S[9]); p1[1] = pk(S[10], S[11]); PIN(p1); PIN(S); SB(); \
    o = mfma32(v0, asb(p0), o); k1 = ldsA((KP) + 1024); SB();                                                        \
    S[14] = EX2(S[14]); S[15] = EX2(S[15]); p1[2] = pk(S[12], S[13]); p1[3] = pk(S[14], S[15]); PIN(p1); SB();       \
    ls = mfma16(ones, asb(p0), ls); v0 = ldsA(VP); SB(); }
#define STAGE(P, Q, ...)                                                                                              \
  TILE(sA, sB, P + 2 * 4096, P + 1 * 4096 + 2048) TILE(sB, sA, P + 3 * 4096, P + 2 * 4096 + 2048)                     \
  TILE(sA, sB, P + 4 * 4096, P + 3 * 4096 + 2048) TILE(sB, sA, P + 5 * 4096, P + 4 * 4096 + 2048)                     \
  TILE(sA, sB, P + 6 * 4096, P + 5 * 4096 + 2048) TILE(sB, sA, P + 7 * 4096, P + 6 * 4096 + 2048)                     \
  { __VA_ARGS__ }                                                                                                     \
  TILE(sA, sB, Q, P + 7 * 4096 + 2048) TILE(sB, sA, Q + 4096, Q + 2048)
  const int ub = b < 224 ? b : b - 224, cg = ub % 56, ks = ub / 56;
  u32x2 vf[4];
  ST(4);
  STAGE(kp0, kp1, waitbar<0>(); kvload(2, K2_KV2);
        { SETM0(K2_UW + w * 4096); const char* s = w1 + (size_t)(ks * 256 + w * 32) * (FF * 2) + cg * 128;
          const unsigned vo = (l >> 3) * (FF * 2) + (((l & 7) ^ (((l >> 4) & 1) << 2)) * 16);
          glds<0>(vo, s); glds<1024>(vo, s + 8 * FF * 2 - 1024); glds<2048>(vo, s + 16 * FF * 2 - 2048); glds<3072>(vo, s + 24 * FF * 2 - 3072);
          const char* mv = modh + (ks * 256 + w * 32) * 2; const unsigned vm = ((4 - (l & 1)) * DM + 4 * (l >> 5)) * 2;
          vf[0] = gload2<0>(vm, mv); vf[1] = gload2<16>(vm, mv); vf[2] = gload2<32>(vm, mv); vf[3] = gload2<48>(vm, mv); })
  STAGE(kp1, kp2, waitbar<8>(); kvload(3, K2_KV0);)
  STAGE(kp2, kp0, waitbar<0>();
        if (b < 224) { PIN(vf[0]); PIN(vf[1]); PIN(vf[2]); PIN(vf[3]); const int q = (l >> 2) & 3; const int p = l & 3; f32x16 d0 = {}; f32x16 d1 = {};
          const lptr rb = sm + K2_UW + (w * 32 + 4 * (l >> 5) + q) * 128 + (((((l >> 4) & 1) * 2 + (p >> 1)) ^ ((q >> 1) << 2)) * 16) + (p & 1) * 8;
          const lptr rc = sm + K2_UW + (w * 32 + 4 * (l >> 5) + q) * 128 + (((4 + ((l >> 4) & 1) * 2 + (p >> 1)) ^ ((q >> 1) << 2)) * 16) + (p & 1) * 8;
          _Pragma("unroll") for (int kk = 0; kk < 2; ++kk) { const bf16x8 vv = asb((u32x4){vf[2 * kk][0], vf[2 * kk][1], vf[2 * kk + 1][0], vf[2 * kk + 1][1]});
            d0 = mfma32(ldsT(rb + kk * 2048, 1024), vv, d0); d1 = mfma32(ldsT(rc + kk * 2048, 1024), vv, d1); }
          if ((l & 31) < 2) { const lptr d = sm + K2_RED + w * 512 + (l & 31) * 256 + (l >> 5) * 16;
            _Pragma("unroll") for (int i = 0; i < 4; ++i) { *(LDS f32x4*)(d + i * 32) = (f32x4){d0[4 * i], d0[4 * i + 1], d0[4 * i + 2], d0[4 * i + 3]};
              *(LDS f32x4*)(d + 128 + i * 32) = (f32x4){d1[4 * i], d1[4 * i + 1], d1[4 * i + 2], d1[4 * i + 3]}; } } }
        kvload(4, K2_KV1);
        { SETM0(K2_DUMP + w * 1024); glds<0>(l16, wo + b * 8192 + w * 1024); glds<0>(l16, w2 + (size_t)b * 28672 + w * 1024); })
  STAGE(kp0, kp1, waitbar<2>();
        if (b < 224 && w < 2) { float v = 0.f;
          _Pragma("unroll") for (int u = 0; u < 8; ++u) v += *(const LDS float*)(sm + K2_RED + u * 512 + w * 256 + l * 4);
          utp[(ks * 2 + w) * FF + cg * 64 + l] = v; }
        kvload(5, K2_KV2);
        { SETM0(K2_DUMP + w * 1024); glds<0>(l16, w2 + (size_t)b * 28672 + 8192 + w * 1024); glds<0>(l16, w2 + (size_t)b * 28672 + 16384 + w * 1024); })
  STAGE(kp1, kp2, waitbar<2>(); kvload(6, K2_KV0);
        { SETM0(K2_DUMP + w * 1024); glds<0>(l16, w2 + (size_t)b * 28672 + 24576 + (w & 3) * 1024); })
  STAGE(kp2, kp0, waitbar<1>(); kvload(7, K2_KV1);
        if (NEXTPF && w == 0) { CODE_RANGE(k_oproj, k_mod, cb, cs) const unsigned o = j * 1024 + l16; SETM0(K2_DUMP); glds<0>(o < cs ? o : 0, cb); })
  STAGE(kp0, kp1, waitbar<0>(); { char* d = xch + b * 16384 + w * 2048 + l16; const u32x4 e = {~0u, ~0u, ~0u, ~0u}; gstore(d, e); gstore(d + 1024, e); })
  STAGE(kp1, kp2, )
  ST(5);
  o = mfma32(v1p, asb(p1), o); ls = mfma16(ones, asb(p1), ls);
  float lsum = ls[0];
  { float chk = lsum;
#pragma unroll
    for (int i = 0; i < 16; ++i) chk = __builtin_fmaxf(chk, __builtin_fabsf(o[i]) * 0x1p-40f);
    bool bad = !(chk < 0x1p80f);
#pragma unroll
    for (int i = 0; i < 16; ++i) bad |= (o[i] != o[i]);
    bad |= (lsum != lsum);
    if (__any(bad)) {
      float m = -INFINITY; lsum = 0.f; o = (f32x16){};
      for (int t = 0; t < 64; ++t) { const bf16x8* g = (const bf16x8*)(kv + t * 4096) + l;
        f32x16 s = {}; s = mfma32(g[0], q0, s); s = mfma32(g[64], q1, s);
        const float mn = __builtin_fmaxf(m, max16(s)), al = EX2(m - mn); m = mn; float rsum = 0.f;
#pragma unroll
        for (int i = 0; i < 16; ++i) { s[i] = rbf(EX2(s[i] - mn)); rsum += s[i]; o[i] *= al; }
        lsum = lsum * al + swap32add(rsum);
        u32x4 a, c; pack16(s, a, c); o = mfma32(g[128], asb(a), o); o = mfma32(g[192], asb(c), o); } } }
  o *= 1.f / lsum;
  u32x4 o0, o1; pack16(o, o0, o1);
  char* d = obuf + (size_t)((tb * 4 + 2 * (w & 1) + (w >> 2)) * 64 + (hp * 2 + ((w >> 1) & 1)) * 2) * 1024 + l16;
  gstore(d, o0); gstore(d + 1024, o1);
  ST_END(2);
}

DEV u32x4 rowswap(u32x4 v) { auto a = __builtin_amdgcn_permlane32_swap(v[0], v[2], false, false); auto c = __builtin_amdgcn_permlane32_swap(v[1], v[3], false, false);
  return (u32x4){a[0], c[0], a[1], c[1]}; }

constexpr int K3_S = 6, K3_LDS = K3_S * GA_STAGE;
extern "C" __global__ __launch_bounds__(512, 2) void k_oproj(const char* __restrict__ obuf, const char* __restrict__ wo, const char* __restrict__ x, const float* __restrict__ modf,
                                                  char* __restrict__ x1b, char* __restrict__ yb, f32x2* __restrict__ stat) {
  const lptr sm = (lptr)smem_raw;
  ST_INIT();
  const int b = blockIdx.x, l = threadIdx.x & 63, w = __builtin_amdgcn_readfirstlane(threadIdx.x >> 6);
  const int xcd = b & 7, j = b >> 3, tb = (xcd >> 1) * 4 + (j & 3), nt = (xcd & 1) * 8 + (j >> 2);
  if (w >= 4) { u32x4 e; gemmA_load<16, K3_S, DM, NEXTPF>(obuf + (size_t)tb * 4 * 64 * 1024, wo + nt * 128, l, w - 4, [&] {
      if (NEXTPF) { CODE_RANGE(k_ffn1, k_ffn2, cb, cs) const unsigned o = (j & 3) * 4096 + (w - 4) * 1024 + l * 16; e = gload<0>(o < cs ? o : 0, cb); } });
    if (NEXTPF) asm volatile("s_waitcnt vmcnt(0)" : "+v"(e)::"memory"); return; }
  const int hi = l >> 5, tb32 = tb * 4 + 2 * (w & 1), n0 = nt * 64 + (w >> 1) * 32;
  u32x4 xr[2][2]; f32x4 ga[4], cb[4];
#pragma unroll
  for (int i = 0; i < 2; ++i)
#pragma unroll
    for (int h = 0; h < 2; ++h) xr[i][h] = *(const u32x4*)(x + (size_t)((tb32 + i) * 32 + (l & 31)) * 2048 + (n0 + 16 * h + 8 * hi) * 2);
#pragma unroll
  for (int q = 0; q < 4; ++q) { ga[q] = *(const f32x4*)(modf + 2 * DM + n0 + 8 * q + 4 * hi); cb[q] = *(const f32x4*)(modf + 4 * DM + n0 + 8 * q + 4 * hi); }
  f32x16 acc[2]; gemmA_compute<16, K3_S>(sm, l, w, acc[0], acc[1], S_);
#pragma unroll
  for (int i = 0; i < 2; ++i) { unsigned xo[8], yo[8]; float x1[16], s = 0.f;
#pragma unroll
    for (int h = 0; h < 2; ++h) { const u32x4 xs = rowswap(xr[i][h]);
#pragma unroll
      for (int e = 0; e < 4; ++e) { const int r = 8 * h + 2 * e, q = r >> 2, c = r & 3;
        const unsigned u = pk(blo(xs[e]) + ga[q][c] * acc[i][r], bhi(xs[e]) + ga[q][c + 1] * acc[i][r + 1]);
        x1[r] = blo(u); x1[r + 1] = bhi(u); xo[4 * h + e] = u; yo[4 * h + e] = pk(x1[r] * cb[q][c], x1[r + 1] * cb[q][c + 1]); s += x1[r] + x1[r + 1]; } }
    const size_t off = (size_t)((tb32 + i) * 64 + (n0 >> 4)) * 1024 + l * 16;
    gstore(yb + off, (u32x4){yo[0], yo[1], yo[2], yo[3]}); gstore(yb + off + 1024, (u32x4){yo[4], yo[5], yo[6], yo[7]});
    gstore(x1b + off, (u32x4){xo[0], xo[1], xo[2], xo[3]}); gstore(x1b + off + 1024, (u32x4){xo[4], xo[5], xo[6], xo[7]});
    const float mean = swap32add(s) * (1.f / 32.f); float m2 = 0.f;
#pragma unroll
    for (int e = 0; e < 16; ++e) m2 += (x1[e] - mean) * (x1[e] - mean);
    m2 = swap32add(m2);
    if (hi == 0) stat[((tb32 + i) * 32 + (n0 >> 5)) * 32 + l] = (f32x2){mean, m2}; }
  ST_END(3);
}

constexpr int K4_STAGE = 45056, K4_UT = 3 * K4_STAGE, K4_RS = K4_UT + 2048, K4_LDS = K4_RS + 1024;
struct FragB { bf16x8 a[4]; s16x4 b[14]; };
extern "C" __global__ __launch_bounds__(512, 2) void k_ffn1(const char* __restrict__ yb, const char* __restrict__ w1, const char* __restrict__ stat,
                                                            const char* __restrict__ utp, char* __restrict__ gb) {
  const lptr sm = (lptr)smem_raw;
  ST_INIT();
  const int b = blockIdx.x, t = threadIdx.x, l = t & 63, w = __builtin_amdgcn_readfirstlane(t >> 6);
  const int xcd = b & 7, j = b >> 3, tb = (xcd >> 2) * 8 + (j & 7), nt = (xcd & 3) * 4 + (j >> 3);
  constexpr float NL2E = -1.4426950408889634f;
  if (w >= 4) { const int lw = w - 4;
    const char* ap = yb + (size_t)(tb * 4 + lw) * 64 * 1024; const char* wp = w1 + nt * 448;
    unsigned vb[7];
#pragma unroll
    for (int i = 0; i < 7; ++i) { const int c = (lw * 7 + i) * 64 + l, r = c / 28; vb[i] = r * (FF * 2) + ((c - r * 28) ^ ((r >> 1) & 2)) * 16 + 3072 - i * 1024; }
    const unsigned va = l * 16;
    u32x2 st[16]; unsigned up[8]; u32x4 e;
    loader<16, 3, 11, K4_STAGE, 1>([&](int tt, int slot) {
      SETM0(slot + lw * 4096); const char* a = ap + (size_t)tt * 4096;
      glds<0>(va, a); glds<1024>(va, a); glds<2048>(va, a); glds<3072>(va, a);
      SETM0(slot + 16384 + lw * 7168 + 3072); const char* s = wp + (size_t)tt * (64 * FF * 2);
      glds<-3072>(vb[0], s); glds<-2048>(vb[1], s); glds<-1024>(vb[2], s); glds<0>(vb[3], s); glds<1024>(vb[4], s); glds<2048>(vb[5], s); glds<3072>(vb[6], s); },
      [&] {
#pragma unroll
        for (int i = 0; i < 16; ++i) asm volatile("" : "+v"(st[i]));
#pragma unroll
        for (int i = 0; i < 8; ++i) asm volatile("" : "+v"(up[i]));
        float mean = 0.f;
#pragma unroll
        for (int i = 0; i < 16; ++i) mean += __uint_as_float(st[i][0]);
        mean = swap32add(mean) * (1.f / 32.f); float m2 = 0.f;
#pragma unroll
        for (int i = 0; i < 16; ++i) { const float d = __uint_as_float(st[i][0]) - mean; m2 += __uint_as_float(st[i][1]) + 32.f * d * d; }
        const float rs = NL2E * __builtin_amdgcn_rsqf(swap32add(m2) * (1.f / 1024.f) + 1e-5f);
        if (l < 32) *(LDS f32x2*)(sm + K4_RS + (lw * 32 + l) * 8) = (f32x2){rs, -rs * mean};
        auto f = [&](int i) { return __uint_as_float(up[i]); };
        *(LDS float*)(sm + K4_UT + (t - 256) * 4) = (f(0) + f(2)) + (f(4) + f(6)); *(LDS float*)(sm + K4_UT + 1024 + (t - 256) * 4) = NL2E * ((f(1) + f(3)) + (f(5) + f(7)));
        CODE_RANGE(k_ffn2, k_mod, cb, cs) const unsigned o = lw * 1024 + l * 16; e = gload<0>(o < cs ? o : 0, cb); },
      [&] { const char* sp = stat + (size_t)(tb * 4 + lw) * 8192; const unsigned vs = l * 8, vu = (nt * 224 + (t < 480 ? t - 256 : 0)) * 4;
#define S2(i) st[2 * i] = gload2<0>(vs, sp + i * 1024); st[2 * i + 1] = gload2<512>(vs, sp + i * 1024); up[i] = gload1<0>(vu, utp + i * FF * 4);
        S2(0) S2(1) S2(2) S2(3) S2(4) S2(5) S2(6) S2(7)
#undef S2
      });
    asm volatile("s_waitcnt vmcnt(0)" : "+v"(e)::"memory");
    return; }
  const int g = l >> 4, th = w & 1, nh = w >> 1, sw = (g & 1) * 32;
  const lptr ra = sm + th * 8192 + (g >> 1) * 1024 + (g & 1) * 512 + (l & 15) * 16;
  const lptr rb = sm + 16384 + (16 * (g >> 1) + 4 * (g & 1) + ((l >> 2) & 3)) * 448 + (l & 3) * 8;
  lptr base[4] = {ra, rb + nh * 256 + sw, rb + nh * 256 - sw, rb + (((6 + nh) * 32) ^ sw)};
  PIN(base[0]); PIN(base[1]); PIN(base[2]); PIN(base[3]);
  lptr p[4] = {base[0], base[1], base[2], base[3]};
  f32x4 acc[7][4];
  FragB f, h; int slot = 0;
  auto rd = [&](FragB& d, int i, int ks) {
    if (i < 4) d.a[i] = ldsA(p[0] + (i >> 1) * 4096 + (i & 1) * 256 + ks * 2048);
    else { const int n = (i - 4) >> 1; d.b[i - 4] = ldsT1((n == 6 ? p[3] : p[1 + (n & 1)] + n * 32) + ks * (32 * 448) + (i & 1) * (8 * 448)); } };
  auto kstep = [&](FragB& cur, FragB& nxt, int ks, auto first) {
#pragma unroll
    for (int n = 0; n < 7; ++n)
#pragma unroll
      for (int i = 0; i < 4; ++i) { const int m = n * 4 + i;
        acc[n][i] = mfma16(cat(cur.b[2 * n], cur.b[2 * n + 1]), cur.a[i], first ? (f32x4){} : acc[n][i]); SB();
        if (m < 18) rd(nxt, m, ks); else if (ks == 1 && m < 22) { p[m - 18] = base[m - 18] + slot; PIN(p[m - 18]); }
        SB(); } };
  bar(); ST(2);
#pragma unroll
  for (int i = 0; i < 18; ++i) rd(f, i, 0);
  slot = K4_STAGE; kstep(f, h, 1, std::true_type{}); bar(); kstep(h, f, 0, std::false_type{});
  for (int tt = 1; tt < 15; ++tt) { slot = slot == 2 * K4_STAGE ? 0 : slot + K4_STAGE; kstep(f, h, 1, std::false_type{}); bar(); kstep(h, f, 0, std::false_type{}); }
  kstep(f, h, 1, std::false_type{});
#pragma unroll
  for (int n = 0; n < 7; ++n)
#pragma unroll
    for (int i = 0; i < 4; ++i) acc[n][i] = mfma16(cat(h.b[2 * n], h.b[2 * n + 1]), h.a[i], acc[n][i]);
  ST(3);
  f32x2 rn[4];
#pragma unroll
  for (int i = 0; i < 4; ++i) rn[i] = *(const LDS f32x2*)(sm + K4_RS + (th * 64 + i * 16 + (l & 15)) * 8);
  auto act = [&](int n, int i) { const int col = ((n < 6 ? 8 * nh + n : 6 + nh) * 16 + 4 * g) * 4;
    const f32x4 u = *(const LDS f32x4*)(sm + K4_UT + col), tv = *(const LDS f32x4*)(sm + K4_UT + 1024 + col); u32x2 o;
#pragma unroll
    for (int e = 0; e < 2; ++e) { const f32x2 z = rn[i][0] * (f32x2){acc[n][i][2 * e], acc[n][i][2 * e + 1]} + (rn[i][1] * (f32x2){u[2 * e], u[2 * e + 1]} + (f32x2){tv[2 * e], tv[2 * e + 1]});
      const f32x2 dn = NL2E * (f32x2){EX2(z[0]), EX2(z[1])} + NL2E;
      const f32x2 r = z * (f32x2){__builtin_amdgcn_rcpf(dn[0]), __builtin_amdgcn_rcpf(dn[1])}; o[e] = pk(r[0], r[1]); }
    return o; };
  auto dst = [&](int gi, int i) { const int kk = nt * 7 + gi; return gb + (size_t)(((tb * 56 + (kk >> 1)) * 8 + th * 4 + i) * 2 + (kk & 1)) * 1024 + l * 16; };
#pragma unroll
  for (int p = 0; p < 3; ++p)
#pragma unroll
    for (int i = 0; i < 4; ++i) { const u32x2 lo = act(2 * p, i), hi = act(2 * p + 1, i); gstore(dst(4 * nh + p, i), (u32x4){lo[0], lo[1], hi[0], hi[1]}); }
#pragma unroll
  for (int i = 0; i < 4; ++i) gstore2(dst(3, i) + nh * 8, act(6, i));
  ST_END(4);
}

constexpr int K5_STAGE = 32768, K5_S = 5, K5_LDS = K5_S * K5_STAGE;
struct FragC { bf16x8 a[4]; s16x4 b[8]; };
extern "C" __global__ __launch_bounds__(512, 2) void k_ffn2(const char* __restrict__ gb, const char* __restrict__ w2, const char* __restrict__ x1b,
                                                            const float* __restrict__ modf, char* __restrict__ out, char* xch) {
  const lptr sm = (lptr)smem_raw;
  ST_INIT();
  const int b = blockIdx.x, l = threadIdx.x & 63, w = __builtin_amdgcn_readfirstlane(threadIdx.x >> 6);
  const int kh = b & 1, j = b >> 3, tb = ((b & 7) >> 1) * 4 + (j & 3), nt = j >> 2;
  if (w >= 4) { const int lw = w - 4;
    const char* ap = gb + (size_t)(tb * 56 + kh * 28) * 16384 + lw * 4096; const char* wp = w2 + (size_t)(kh * 1792 + lw * 16) * 2048 + nt * 256;
    const unsigned va = l * 16, v0 = (l >> 4) * 2048 + ((l & 15) ^ ((l >> 4) * 2)) * 16, v1 = v0 ^ 128;
    loader<28, K5_S, 8, K5_STAGE, 0>([&](int t, int slot) {
      SETM0(slot + lw * 4096); const char* a = ap + (size_t)t * 16384; glds<0>(va, a); glds<1024>(va, a); glds<2048>(va, a); glds<3072>(va, a);
      SETM0(slot + 16384 + lw * 4096); const char* s = wp + (size_t)t * (64 * 2048);
      glds<0>(v0, s); glds<1024>(v1, s + 8192 - 1024); glds<2048>(v0, s + 16384 - 2048); glds<3072>(v1, s + 24576 - 3072); }, [] {}, [] {});
    return; }
  const int g = l >> 4, th = w & 1, nh = w >> 1, rx = 4 * (g & 1) + ((l >> 2) & 3);
  const int tok0 = tb * 128 + th * 64 + kh * 32, n0 = nt * 128 + nh * 64;
  u32x2 xr[2][4]; f32x4 gv[4];
#pragma unroll
  for (int n = 0; n < 4; ++n) { gv[n] = *(const f32x4*)(modf + 5 * DM + n0 + 16 * n + 4 * g);
#pragma unroll
    for (int i = 0; i < 2; ++i) xr[i][n] = *(const u32x2*)(x1b + (size_t)((tok0 >> 5) * 64 + (n0 >> 4) + n) * 1024 + (g & 1) * 512 + (i * 16 + (l & 15)) * 16 + (g >> 1) * 8); }
  lptr base[6], p[6];
  base[4] = sm + th * 8192 + kh * 4096 + l * 16; base[5] = sm + th * 8192 + (kh ^ 1) * 4096 + l * 16;
#pragma unroll
  for (int n = 0; n < 4; ++n) base[n] = sm + 16384 + (4 * g + ((l >> 2) & 3)) * 256 + ((4 * nh + n) ^ rx) * 32 + (l & 3) * 8;
#pragma unroll
  for (int n = 0; n < 6; ++n) { PIN(base[n]); p[n] = base[n]; }
  f32x4 acc[4][4];
  FragC f, h; int slot = 0;
  auto rd = [&](FragC& d, int i, int ks) {
    if (i < 4) d.a[i] = ldsA(p[4 + (i >> 1)] + (i & 1) * 2048 + ks * 1024); else d.b[i - 4] = ldsT1(p[(i - 4) >> 1] + ks * 8192 + (i & 1) * 4096); };
  auto kstep = [&](FragC& cur, FragC& nxt, int ks, auto first) {
#pragma unroll
    for (int n = 0; n < 4; ++n)
#pragma unroll
      for (int i = 0; i < 4; ++i) { const int m = n * 4 + i;
        acc[n][i] = mfma16(cat(cur.b[2 * n], cur.b[2 * n + 1]), cur.a[i], first ? (f32x4){} : acc[n][i]); SB();
        if (m < 12) rd(nxt, m, ks);
        else if (ks == 1) { p[m - 12] = base[m - 12] + slot; PIN(p[m - 12]); if (m < 14) { p[m - 8] = base[m - 8] + slot; PIN(p[m - 8]); } }
        SB(); } };
  bar(); ST(2);
#pragma unroll
  for (int i = 0; i < 12; ++i) rd(f, i, 0);
  slot = K5_STAGE; kstep(f, h, 1, std::true_type{}); bar(); kstep(h, f, 0, std::false_type{});
  for (int tt = 1; tt < 25; ++tt) { slot = slot == (K5_S - 1) * K5_STAGE ? 0 : slot + K5_STAGE; kstep(f, h, 1, std::false_type{}); bar(); kstep(h, f, 0, std::false_type{}); }
  auto hstep = [&](FragC& cur, FragC& nxt, int half, int nhalf, int ns) {
    const int off = (ns >> 1) * K5_STAGE + (ns & 1) * 1024, offw = (ns >> 1) * K5_STAGE + (ns & 1) * 8192;
#pragma unroll
    for (int m = 0; m < 8; ++m) { const int n = m >> 1, i = 2 * half + (m & 1);
      acc[n][i] = mfma16(cat(cur.b[2 * n], cur.b[2 * n + 1]), cur.a[i], acc[n][i]); SB();
      if (ns >= 0) { if (m < 2) nxt.a[2 * nhalf + m] = ldsA(base[4 + nhalf] + off + m * 2048); nxt.b[m] = ldsT1(base[m >> 1] + offw + (m & 1) * 4096); }
      SB(); } };
  hstep(f, h, 1, 1, 1); bar(); hstep(h, f, 1, 1, 2); hstep(f, h, 1, 1, 3); bar(); hstep(h, f, 1, 1, 4); hstep(f, h, 1, 1, 5); hstep(h, f, 1, 0, 0);
  ST(3);
  hstep(f, h, 0, 0, 1);
  { char* d = xch + (size_t)(b * 4 + w) * 4096 + l * 16;
#pragma unroll
    for (int i = 0; i < 2; ++i)
#pragma unroll
      for (int q = 0; q < 2; ++q) { const f32x4 u = acc[2 * q][2 + i], v = acc[2 * q + 1][2 + i];
        gstore(d + (i * 2 + q) * 1024, (u32x4){min(pk(u[0], u[1]), ~1u), min(pk(u[2], u[3]), ~1u), min(pk(v[0], v[1]), ~1u), min(pk(v[2], v[3]), ~1u)}); } }
  SB();
  hstep(h, f, 0, 0, 2); hstep(f, h, 0, 0, 3); hstep(h, f, 0, 0, 4); hstep(f, h, 0, 0, 5); hstep(h, f, 0, 0, -1);
  ST(4);
  u32x4 pr[4];
  { const char* s = xch + (size_t)((b ^ 1) * 4 + w) * 4096; const unsigned vo = l * 16;
    for (int spin = 0; spin < (1 << 16); ++spin) {
      pr[0] = gloadd<0>(vo, s); pr[1] = gloadd<1024>(vo, s); pr[2] = gloadd<2048>(vo, s); pr[3] = gloadd<3072>(vo, s);
      asm volatile("s_waitcnt vmcnt(0)" : "+v"(pr[0]), "+v"(pr[1]), "+v"(pr[2]), "+v"(pr[3])::"memory");
      unsigned m = 0;
#pragma unroll
      for (int i = 0; i < 4; ++i) m = max(max(max(m, pr[i][0]), max(pr[i][1], pr[i][2])), pr[i][3]);
      if (!__any(m == ~0u)) break; } }
  ST(6);
#pragma unroll
  for (int i = 0; i < 2; ++i)
#pragma unroll
    for (int p = 0; p < 2; ++p) { unsigned o[2][2];
#pragma unroll
      for (int q = 0; q < 2; ++q) { const int n = 2 * p + q; const unsigned u0 = pr[2 * i + p][2 * q], u1 = pr[2 * i + p][2 * q + 1];
        const f32x4 a = acc[n][i] + (f32x4){blo(u0), bhi(u0), blo(u1), bhi(u1)};
        o[q][0] = pk(blo(xr[i][n][0]) + gv[n][0] * a[0], bhi(xr[i][n][0]) + gv[n][1] * a[1]); o[q][1] = pk(blo(xr[i][n][1]) + gv[n][2] * a[2], bhi(xr[i][n][1]) + gv[n][3] * a[3]); }
      auto r0 = __builtin_amdgcn_permlane16_swap(o[0][0], o[1][0], false, false); auto r1 = __builtin_amdgcn_permlane16_swap(o[0][1], o[1][1], false, false);
      gstore(out + (size_t)(tok0 + i * 16 + (l & 15)) * 2048 + (n0 + 32 * p + 16 * (g & 1) + 8 * (g >> 1)) * 2, (u32x4){r0[0], r1[0], r0[1], r1[1]}); }
  ST_END(5);
}

extern "C" __global__ __launch_bounds__(256) void k_mod(const uint16_t* __restrict__ c, const char* __restrict__ wmod, float* __restrict__ modp,
                                             const char* __restrict__ x, const char* __restrict__ wq, const char* __restrict__ wk, const char* __restrict__ wv) {
  __shared__ float red[4][32];
  ST_INIT();
  const int t = threadIdx.x;
  if (blockIdx.x >= PF0 && blockIdx.x < PF0 + 256) { const int p = blockIdx.x - PF0; u32x4 d[7];
    const char* xb = x + (size_t)((p & 7) * 32 + (p >> 3)) * 16384;
#pragma unroll
    for (int i = 0; i < 4; ++i) d[i] = gload<0>(t * 16, xb + i * 4096);
    d[4] = gload<0>(t * 16, wq + p * 8192); d[5] = gload<0>(t * 16, wq + p * 8192 + 4096);
    d[6] = gload<0>((t & 31) * 16, (p & 128 ? wv : wk) + (p & 127) * 512);
    { CODE_RANGE(k_ln1kv, k_mod, cb, cs) const unsigned o = (p >> 3) * 2048 + t * 16; u32x4 e = gload<0>(t < 128 && o < cs ? o : 0, cb);
      asm volatile("s_waitcnt vmcnt(0)" : "+v"(e)::"memory"); }
    asm volatile("s_waitcnt vmcnt(0)" : "+v"(d[0]), "+v"(d[1]), "+v"(d[2]), "+v"(d[3]), "+v"(d[4]), "+v"(d[5]), "+v"(d[6])::"memory");
    return; }
  const int b = blockIdx.x < PF0 ? blockIdx.x : blockIdx.x - 256, xcd = b & 7, j = b >> 3;
  const int cg = (j & 1) + 2 * (xcd + 8 * ((j >> 1) % 12)), ks = j / 24;
  const int row0 = ks * 256 + (t >> 2);
  const char* p = wmod + (size_t)row0 * (NMOD * 2) + cg * 64 + (t & 3) * 16;
  u32x4 w[4]; float s[4];
#pragma unroll
  for (int i = 0; i < 4; ++i) { w[i] = *(const u32x4*)(p + (size_t)i * 64 * NMOD * 2); s[i] = __uint_as_float((unsigned)c[row0 + 64 * i] << 16); }
  float a[8] = {0, 0, 0, 0, 0, 0, 0, 0};
#pragma unroll
  for (int i = 0; i < 4; ++i) { const float f = rbf(silu(s[i]));
#pragma unroll
    for (int e = 0; e < 4; ++e) { a[2 * e] += f * blo(w[i][e]); a[2 * e + 1] += f * bhi(w[i][e]); } }
#pragma unroll
  for (int e = 0; e < 8; ++e) { float v = a[e]; v += __shfl_xor(v, 4); v += __shfl_xor(v, 8); v = swap16add(v); a[e] = swap32add(v); }
  if ((t & 63) < 4) {
#pragma unroll
    for (int e = 0; e < 8; ++e) red[t >> 6][(t & 3) * 8 + e] = a[e]; }
  __syncthreads();
  if (t < 32) modp[ks * NMOD + cg * 32 + t] = (red[0][t] + red[1][t]) + (red[2][t] + red[3][t]);
  ST_END(0);
}

extern "C" void kernel_launch(void* const* d_in, const int* in_sizes, int n_in, void* d_out, int out_size, void* d_ws, size_t ws_size, hipStream_t stream) {
  const char* x = (const char*)d_in[0]; const char* wq = (const char*)d_in[1]; const char* wk = (const char*)d_in[2]; const char* wv = (const char*)d_in[3];
  const char* wo = (const char*)d_in[4]; const char* w1 = (const char*)d_in[6]; const char* w2 = (const char*)d_in[7];
  const uint16_t* c = (const uint16_t*)d_in[8]; const char* wmod = (const char*)d_in[9];
  char* ws = (char*)d_ws;
  (void)hipFuncSetAttribute((const void*)k_ln1kv, hipFuncAttributeMaxDynamicSharedMemorySize, K1_LDS);
  (void)hipFuncSetAttribute((const void*)k_qattn, hipFuncAttributeMaxDynamicSharedMemorySize, K2_LDS);
  (void)hipFuncSetAttribute((const void*)k_oproj, hipFuncAttributeMaxDynamicSharedMemorySize, K3_LDS);
  (void)hipFuncSetAttribute((const void*)k_ffn1, hipFuncAttributeMaxDynamicSharedMemorySize, K4_LDS);
  (void)hipFuncSetAttribute((const void*)k_ffn2, hipFuncAttributeMaxDynamicSharedMemorySize, K5_LDS);
  float* modp = (float*)(ws + WS_MODP); float* modf = (float*)(ws + WS_MODF);
  k_mod<<<1024, 256, 0, stream>>>(c, wmod, modp, x, wq, wk, wv);
  k_ln1kv<<<256, 512, K1_LDS, stream>>>(x, wk, wv, (const char*)modp, modf, (uint16_t*)(ws + WS_MODH), ws + WS_H, ws + WS_KV);
  k_qattn<<<256, 512, K2_LDS, stream>>>(ws + WS_H, wq, ws + WS_KV, ws + WS_O, w1, ws + WS_MODH, (float*)(ws + WS_UTP), wo, w2, ws + WS_XCH);
  k_oproj<<<256, 512, K3_LDS, stream>>>(ws + WS_O, wo, x, modf, ws + WS_X1, ws + WS_Y, (f32x2*)(ws + WS_STAT));
  k_ffn1<<<256, 512, K4_LDS, stream>>>(ws + WS_Y, w1, ws + WS_STAT, ws + WS_UTP, ws + WS_G);
  k_ffn2<<<256, 512, K5_LDS, stream>>>(ws + WS_G, w2, ws + WS_X1, modf, (char*)d_out, ws + WS_XCH);
}
```

```cpp
#include <hip/hip_runtime.h>
#include <hip/hip_bf16.h>
#include <stdint.h>
#include <type_traits>

typedef __attribute__((ext_vector_type(2))) float f32x2;
typedef __attribute__((ext_vector_type(4))) float f32x4;
typedef __attribute__((ext_vector_type(16))) float f32x16;
typedef __attribute__((ext_vector_type(2))) __bf16 bf16x2;
typedef __attribute__((ext_vector_type(8))) __bf16 bf16x8;
typedef __attribute__((ext_vector_type(4))) short s16x4;
typedef __attribute__((ext_vector_type(2))) unsigned u32x2;
typedef __attribute__((ext_vector_type(4))) unsigned u32x4;
#define LDS __attribute__((address_space(3)))
typedef LDS char* lptr;
#define PF0 768
#define NEXTPF 1
#define LFIRST 1
#define DEV __device__ __forceinline__

constexpr int SEQ = 2048, DM = 1024, FF = 3584, NMOD = 6 * DM;
constexpr size_t WS_MODP = 0;
constexpr size_t WS_UTP = 98304;
constexpr size_t WS_MODF = 212992;
constexpr size_t WS_MODH = 237568;
constexpr size_t WS_STAT = 262144;
constexpr size_t WS_KV = 786432;
constexpr size_t WS_H = (size_t)1 << 20;
constexpr size_t WS_O = (size_t)5 << 20;
constexpr size_t WS_X1 = (size_t)9 << 20;
constexpr size_t WS_Y = (size_t)13 << 20;
constexpr size_t WS_G = (size_t)17 << 20;
constexpr size_t WS_XCH = (size_t)32 << 20;

DEV unsigned pk(float a, float b) { return __builtin_bit_cast(unsigned, __builtin_convertvector((f32x2){a, b}, bf16x2)); }
DEV float rbf(float a) { return __uint_as_float(pk(a, 0.f) << 16); }
DEV float blo(unsigned u) { return __uint_as_float(u << 16); }
DEV float bhi(unsigned u) { return __uint_as_float(u & 0xffff0000u); }
DEV float silu(float z) { return z * __builtin_amdgcn_rcpf(1.f + __builtin_amdgcn_exp2f(-1.4426950408889634f * z)); }
DEV f32x16 mfma32(bf16x8 a, bf16x8 b, f32x16 c) { return __builtin_amdgcn_mfma_f32_32x32x16_bf16(a, b, c, 0, 0, 0); }
DEV f32x4 mfma16(bf16x8 a, bf16x8 b, f32x4 c) { return __builtin_amdgcn_mfma_f32_16x16x32_bf16(a, b, c, 0, 0, 0); }
DEV bf16x8 asb(u32x4 v) { return __builtin_bit_cast(bf16x8, v); }
DEV bf16x8 ldsA(lptr p) { return *(const LDS bf16x8*)p; }
DEV bf16x8 ldsT(lptr p, int second) {
  s16x4 a = __builtin_amdgcn_ds_read_tr16_b64_v4i16((LDS s16x4*)p);
  s16x4 b = __builtin_amdgcn_ds_read_tr16_b64_v4i16((LDS s16x4*)(p + second));
  return __builtin_bit_cast(bf16x8, __builtin_shufflevector(a, b, 0, 1, 2, 3, 4, 5, 6, 7));
}
DEV s16x4 ldsT1(lptr p) { return __builtin_amdgcn_ds_read_tr16_b64_v4i16((LDS s16x4*)p); }
DEV bf16x8 cat(s16x4 a, s16x4 b) { return __builtin_bit_cast(bf16x8, __builtin_shufflevector(a, b, 0, 1, 2, 3, 4, 5, 6, 7)); }
template <int C> DEV float dppadd(float v) { return v + __int_as_float(__builtin_amdgcn_update_dpp(0, __float_as_int(v), C, 0xf, 0xf, true)); }
DEV float swap32add(float v) { auto r = __builtin_amdgcn_permlane32_swap(__float_as_uint(v), __float_as_uint(v), false, false); return __uint_as_float(r[0]) + __uint_as_float(r[1]); }
DEV float swap16add(float v) { auto r = __builtin_amdgcn_permlane16_swap(__float_as_uint(v), __float_as_uint(v), false, false); return __uint_as_float(r[0]) + __uint_as_float(r[1]); }
DEV float swap32max(float v) { auto r = __builtin_amdgcn_permlane32_swap(__float_as_uint(v), __float_as_uint(v), false, false); return __builtin_fmaxf(__uint_as_float(r[0]), __uint_as_float(r[1])); }
DEV float wavesum(float v) { v = dppadd<0xB1>(v); v = dppadd<0x4E>(v); v = dppadd<0x141>(v); v = dppadd<0x140>(v); v = swap16add(v); return swap32add(v); }

#define SETM0(x) asm volatile("s_mov_b32 m0, %0\n\ts_nop 0" ::"s"(x) : "memory")
template <int OFF> DEV void glds(unsigned voff, const void* base) { asm volatile("global_load_lds_dwordx4 %0, %1 offset:%2" ::"v"(voff), "s"(base), "i"(OFF) : "memory"); }
template <int N> DEV void waitbar() { asm volatile("s_waitcnt vmcnt(%0) lgkmcnt(0)\n\ts_barrier" ::"i"(N) : "memory"); }
template <int OFF> DEV u32x4 gload(unsigned voff, const void* base) { u32x4 r; asm volatile("global_load_dwordx4 %0, %1, %2 offset:%3" : "=v"(r) : "v"(voff), "s"(base), "i"(OFF) : "memory"); return r; }
template <int OFF> DEV u32x2 gload2(unsigned voff, const void* base) { u32x2 r; asm volatile("global_load_dwordx2 %0, %1, %2 offset:%3" : "=v"(r) : "v"(voff), "s"(base), "i"(OFF) : "memory"); return r; }
template <int OFF> DEV unsigned gload1(unsigned voff, const void* base) { unsigned r; asm volatile("global_load_dword %0, %1, %2 offset:%3" : "=v"(r) : "v"(voff), "s"(base), "i"(OFF) : "memory"); return r; }
template <int OFF> DEV u32x4 gloadd(unsigned voff, const void* base) { u32x4 r; asm volatile("global_load_dwordx4 %0, %1, %2 offset:%3 sc1" : "=v"(r) : "v"(voff), "s"(base), "i"(OFF) : "memory"); return r; }
template <int OFF> DEV void gstore2(unsigned voff, void* base, u32x2 v) { asm volatile("global_store_dwordx2 %0, %1, %2 offset:%3 sc1" ::"v"(voff), "v"(v), "s"(base), "i"(OFF) : "memory"); }
template <int OFF> DEV void gstore(unsigned voff, void* base, u32x4 v) { asm volatile("global_store_dwordx4 %0, %1, %2 offset:%3 sc1\n\ts_nop 1" ::"v"(voff), "v"(v), "s"(base), "i"(OFF) : "memory"); }
#define CODE_RANGE(A, B, base, size) const char* base; unsigned size; { unsigned long long pc_; int o_; \
  asm volatile("s_getpc_b64 %0\n.Lcr%=:\n\ts_mov_b32 %1, " #A "-.Lcr%=\n\ts_mov_b32 %2, " #B "-" #A : "=s"(pc_), "=s"(o_), "=s"(size)); base = (const char*)(pc_ + o_); }
#define SB() __builtin_amdgcn_sched_barrier(0)
extern __shared__ __attribute__((aligned(16))) char smem_raw[];

struct Stamps {};
#define ST_INIT() Stamps S_
#define ST(i)
#define ST_END(k)

constexpr int K1_W = 0, K1_H = 65536, K1_MV = K1_H + 64 * 528, K1_RED = K1_MV + 8192, K1_LDS = K1_RED + 32768;
extern "C" __global__ __launch_bounds__(512, 2) void k_ln1kv(const char* __restrict__ x, const char* __restrict__ wk, const char* __restrict__ wv,
                                                  const char* __restrict__ modp, float* __restrict__ modf, uint16_t* __restrict__ modh,
                                                  char* __restrict__ hbuf, char* __restrict__ kv) {
  const lptr sm = (lptr)smem_raw;
  ST_INIT();
  const int b = blockIdx.x, t = threadIdx.x, l = t & 63, w = __builtin_amdgcn_readfirstlane(t >> 6);
  const int which = (b >> 3) & 1, rb = (b & 7) * 16 + (b >> 4);
  u32x4 xv[4], mp[4];
  { const char* xb = x + (size_t)(rb * 16 + 2 * w) * 2048; const unsigned vo = l * 32;
    xv[0] = gload<0>(vo, xb); xv[1] = gload<16>(vo, xb); xv[2] = gload<2048>(vo, xb); xv[3] = gload<2064>(vo, xb);
#pragma unroll
    for (int s = 0; s < 4; ++s) mp[s] = gload<0>(t * 16, modp + s * NMOD * 4); }
  { const char* wsrc = (which ? wv : wk) + w * 8192;
#pragma unroll
    for (int g = 0; g < 2; ++g) { SETM0(K1_W + w * 8192 + g * 4096); const char* s = wsrc + g * 4096;
      glds<0>(l * 16, s); glds<1024>(l * 16, s); glds<2048>(l * 16, s); glds<3072>(l * 16, s); } }
  asm volatile("s_waitcnt vmcnt(8)" : "+v"(xv[0]), "+v"(xv[1]), "+v"(xv[2]), "+v"(xv[3]), "+v"(mp[0]), "+v"(mp[1]), "+v"(mp[2]), "+v"(mp[3])::"memory");
  ST(1);
  { f32x4 o;
#pragma unroll
    for (int e = 0; e < 4; ++e) { const float v = rbf((__uint_as_float(mp[0][e]) + __uint_as_float(mp[1][e])) + (__uint_as_float(mp[2][e]) + __uint_as_float(mp[3][e])));
      o[e] = t >= 256 ? rbf(1.f + v) : v; }
    *(LDS f32x4*)(sm + K1_MV + (t & 256) * 16 + (t & 3) * 1024 + ((t & 255) >> 2) * 16) = o; }
  f32x2 v[2][8]; float rs[2];
#pragma unroll
  for (int r = 0; r < 2; ++r) { f32x2 s = {0.f, 0.f};
#pragma unroll
    for (int e = 0; e < 8; ++e) { const unsigned u = xv[2 * r + (e >> 2)][e & 3]; v[r][e] = (f32x2){blo(u), bhi(u)}; s += v[r][e]; }
    const float m = wavesum(s[0] + s[1]) * (1.f / 1024.f); f32x2 q = {0.f, 0.f};
#pragma unroll
    for (int e = 0; e < 8; ++e) { v[r][e] -= m; q += v[r][e] * v[r][e]; }
    rs[r] = __builtin_amdgcn_rsqf(wavesum(q[0] + q[1]) * (1.f / 1024.f) + 1e-5f); }
  asm volatile("s_waitcnt lgkmcnt(0)\n\ts_barrier" ::: "memory");
  { unsigned u[2][8];
#pragma unroll
    for (int e = 0; e < 4; ++e) { const f32x4 a = *(const LDS f32x4*)(sm + K1_MV + e * 1024 + l * 16), cc = *(const LDS f32x4*)(sm + K1_MV + 4096 + e * 1024 + l * 16);
#pragma unroll
      for (int r = 0; r < 2; ++r)
#pragma unroll
        for (int i = 0; i < 2; ++i) { const f32x2 h = v[r][2 * e + i] * rs[r] * (f32x2){cc[2 * i], cc[2 * i + 1]} + (f32x2){a[2 * i], a[2 * i + 1]}; u[r][2 * e + i] = pk(h[0], h[1]); } }
#pragma unroll
    for (int r = 0; r < 2; ++r) { const lptr d = sm + K1_H + l * 528 + (2 * w + r) * 16;
      *(LDS u32x4*)d = (u32x4){u[r][0], u[r][1], u[r][4], u[r][5]}; *(LDS u32x4*)(d + 256) = (u32x4){u[r][2], u[r][3], u[r][6], u[r][7]}; } }
  ST(2); waitbar<0>(); ST(3);
  f32x16 acc = {};
  { const int hi = l >> 5; const lptr hp = sm + K1_H + w * 8 * 528 + hi * 256 + (l & 15) * 16;
    const lptr wp = sm + K1_W + (w * 128 + 4 * hi + ((l >> 2) & 3)) * 64 + ((l >> 4) & 1) * 32 + (l & 3) * 8;
    bf16x8 fa[8], fb[8];
#pragma unroll
    for (int i = 0; i < 8; ++i) { fa[i] = ldsA(hp + i * 528); fb[i] = ldsT(wp + i * 1024, 512); }
#pragma unroll
    for (int i = 0; i < 2; ++i) { const int id = t + 512 * i, tok = id & 15, h2 = (id >> 4) & 1, kb = which * 32 + (id >> 5);
      gstore<0>(kb * 1024 + (h2 * 32 + tok) * 16, hbuf + (size_t)(rb >> 1) * 65536 + (rb & 1) * 256, *(const LDS u32x4*)(sm + K1_H + kb * 528 + h2 * 256 + tok * 16)); }
    if (which == 0) {
#pragma unroll
      for (int i = 0; i < 8; ++i) acc = mfma32(fb[i], fa[i], acc);
    } else {
#pragma unroll
      for (int i = 0; i < 8; ++i) acc = mfma32(fa[i], fb[i], acc);
    } }
#pragma unroll
  for (int q = 0; q < 4; ++q) *(LDS f32x4*)(sm + K1_RED + w * 4096 + q * 1024 + l * 16) = (f32x4){acc[4 * q], acc[4 * q + 1], acc[4 * q + 2], acc[4 * q + 3]};
  asm volatile("s_waitcnt lgkmcnt(0)\n\ts_barrier" ::: "memory");
  if (w == 0) {
    const lptr rp = sm + K1_RED + (which ? l * 16 : (l >> 5) * 2048 + (((l >> 4) & 1) * 32 + (l & 15)) * 16);
    f32x4 a = {}, c = {};
#pragma unroll
    for (int u = 0; u < 8; ++u) { a += *(const LDS f32x4*)(rp + u * 4096); c += *(const LDS f32x4*)(rp + u * 4096 + 1024); }
    char* base = kv + (rb >> 1) * 4096;
    char* d = which ? base + 2048 + (rb & 1) * 1024 + l * 16 : base + (l >> 5) * 1024 + (((l >> 4) & 1) * 32 + (rb & 1) * 16 + (l & 15)) * 16;
    *(u32x4*)d = (u32x4){pk(a[0], a[1]), pk(a[2], a[3]), pk(c[0], c[1]), pk(c[2], c[3])};
  } else if (b < 64 && w == 1) {
    const int col = 2048 + b * 64 + l; const float* mpf = (const float*)modp;
    float v2 = rbf((mpf[col] + mpf[NMOD + col]) + (mpf[2 * NMOD + col] + mpf[3 * NMOD + col]));
    if ((col >> 10) == 4) v2 = rbf(1.f + v2);
    modf[col] = v2; modh[col] = (uint16_t)(__float_as_uint(v2) >> 16);
  }
  ST_END(1);
}

DEV void mfma_done() { SB(); asm volatile("s_nop 7\n\ts_nop 7\n\ts_nop 7"); SB(); }
DEV void bar() { asm volatile("s_waitcnt lgkmcnt(0)\n\ts_barrier" ::: "memory"); }
template <int N, int G, int EXTRA> DEV void loader_tail() { waitbar<G * N + EXTRA>(); if constexpr (N > 0) loader_tail<N - 1, G, EXTRA>(); }
constexpr int issued(int t, int S) { return t == 0 ? 2 : (2 + 2 * t < S + t - 1 ? 2 + 2 * t : S + t - 1); }
template <int T, int S, int G, int STAGE, class I, class E> DEV void loader_ramp(I&& issue, E&& early) {
  if constexpr (T == 1) early();
#pragma unroll
  for (int s = T == 0 ? 0 : issued(T - 1, S); s < issued(T, S); ++s) issue(s, (s % S) * STAGE);
  waitbar<G*(issued(T, S) - T - 1)>();
  if constexpr (issued(T, S) < S + T - 1 || T == 0) loader_ramp<T + 1, S, G, STAGE>(issue, early);
}
constexpr int ramp_end(int S) { int t = 1; while (issued(t, S) < S + t - 1) ++t; return t; }
template <int KT, int S, int G, int STAGE, int EXTRA, class I, class X, class E>
DEV void loader(I&& issue, X&& extra, E&& early) {
  loader_ramp<0, S, G, STAGE>(issue, early);
  constexpr int T0 = ramp_end(S);
  int slot = ((S + T0 - 1) % S) * STAGE;
  for (int t = T0; t <= KT - S; ++t) {
    issue(t - 1 + S, slot);
 slot = slot == (S - 1) * STAGE ? 0 : slot + STAGE; if (t < KT - S) waitbar<G*(S - 2)>(); }
  extra();
  loader_tail<S - 2, G, EXTRA>();
}
constexpr int GA_STAGE = 24576;
template <int KT, int S, int LDW, int EXTRA, class X>
DEV void gemmA_load(const char* act, const char* wgt, int l, int lw, X&& extra) {
  const char* ap = act + (size_t)lw * (KT * 4096);
  const char* wp = wgt + (size_t)lw * (16 * LDW * 2);
  const unsigned va = l * 16, vb = (l >> 3) * (LDW * 2) + (((l & 7) ^ (((l >> 4) & 1) << 2)) * 16);
  loader<KT, S, 6, GA_STAGE, EXTRA>([&](int t, int slot) {
    SETM0(slot + lw * 4096); const char* a = ap + (size_t)t * 4096; glds<0>(va, a); glds<1024>(va, a); glds<2048>(va, a); glds<3072>(va, a);
    SETM0(slot + 16384 + lw * 2048); const char* s = wp + (size_t)t * (64 * LDW * 2); glds<0>(vb, s); glds<1024>(vb, s + 8 * LDW * 2 - 1024); }, extra, [] {});
}
struct FragA { bf16x8 a0[4], a1[4], b[4]; };
template <int KT, int S, class H>
DEV void gemmA_compute(const lptr sm, int l, int w, f32x16& c0, f32x16& c1, Stamps& S_, H&& hook) {
  const int q = (l >> 2) & 3, p = l & 3;
  const lptr ra = sm + (w & 1) * 8192 + l * 16;
  const lptr rb = sm + 16384 + (4 * (l >> 5) + q) * 128 + ((((w >> 1) * 4 + ((l >> 4) & 1) * 2 + (p >> 1)) ^ ((q >> 1) << 2)) * 16) + (p & 1) * 8;
  c0 = (f32x16){}; c1 = (f32x16){};
  FragA f, g; int slot = 0;
  auto rd = [&](FragA& d, int k) { d.a0[k] = ldsA(ra + slot + k * 1024); d.b[k] = ldsT(rb + slot + k * 2048, 1024); d.a1[k] = ldsA(ra + slot + 4096 + k * 1024); };
  auto step = [&](FragA& cur, FragA& nxt) { bar(); slot = slot == (S - 1) * GA_STAGE ? 0 : slot + GA_STAGE;
#pragma unroll
    for (int k = 0; k < 4; ++k) { if (k < 2) rd(nxt, 2 * k); c0 = mfma32(cur.b[k], cur.a0[k], c0); SB(); if (k < 2) rd(nxt, 2 * k + 1); c1 = mfma32(cur.b[k], cur.a1[k], c1); SB(); } };
  bar(); ST(2);
#pragma unroll
  for (int k = 0; k < 4; ++k) rd(f, k);
  hook();
  for (int t = 0; t < KT - 2; t += 2) { step(f, g); step(g, f); }
  step(f, g);
#pragma unroll
  for (int k = 0; k < 4; ++k) { c0 = mfma32(g.b[k], g.a0[k], c0); c1 = mfma32(g.b[k], g.a1[k], c1); }
  mfma_done();
  ST(3);
}

constexpr int K2_KV0 = 98304, K2_KV1 = 131072, K2_KV2 = 0, K2_UW = 32768, K2_Q = 65536, K2_RED = 73728, K2_DUMP = 81920, K2_LDS = 163840;
DEV void pack16(const f32x16& s, u32x4& a, u32x4& b) {
  a = (u32x4){pk(s[0], s[1]), pk(s[2], s[3]), pk(s[4], s[5]), pk(s[6], s[7])};
  b = (u32x4){pk(s[8], s[9]), pk(s[10], s[11]), pk(s[12], s[13]), pk(s[14], s[15])}; }
DEV float max16(const f32x16& s) { float m = s[0];
#pragma unroll
  for (int i = 1; i < 16; ++i) m = __builtin_fmaxf(m, s[i]);
  return swap32max(m); }
#define EX2(v) __builtin_amdgcn_exp2f(v)
#define PIN(x) asm volatile("" : "+v"(x))

extern "C" __global__ __launch_bounds__(512, 2) void k_qattn(const char* __restrict__ hbuf, const char* __restrict__ wq, const char* __restrict__ kv, char* __restrict__ obuf,
                                                  const char* __restrict__ w1, const char* __restrict__ modh, float* __restrict__ utp,
                                                  const char* __restrict__ wo, const char* __restrict__ w2, char* __restrict__ xch) {
  const lptr sm = (lptr)smem_raw;
  ST_INIT();
  const int b = blockIdx.x, l = threadIdx.x & 63, w = __builtin_amdgcn_readfirstlane(threadIdx.x >> 6);
  const int xcd = b & 7, j = b >> 3, tb = (xcd >> 1) * 4 + (j & 3), hp = (xcd & 1) * 8 + (j >> 2);
  const unsigned l16 = l * 16;
  if (w >= 4) { const int lw = w - 4;
    auto kvload = [&](int st, int slot) {
#pragma unroll
      for (int g = 0; g < 2; ++g) { SETM0(slot + lw * 8192 + g * 4096); const char* s = kv + st * 32768 + lw * 8192 + g * 4096;
        glds<0>(l16, s); glds<1024>(l16, s); glds<2048>(l16, s); glds<3072>(l16, s); } };
    gemmA_load<16, 4, DM, 16>(hbuf + (size_t)tb * 4 * 64 * 1024, wq + hp * 128, l, lw, [&] { kvload(0, K2_KV0); kvload(1, K2_KV1); });
    waitbar<8>();
    const int ub = b < 224 ? b : b - 224, cg = ub % 56, ks = ub / 56;
    u32x2 vf[8];
    auto touch = [&](const char* s) { glds<0>(l16, s); };
    waitbar<0>(); kvload(2, K2_KV2);
    { const unsigned vo = (l >> 3) * (FF * 2) + (((l & 7) ^ (((l >> 4) & 1) << 2)) * 16);
#pragma unroll
      for (int g = 0; g < 2; ++g) { SETM0(K2_UW + lw * 8192 + g * 4096); const char* s = w1 + (size_t)(ks * 256 + lw * 64 + g * 32) * (FF * 2) + cg * 128;
        glds<0>(vo, s); glds<1024>(vo, s + 8 * FF * 2 - 1024); glds<2048>(vo, s + 16 * FF * 2 - 2048); glds<3072>(vo, s + 24 * FF * 2 - 3072); }
      const char* mv = modh + (ks * 256 + lw * 64) * 2; const unsigned vm = ((4 - (l & 1)) * DM + 4 * (l >> 5)) * 2;
      vf[0] = gload2<0>(vm, mv); vf[1] = gload2<16>(vm, mv); vf[2] = gload2<32>(vm, mv); vf[3] = gload2<48>(vm, mv);
      vf[4] = gload2<64>(vm, mv); vf[5] = gload2<80>(vm, mv); vf[6] = gload2<96>(vm, mv); vf[7] = gload2<112>(vm, mv); }
    waitbar<16>(); kvload(3, K2_KV0);
    SETM0(K2_DUMP + lw * 1024);
    touch(wo + b * 8192 + lw * 2048); touch(wo + b * 8192 + lw * 2048 + 1024);
#pragma unroll
    for (int i = 0; i < 7; ++i) touch(w2 + (size_t)b * 28672 + lw * 7168 + i * 1024);
    waitbar<9>(); kvload(4, K2_KV1);
    { const int q = (l >> 2) & 3, p = l & 3; f32x16 d0 = {}, d1 = {};
      const lptr rb = sm + K2_UW + (lw * 64 + 4 * (l >> 5) + q) * 128 + (((((l >> 4) & 1) * 2 + (p >> 1)) ^ ((q >> 1) << 2)) * 16) + (p & 1) * 8;
      const lptr rc = sm + K2_UW + (lw * 64 + 4 * (l >> 5) + q) * 128 + (((4 + ((l >> 4) & 1) * 2 + (p >> 1)) ^ ((q >> 1) << 2)) * 16) + (p & 1) * 8;
#pragma unroll
      for (int kk = 0; kk < 4; ++kk) { PIN(vf[2 * kk]); PIN(vf[2 * kk + 1]); const bf16x8 vv = asb((u32x4){vf[2 * kk][0], vf[2 * kk][1], vf[2 * kk + 1][0], vf[2 * kk + 1][1]});
        d0 = mfma32(ldsT(rb + kk * 2048, 1024), vv, d0); d1 = mfma32(ldsT(rc + kk * 2048, 1024), vv, d1); }
      if ((l & 31) < 2) { const lptr d = sm + K2_RED + lw * 512 + (l & 31) * 256 + (l >> 5) * 16;
#pragma unroll
        for (int i = 0; i < 4; ++i) { *(LDS f32x4*)(d + i * 32) = (f32x4){d0[4 * i], d0[4 * i + 1], d0[4 * i + 2], d0[4 * i + 3]};
          *(LDS f32x4*)(d + 128 + i * 32) = (f32x4){d1[4 * i], d1[4 * i + 1], d1[4 * i + 2], d1[4 * i + 3]}; } } }
    waitbar<0>(); kvload(5, K2_KV2);
    if (b < 224 && lw < 2) { float v = 0.f;
#pragma unroll
      for (int u = 0; u < 4; ++u) v += *(const LDS float*)(sm + K2_RED + u * 512 + lw * 256 + l * 4);
      utp[(ks * 2 + lw) * FF + cg * 64 + l] = v; }
    waitbar<0>(); kvload(6, K2_KV0);
    { char* d = xch + b * 16384 + lw * 4096; const u32x4 e = {~0u, ~0u, ~0u, ~0u}; gstore<0>(l16, d, e); gstore<1024>(l16, d, e); gstore<2048>(l16, d, e); gstore<3072>(l16, d, e); }
    waitbar<4>(); kvload(7, K2_KV1);
    { CODE_RANGE(k_oproj, k_mod, cb, cs) const unsigned o = (j & 7) * 4096 + lw * 1024 + l16; SETM0(K2_DUMP + lw * 1024); glds<0>(o < cs ? o : 0, cb); }
    waitbar<1>();
    asm volatile("s_waitcnt vmcnt(0)" ::: "memory");
    return; }
  bf16x8 qA0, qA1, qB0, qB1;
  { f32x16 c0, c1; gemmA_compute<16, 4>(sm, l, w, c0, c1, S_, [] {});
    constexpr float QS = 0.17677669529663687f * 1.4426950408889634f;
    c0 *= QS; c1 *= QS;
    u32x4 a, c; pack16(c0, a, c); qA0 = asb(a); qA1 = asb(c); pack16(c1, a, c); qB0 = asb(a); qB1 = asb(c); }
  bar();
  const unsigned one2 = ((l & 7) == ((l >> 4) & 1) * 4) ? 0x3f803f80u : 0u;
  const bf16x8 ones = asb((u32x4){one2, one2, one2, one2});
  const lptr kp0 = sm + K2_KV0 + l16, kp1 = sm + K2_KV1 + l16, kp2 = sm + K2_KV2 + l16;
  bf16x8 k0 = ldsA(kp0), k1 = ldsA(kp0 + 1024), v0 = ldsA(kp0 + 2048), v1 = k0;
  f32x16 nA, nB, sA, sB, oA = {}, oB = {}; f32x4 lA = {}, lB = {};
  { f32x16 z = {}, y = {}; z = mfma32(k0, qA0, z); y = mfma32(k0, qB0, y); z = mfma32(k1, qA1, z); y = mfma32(k1, qB1, y); const float m = -max16(z), n = -max16(y);
#pragma unroll
    for (int i = 0; i < 16; ++i) { nA[i] = m; nB[i] = n; } }
  PIN(nA); PIN(nB);
  sA = mfma32(k0, qA0, nA); sA = mfma32(k1, qA1, sA);
  u32x4 p0, p1 = {0, 0, 0, 0};
#define HALF(S, N, Q0, Q1, NEG, OS, LS, ON, LN, R0, R1, R2, R3)                                                        \
  { S[0] = EX2(S[0]); S[1] = EX2(S[1]); PIN(S); SB();                                                                 \
    N = mfma32(k0, Q0, NEG); SB(); R0; SB();                                                                          \
    S[2] = EX2(S[2]); S[3] = EX2(S[3]); p0[0] = pk(S[0], S[1]); p0[1] = pk(S[2], S[3]); PIN(p0); SB();                \
    ON = mfma32(v1, asb(p1), ON); SB(); R1; SB();                                                                     \
    S[4] = EX2(S[4]); S[5] = EX2(S[5]); S[6] = EX2(S[6]); S[7] = EX2(S[7]); p0[2] = pk(S[4], S[5]); p0[3] = pk(S[6], S[7]); PIN(p0); SB(); \
    LN = mfma16(ones, asb(p1), LN); SB();                                                                             \
    S[8] = EX2(S[8]); S[9] = EX2(S[9]); PIN(S); SB();                                                                 \
    N = mfma32(k1, Q1, N); SB(); R2; SB();                                                                            \
    S[10] = EX2(S[10]); S[11] = EX2(S[11]); S[12] = EX2(S[12]); S[13] = EX2(S[13]); p1[0] = pk(S[8], S[9]); p1[1] = pk(S[10], S[11]); PIN(p1); PIN(S); SB(); \
    OS = mfma32(v0, asb(p0), OS); SB(); R3; SB();                                                                     \
    S[14] = EX2(S[14]); S[15] = EX2(S[15]); p1[2] = pk(S[12], S[13]); p1[3] = pk(S[14], S[15]); PIN(p1); SB();        \
    LS = mfma16(ones, asb(p0), LS); SB(); }
#define TILE(T, X) HALF(sA, sB, qB0, qB1, nB, oA, lA, oB, lB, k0 = ldsA(X), v1 = ldsA((T) + 3072), k1 = ldsA((X) + 1024), )  \
                   HALF(sB, sA, qA0, qA1, nA, oB, lB, oA, lA, , , , v0 = ldsA((X) + 2048))
#define STAGE(P, Q, BAR) TILE(P, P + 4096) TILE(P + 4096, P + 2 * 4096) TILE(P + 2 * 4096, P + 3 * 4096) TILE(P + 3 * 4096, P + 4 * 4096) \
  TILE(P + 4 * 4096, P + 5 * 4096) TILE(P + 5 * 4096, P + 6 * 4096) BAR TILE(P + 6 * 4096, P + 7 * 4096) TILE(P + 7 * 4096, Q)
  ST(4);
  { lptr c = kp0, n = kp1, f = kp2;
    for (int st = 0; st < 7; ++st) { STAGE(c, n, bar();) const lptr t = c; c = n; n = f; f = t; }
    STAGE(c, n, ) }
  ST(5);
  oB = mfma32(v1, asb(p1), oB); lB = mfma16(ones, asb(p1), lB);
  mfma_done();
  auto finish = [&](f32x16& o, float lsum, bf16x8 q0, bf16x8 q1, int i) {
    float chk = lsum;
#pragma unroll
    for (int e = 0; e < 16; ++e) chk = __builtin_fmaxf(chk, __builtin_fabsf(o[e]) * 0x1p-40f);
    bool bad = !(chk < 0x1p80f);
#pragma unroll
    for (int e = 0; e < 16; ++e) bad |= (o[e] != o[e]);
    bad |= (lsum != lsum);
    if (__any(bad)) {
      float m = -INFINITY; lsum = 0.f; o = (f32x16){};
      for (int t = 0; t < 64; ++t) { const bf16x8* g = (const bf16x8*)(kv + t * 4096) + l;
        f32x16 s = {}; s = mfma32(g[0], q0, s); s = mfma32(g[64], q1, s); mfma_done();
        const float mn = __builtin_fmaxf(m, max16(s)), al = EX2(m - mn); m = mn; float rsum = 0.f;
#pragma unroll
        for (int e = 0; e < 16; ++e) { s[e] = rbf(EX2(s[e] - mn)); rsum += s[e]; o[e] *= al; }
        lsum = lsum * al + swap32add(rsum);
        u32x4 a, c; pack16(s, a, c); o = mfma32(g[128], asb(a), o); o = mfma32(g[192], asb(c), o); mfma_done(); } }
    o *= 1.f / lsum;
    u32x4 o0, o1; pack16(o, o0, o1);
    char* d = obuf + (size_t)((tb * 4 + 2 * (w & 1) + i) * 64 + (hp * 2 + (w >> 1)) * 2) * 1024;
    gstore<0>(l16, d, o0); gstore<1024>(l16, d, o1); };
  finish(oA, lA[0], qA0, qA1, 0); finish(oB, lB[0], qB0, qB1, 1);
  ST_END(2);
}

DEV u32x4 rowswap(u32x4 v) { auto a = __builtin_amdgcn_permlane32_swap(v[0], v[2], false, false); auto c = __builtin_amdgcn_permlane32_swap(v[1], v[3], false, false);
  return (u32x4){a[0], c[0], a[1], c[1]}; }

constexpr int K3_S = 6, K3_LDS = K3_S * GA_STAGE;
extern "C" __global__ __launch_bounds__(512, 2) void k_oproj(const char* __restrict__ obuf, const char* __restrict__ wo, const char* __restrict__ x, const float* __restrict__ modf,
                                                  char* __restrict__ x1b, char* __restrict__ yb, f32x2* __restrict__ stat) {
  const lptr sm = (lptr)smem_raw;
  ST_INIT();
  const int b = blockIdx.x, l = threadIdx.x & 63, w = __builtin_amdgcn_readfirstlane(threadIdx.x >> 6);
  const int xcd = b & 7, j = b >> 3, tb = (xcd >> 1) * 4 + (j & 3), nt = (xcd & 1) * 8 + (j >> 2);
  if (__builtin_expect(w >= 4, LFIRST)) { u32x4 e; gemmA_load<16, K3_S, DM, NEXTPF>(obuf + (size_t)tb * 4 * 64 * 1024, wo + nt * 128, l, w - 4, [&] {
      if (NEXTPF) { CODE_RANGE(k_ffn1, k_ffn2, cb, cs) const unsigned o = (j & 3) * 4096 + (w - 4) * 1024 + l * 16; e = gload<0>(o < cs ? o : 0, cb); } });
    if (NEXTPF) asm volatile("s_waitcnt vmcnt(0)" : "+v"(e)::"memory"); return; }
  const int hi = l >> 5, tb32 = tb * 4 + 2 * (w & 1), n0 = nt * 64 + (w >> 1) * 32;
  u32x4 xr[2][2]; f32x4 ga[4], cb[4];
  f32x16 acc[2]; gemmA_compute<16, K3_S>(sm, l, w, acc[0], acc[1], S_, [&] {
#pragma unroll
    for (int i = 0; i < 2; ++i)
#pragma unroll
      for (int h = 0; h < 2; ++h) xr[i][h] = *(const u32x4*)(x + (size_t)((tb32 + i) * 32 + (l & 31)) * 2048 + (n0 + 16 * h + 8 * hi) * 2);
#pragma unroll
    for (int q = 0; q < 4; ++q) { ga[q] = *(const f32x4*)(modf + 2 * DM + n0 + 8 * q + 4 * hi); cb[q] = *(const f32x4*)(modf + 4 * DM + n0 + 8 * q + 4 * hi); } });
#pragma unroll
  for (int i = 0; i < 2; ++i) { unsigned xo[8], yo[8]; float x1[16], s = 0.f;
#pragma unroll
    for (int h = 0; h < 2; ++h) { const u32x4 xs = rowswap(xr[i][h]);
#pragma unroll
      for (int e = 0; e < 4; ++e) { const int r = 8 * h + 2 * e, q = r >> 2, c = r & 3;
        const unsigned u = pk(blo(xs[e]) + ga[q][c] * acc[i][r], bhi(xs[e]) + ga[q][c + 1] * acc[i][r + 1]);
        x1[r] = blo(u); x1[r + 1] = bhi(u); xo[4 * h + e] = u; yo[4 * h + e] = pk(x1[r] * cb[q][c], x1[r + 1] * cb[q][c + 1]); s += x1[r] + x1[r + 1]; } }
    const size_t off = (size_t)((tb32 + i) * 64 + (n0 >> 4)) * 1024;
    gstore<0>(l * 16, yb + off, (u32x4){yo[0], yo[1], yo[2], yo[3]}); gstore<1024>(l * 16, yb + off, (u32x4){yo[4], yo[5], yo[6], yo[7]});
    gstore<0>(l * 16, x1b + off, (u32x4){xo[0], xo[1], xo[2], xo[3]}); gstore<1024>(l * 16, x1b + off, (u32x4){xo[4], xo[5], xo[6], xo[7]});
    const float mean = swap32add(s) * (1.f / 32.f); float m2 = 0.f;
#pragma unroll
    for (int e = 0; e < 16; ++e) m2 += (x1[e] - mean) * (x1[e] - mean);
    m2 = swap32add(m2);
    if (hi == 0) stat[((tb32 + i) * 32 + (n0 >> 5)) * 32 + l] = (f32x2){mean, m2}; }
  ST_END(3);
}

constexpr int K4_STAGE = 45056, K4_UT = 3 * K4_STAGE, K4_RS = K4_UT + 2048, K4_LDS = K4_RS + 1024;
struct FragB { bf16x8 a[4]; s16x4 b[14]; };
extern "C" __global__ __launch_bounds__(512, 2) void k_ffn1(const char* __restrict__ yb, const char* __restrict__ w1, const char* __restrict__ stat,
                                                            const char* __restrict__ utp, char* __restrict__ gb) {
  const lptr sm = (lptr)smem_raw;
  ST_INIT();
  const int b = blockIdx.x, t = threadIdx.x, l = t & 63, w = __builtin_amdgcn_readfirstlane(t >> 6);
  const int xcd = b & 7, j = b >> 3, tb = (xcd >> 2) * 8 + (j & 7), nt = (xcd & 3) * 4 + (j >> 3);
  constexpr float NL2E = -1.4426950408889634f;
  if (__builtin_expect(w >= 4, LFIRST)) { const int lw = w - 4;
    const char* ap = yb + (size_t)(tb * 4 + lw) * 64 * 1024; const char* wp = w1 + nt * 448;
    unsigned vb[7];
#pragma unroll
    for (int i = 0; i < 7; ++i) { const int c = (lw * 7 + i) * 64 + l, r = c / 28; vb[i] = r * (FF * 2) + ((c - r * 28) ^ ((r >> 1) & 2)) * 16 + 3072 - i * 1024; }
    const unsigned va = l * 16;
    u32x2 st[16]; unsigned up[8]; u32x4 e;
    loader<16, 3, 11, K4_STAGE, 1>([&](int tt, int slot) {
      SETM0(slot + lw * 4096); const char* a = ap + (size_t)tt * 4096;
      glds<0>(va, a); glds<1024>(va, a); glds<2048>(va, a); glds<3072>(va, a);
      SETM0(slot + 16384 + lw * 7168 + 3072); const char* s = wp + (size_t)tt * (64 * FF * 2);
      glds<-3072>(vb[0], s); glds<-2048>(vb[1], s); glds<-1024>(vb[2], s); glds<0>(vb[3], s); glds<1024>(vb[4], s); glds<2048>(vb[5], s); glds<3072>(vb[6], s); },
      [&] {
#pragma unroll
        for (int i = 0; i < 16; ++i) asm volatile("" : "+v"(st[i]));
#pragma unroll
        for (int i = 0; i < 8; ++i) asm volatile("" : "+v"(up[i]));
        float mean = 0.f;
#pragma unroll
        for (int i = 0; i < 16; ++i) mean += __uint_as_float(st[i][0]);
        mean = swap32add(mean) * (1.f / 32.f); float m2 = 0.f;
#pragma unroll
        for (int i = 0; i < 16; ++i) { const float d = __uint_as_float(st[i][0]) - mean; m2 += __uint_as_float(st[i][1]) + 32.f * d * d; }
        const float rs = NL2E * __builtin_amdgcn_rsqf(swap32add(m2) * (1.f / 1024.f) + 1e-5f);
        if (l < 32) *(LDS f32x2*)(sm + K4_RS + (lw * 32 + l) * 8) = (f32x2){rs, -rs * mean};
        auto f = [&](int i) { return __uint_as_float(up[i]); };
        *(LDS float*)(sm + K4_UT + (t - 256) * 4) = (f(0) + f(2)) + (f(4) + f(6)); *(LDS float*)(sm + K4_UT + 1024 + (t - 256) * 4) = NL2E * ((f(1) + f(3)) + (f(5) + f(7)));
        CODE_RANGE(k_ffn2, k_mod, cb, cs) const unsigned o = lw * 1024 + l * 16; e = gload<0>(o < cs ? o : 0, cb); },
      [&] { const char* sp = stat + (size_t)(tb * 4 + lw) * 8192; const unsigned vs = l * 8, vu = (nt * 224 + (t < 480 ? t - 256 : 0)) * 4;
#define S2(i) st[2 * i] = gload2<0>(vs, sp + i * 1024); st[2 * i + 1] = gload2<512>(vs, sp + i * 1024); up[i] = gload1<0>(vu, utp + i * FF * 4);
        S2(0) S2(1) S2(2) S2(3) S2(4) S2(5) S2(6) S2(7)
#undef S2
      });
    asm volatile("s_waitcnt vmcnt(0)" : "+v"(e)::"memory");
    return; }
  const int g = l >> 4, th = w & 1, nh = w >> 1, sw = (g & 1) * 32;
  const lptr ra = sm + th * 8192 + (g >> 1) * 1024 + (g & 1) * 512 + (l & 15) * 16;
  const lptr rb = sm + 16384 + (16 * (g >> 1) + 4 * (g & 1) + ((l >> 2) & 3)) * 448 + (l & 3) * 8;
  lptr base[4] = {ra, rb + nh * 256 + sw, rb + nh * 256 - sw, rb + (((6 + nh) * 32) ^ sw)};
  PIN(base[0]); PIN(base[1]); PIN(base[2]); PIN(base[3]);
  lptr p[4] = {base[0], base[1], base[2], base[3]};
  f32x4 acc[7][4];
  FragB f, h; int slot = 0;
  auto rd = [&](FragB& d, int i, int ks) {
    if (i < 4) d.a[i] = ldsA(p[0] + (i >> 1) * 4096 + (i & 1) * 256 + ks * 2048);
    else { const int n = (i - 4) >> 1; d.b[i - 4] = ldsT1((n == 6 ? p[3] : p[1 + (n & 1)] + n * 32) + ks * (32 * 448) + (i & 1) * (8 * 448)); } };
  auto kstep = [&](FragB& cur, FragB& nxt, int ks, auto first) {
#pragma unroll
    for (int n = 0; n < 7; ++n)
#pragma unroll
      for (int i = 0; i < 4; ++i) { const int m = n * 4 + i;
        acc[n][i] = mfma16(cat(cur.b[2 * n], cur.b[2 * n + 1]), cur.a[i], first ? (f32x4){} : acc[n][i]); SB();
        if (m < 18) rd(nxt, m, ks); else if (ks == 1 && m < 22) { p[m - 18] = base[m - 18] + slot; PIN(p[m - 18]); }
        SB(); } };
  bar(); ST(2);
#pragma unroll
  for (int i = 0; i < 18; ++i) rd(f, i, 0);
  slot = K4_STAGE; kstep(f, h, 1, std::true_type{}); bar(); kstep(h, f, 0, std::false_type{});
  for (int tt = 1; tt < 15; ++tt) { slot = slot == 2 * K4_STAGE ? 0 : slot + K4_STAGE; kstep(f, h, 1, std::false_type{}); bar(); kstep(h, f, 0, std::false_type{}); }
  kstep(f, h, 1, std::false_type{});
#pragma unroll
  for (int n = 0; n < 7; ++n)
#pragma unroll
    for (int i = 0; i < 4; ++i) acc[n][i] = mfma16(cat(h.b[2 * n], h.b[2 * n + 1]), h.a[i], acc[n][i]);
  mfma_done();
  ST(3);
  f32x2 rn[4];
#pragma unroll
  for (int i = 0; i < 4; ++i) rn[i] = *(const LDS f32x2*)(sm + K4_RS + (th * 64 + i * 16 + (l & 15)) * 8);
  struct UT { f32x4 u, t; };
  auto ut = [&](int n) { const int col = ((n < 6 ? 8 * nh + n : 6 + nh) * 16 + 4 * g) * 4; return UT{*(const LDS f32x4*)(sm + K4_UT + col), *(const LDS f32x4*)(sm + K4_UT + 1024 + col)}; };
  auto act = [&](int n, int i, const UT& c) { u32x2 o;
#pragma unroll
    for (int e = 0; e < 2; ++e) { const f32x2 z = rn[i][0] * (f32x2){acc[n][i][2 * e], acc[n][i][2 * e + 1]} + (rn[i][1] * (f32x2){c.u[2 * e], c.u[2 * e + 1]} + (f32x2){c.t[2 * e], c.t[2 * e + 1]});
      const f32x2 dn = NL2E * (f32x2){EX2(z[0]), EX2(z[1])} + NL2E;
      const f32x2 r = z * (f32x2){__builtin_amdgcn_rcpf(dn[0]), __builtin_amdgcn_rcpf(dn[1])}; o[e] = pk(r[0], r[1]); }
    return o; };
  auto dst = [&](int gi, int i) { const int kk = nt * 7 + gi; return gb + (size_t)(((tb * 56 + (kk >> 1)) * 8 + th * 4 + i) * 2 + (kk & 1)) * 1024; };
  const unsigned vo = l * 16;
  UT c[7];
#pragma unroll
  for (int n = 0; n < 2; ++n) c[n] = ut(n);
#pragma unroll
  for (int p = 0; p < 3; ++p) { c[2 * p + 2] = ut(2 * p + 2); if (p < 2) c[2 * p + 3] = ut(2 * p + 3);
#pragma unroll
    for (int i = 0; i < 4; ++i) { const u32x2 lo = act(2 * p, i, c[2 * p]), hi = act(2 * p + 1, i, c[2 * p + 1]); gstore<0>(vo, dst(4 * nh + p, i), (u32x4){lo[0], lo[1], hi[0], hi[1]}); } }
#pragma unroll
  for (int i = 0; i < 4; ++i) gstore2<0>(vo, dst(3, i) + nh * 8, act(6, i, c[6]));
  ST_END(4);
}

constexpr int K5_STAGE = 32768, K5_S = 5, K5_LDS = K5_S * K5_STAGE;
struct FragC { bf16x8 a[4]; s16x4 b[8]; };
extern "C" __global__ __launch_bounds__(512, 2) void k_ffn2(const char* __restrict__ gb, const char* __restrict__ w2, const char* __restrict__ x1b,
                                                            const float* __restrict__ modf, char* __restrict__ out, char* xch) {
  const lptr sm = (lptr)smem_raw;
  ST_INIT();
  const int b = blockIdx.x, l = threadIdx.x & 63, w = __builtin_amdgcn_readfirstlane(threadIdx.x >> 6);
  const int kh = b & 1, j = b >> 3, tb = ((b & 7) >> 1) * 4 + (j & 3), nt = j >> 2;
  if (__builtin_expect(w >= 4, LFIRST)) { const int lw = w - 4;
    const char* ap = gb + (size_t)(tb * 56 + kh * 28) * 16384 + lw * 4096; const char* wp = w2 + (size_t)(kh * 1792 + lw * 16) * 2048 + nt * 256;
    const unsigned va = l * 16, v0 = (l >> 4) * 2048 + ((l & 15) ^ ((l >> 4) * 2)) * 16, v1 = v0 ^ 128;
    loader<28, K5_S, 8, K5_STAGE, 0>([&](int t, int slot) {
      SETM0(slot + lw * 4096); const char* a = ap + (size_t)t * 16384; glds<0>(va, a); glds<1024>(va, a); glds<2048>(va, a); glds<3072>(va, a);
      SETM0(slot + 16384 + lw * 4096); const char* s = wp + (size_t)t * (64 * 2048);
      glds<0>(v0, s); glds<1024>(v1, s + 8192 - 1024); glds<2048>(v0, s + 16384 - 2048); glds<3072>(v1, s + 24576 - 3072); }, [] {}, [] {});
    return; }
  const int g = l >> 4, th = w & 1, nh = w >> 1, rx = 4 * (g & 1) + ((l >> 2) & 3);
  const int tok0 = tb * 128 + th * 64 + kh * 32, n0 = nt * 128 + nh * 64;
  u32x2 xr[2][4]; f32x4 gv[4];
  lptr base[6], p[6];
  base[4] = sm + th * 8192 + kh * 4096 + l * 16; base[5] = sm + th * 8192 + (kh ^ 1) * 4096 + l * 16;
#pragma unroll
  for (int n = 0; n < 4; ++n) base[n] = sm + 16384 + (4 * g + ((l >> 2) & 3)) * 256 + ((4 * nh + n) ^ rx) * 32 + (l & 3) * 8;
#pragma unroll
  for (int n = 0; n < 6; ++n) { PIN(base[n]); p[n] = base[n]; }
  f32x4 acc[4][4];
  FragC f, h; int slot = 0;
  auto rd = [&](FragC& d, int i, int ks) {
    if (i < 4) d.a[i] = ldsA(p[4 + (i >> 1)] + (i & 1) * 2048 + ks * 1024); else d.b[i - 4] = ldsT1(p[(i - 4) >> 1] + ks * 8192 + (i & 1) * 4096); };
  auto kstep = [&](FragC& cur, FragC& nxt, int ks, auto first) {
#pragma unroll
    for (int n = 0; n < 4; ++n)
#pragma unroll
      for (int i = 0; i < 4; ++i) { const int m = n * 4 + i;
        acc[n][i] = mfma16(cat(cur.b[2 * n], cur.b[2 * n + 1]), cur.a[i], first ? (f32x4){} : acc[n][i]); SB();
        if (m < 12) rd(nxt, m, ks);
        else if (ks == 1) { p[m - 12] = base[m - 12] + slot; PIN(p[m - 12]); if (m < 14) { p[m - 8] = base[m - 8] + slot; PIN(p[m - 8]); } }
        SB(); } };
  bar(); ST(2);
#pragma unroll
  for (int i = 0; i < 12; ++i) rd(f, i, 0);
#pragma unroll
  for (int n = 0; n < 4; ++n) { gv[n] = *(const f32x4*)(modf + 5 * DM + n0 + 16 * n + 4 * g);
#pragma unroll
    for (int i = 0; i < 2; ++i) xr[i][n] = *(const u32x2*)(x1b + (size_t)((tok0 >> 5) * 64 + (n0 >> 4) + n) * 1024 + (g & 1) * 512 + (i * 16 + (l & 15)) * 16 + (g >> 1) * 8); }
  slot = K5_STAGE; kstep(f, h, 1, std::true_type{}); bar(); kstep(h, f, 0, std::false_type{});
  for (int tt = 1; tt < 25; ++tt) { slot = slot == (K5_S - 1) * K5_STAGE ? 0 : slot + K5_STAGE; kstep(f, h, 1, std::false_type{}); bar(); kstep(h, f, 0, std::false_type{}); }
  auto hstep = [&](FragC& cur, FragC& nxt, int half, int nhalf, int ns) {
    const int off = (ns >> 1) * K5_STAGE + (ns & 1) * 1024, offw = (ns >> 1) * K5_STAGE + (ns & 1) * 8192;
#pragma unroll
    for (int m = 0; m < 8; ++m) { const int n = m >> 1, i = 2 * half + (m & 1);
      acc[n][i] = mfma16(cat(cur.b[2 * n], cur.b[2 * n + 1]), cur.a[i], acc[n][i]); SB();
      if (ns >= 0) { if (m < 2) nxt.a[2 * nhalf + m] = ldsA(base[4 + nhalf] + off + m * 2048); nxt.b[m] = ldsT1(base[m >> 1] + offw + (m & 1) * 4096); }
      SB(); } };
  hstep(f, h, 1, 1, 1); bar(); hstep(h, f, 1, 1, 2); hstep(f, h, 1, 1, 3); bar(); hstep(h, f, 1, 1, 4); hstep(f, h, 1, 1, 5); hstep(h, f, 1, 0, 0);
  ST(3);
  hstep(f, h, 0, 0, 1);
  { char* d = xch + (size_t)(b * 4 + w) * 4096;
#pragma unroll
    for (int i = 0; i < 2; ++i)
#pragma unroll
      for (int q = 0; q < 2; ++q) { const f32x4 u = acc[2 * q][2 + i], v = acc[2 * q + 1][2 + i];
        gstore<0>(l * 16, d + (i * 2 + q) * 1024, (u32x4){min(pk(u[0], u[1]), ~1u), min(pk(u[2], u[3]), ~1u), min(pk(v[0], v[1]), ~1u), min(pk(v[2], v[3]), ~1u)}); } }
  SB();
  hstep(h, f, 0, 0, 2); hstep(f, h, 0, 0, 3); hstep(h, f, 0, 0, 4); hstep(f, h, 0, 0, 5); hstep(h, f, 0, 0, -1);
  ST(4);
  u32x4 pr[4];
  { const char* s = xch + (size_t)((b ^ 1) * 4 + w) * 4096; const unsigned vo = l * 16;
    for (int spin = 0; spin < (1 << 16); ++spin) {
      pr[0] = gloadd<0>(vo, s); pr[1] = gloadd<1024>(vo, s); pr[2] = gloadd<2048>(vo, s); pr[3] = gloadd<3072>(vo, s);
      asm volatile("s_waitcnt vmcnt(0)" : "+v"(pr[0]), "+v"(pr[1]), "+v"(pr[2]), "+v"(pr[3])::"memory");
      unsigned m = 0;
#pragma unroll
      for (int i = 0; i < 4; ++i) m = max(max(max(m, pr[i][0]), max(pr[i][1], pr[i][2])), pr[i][3]);
      if (!__any(m == ~0u)) break; } }
  ST(6);
#pragma unroll
  for (int i = 0; i < 2; ++i)
#pragma unroll
    for (int p = 0; p < 2; ++p) { unsigned o[2][2];
#pragma unroll
      for (int q = 0; q < 2; ++q) { const int n = 2 * p + q; const unsigned u0 = pr[2 * i + p][2 * q], u1 = pr[2 * i + p][2 * q + 1];
        const f32x4 a = acc[n][i] + (f32x4){blo(u0), bhi(u0), blo(u1), bhi(u1)};
        o[q][0] = pk(blo(xr[i][n][0]) + gv[n][0] * a[0], bhi(xr[i][n][0]) + gv[n][1] * a[1]); o[q][1] = pk(blo(xr[i][n][1]) + gv[n][2] * a[2], bhi(xr[i][n][1]) + gv[n][3] * a[3]); }
      auto r0 = __builtin_amdgcn_permlane16_swap(o[0][0], o[1][0], false, false); auto r1 = __builtin_amdgcn_permlane16_swap(o[0][1], o[1][1], false, false);
      gstore<0>((l & 15) * 2048 + (g & 1) * 32 + (g >> 1) * 16, out + (size_t)(tok0 + i * 16) * 2048 + (n0 + 32 * p) * 2, (u32x4){r0[0], r1[0], r0[1], r1[1]}); }
  ST_END(5);
}

extern "C" __global__ __launch_bounds__(256) void k_mod(const uint16_t* __restrict__ c, const char* __restrict__ wmod, float* __restrict__ modp,
                                             const char* __restrict__ x, const char* __restrict__ wq, const char* __restrict__ wk, const char* __restrict__ wv) {
  __shared__ float red[4][32];
  ST_INIT();
  const int t = threadIdx.x;
  if (blockIdx.x >= PF0 && blockIdx.x < PF0 + 256) { const int p = blockIdx.x - PF0; u32x4 d[7];
    const char* xb = x + (size_t)((p & 7) * 32 + (p >> 3)) * 16384;
#pragma unroll
    for (int i = 0; i < 4; ++i) d[i] = gload<0>(t * 16, xb + i * 4096);
    d[4] = gload<0>(t * 16, wq + p * 8192); d[5] = gload<0>(t * 16, wq + p * 8192 + 4096);
    d[6] = gload<0>((t & 31) * 16, (p & 128 ? wv : wk) + (p & 127) * 512);
    { CODE_RANGE(k_ln1kv, k_mod, cb, cs) const unsigned o = (p >> 3) * 2048 + t * 16; u32x4 e = gload<0>(t < 128 && o < cs ? o : 0, cb);
      asm volatile("s_waitcnt vmcnt(0)" : "+v"(e)::"memory"); }
    asm volatile("s_waitcnt vmcnt(0)" : "+v"(d[0]), "+v"(d[1]), "+v"(d[2]), "+v"(d[3]), "+v"(d[4]), "+v"(d[5]), "+v"(d[6])::"memory");
    return; }
  const int b = blockIdx.x < PF0 ? blockIdx.x : blockIdx.x - 256, xcd = b & 7, j = b >> 3;
  const int cg = (j & 1) + 2 * (xcd + 8 * ((j >> 1) % 12)), ks = j / 24;
  const int row0 = ks * 256 + (t >> 2);
  const char* p = wmod + (size_t)row0 * (NMOD * 2) + cg * 64 + (t & 3) * 16;
  u32x4 w[4]; float s[4];
#pragma unroll
  for (int i = 0; i < 4; ++i) { w[i] = *(const u32x4*)(p + (size_t)i * 64 * NMOD * 2); s[i] = __uint_as_float((unsigned)c[row0 + 64 * i] << 16); }
  float a[8] = {0, 0, 0, 0, 0, 0, 0, 0};
#pragma unroll
  for (int i = 0; i < 4; ++i) { const float f = rbf(silu(s[i]));
#pragma unroll
    for (int e = 0; e < 4; ++e) { a[2 * e] += f * blo(w[i][e]); a[2 * e + 1] += f * bhi(w[i][e]); } }
#pragma unroll
  for (int e = 0; e < 8; ++e) { float v = a[e]; v += __shfl_xor(v, 4); v += __shfl_xor(v, 8); v = swap16add(v); a[e] = swap32add(v); }
  if ((t & 63) < 4) {
#pragma unroll
    for (int e = 0; e < 8; ++e) red[t >> 6][(t & 3) * 8 + e] = a[e]; }
  __syncthreads();
  if (t < 32) modp[ks * NMOD + cg * 32 + t] = (red[0][t] + red[1][t]) + (red[2][t] + red[3][t]);
  ST_END(0);
}

extern "C" void kernel_launch(void* const* d_in, const int* in_sizes, int n_in, void* d_out, int out_size, void* d_ws, size_t ws_size, hipStream_t stream) {
  const char* x = (const char*)d_in[0]; const char* wq = (const char*)d_in[1]; const char* wk = (const char*)d_in[2]; const char* wv = (const char*)d_in[3];
  const char* wo = (const char*)d_in[4]; const char* w1 = (const char*)d_in[6]; const char* w2 = (const char*)d_in[7];
  const uint16_t* c = (const uint16_t*)d_in[8]; const char* wmod = (const char*)d_in[9];
  char* ws = (char*)d_ws;
  (void)hipFuncSetAttribute((const void*)k_ln1kv, hipFuncAttributeMaxDynamicSharedMemorySize, K1_LDS);
  (void)hipFuncSetAttribute((const void*)k_qattn, hipFuncAttributeMaxDynamicSharedMemorySize, K2_LDS);
  (void)hipFuncSetAttribute((const void*)k_oproj, hipFuncAttributeMaxDynamicSharedMemorySize, K3_LDS);
  (void)hipFuncSetAttribute((const void*)k_ffn1, hipFuncAttributeMaxDynamicSharedMemorySize, K4_LDS);
  (void)hipFuncSetAttribute((const void*)k_ffn2, hipFuncAttributeMaxDynamicSharedMemorySize, K5_LDS);
  float* modp = (float*)(ws + WS_MODP); float* modf = (float*)(ws + WS_MODF);
  k_mod<<<1024, 256, 0, stream>>>(c, wmod, modp, x, wq, wk, wv);
  k_ln1kv<<<256, 512, K1_LDS, stream>>>(x, wk, wv, (const char*)modp, modf, (uint16_t*)(ws + WS_MODH), ws + WS_H, ws + WS_KV);
  k_qattn<<<256, 512, K2_LDS, stream>>>(ws + WS_H, wq, ws + WS_KV, ws + WS_O, w1, ws + WS_MODH, (float*)(ws + WS_UTP), wo, w2, ws + WS_XCH);
  k_oproj<<<256, 512, K3_LDS, stream>>>(ws + WS_O, wo, x, modf, ws + WS_X1, ws + WS_Y, (f32x2*)(ws + WS_STAT));
  k_ffn1<<<256, 512, K4_LDS, stream>>>(ws + WS_Y, w1, ws + WS_STAT, ws + WS_UTP, ws + WS_G);
  k_ffn2<<<256, 512, K5_LDS, stream>>>(ws + WS_G, w2, ws + WS_X1, modf, (char*)d_out, ws + WS_XCH);
}
```

```cpp
#include <hip/hip_runtime.h>
#include <hip/hip_bf16.h>
#include <stdint.h>
#include <type_traits>

typedef __attribute__((ext_vector_type(2))) float f32x2;
typedef __attribute__((ext_vector_type(4))) float f32x4;
typedef __attribute__((ext_vector_type(16))) float f32x16;
typedef __attribute__((ext_vector_type(2))) __bf16 bf16x2;
typedef __attribute__((ext_vector_type(8))) __bf16 bf16x8;
typedef __attribute__((ext_vector_type(4))) short s16x4;
typedef __attribute__((ext_vector_type(2))) unsigned u32x2;
typedef __attribute__((ext_vector_type(4))) unsigned u32x4;
typedef __attribute__((ext_vector_type(8))) unsigned u32x8;
typedef __attribute__((ext_vector_type(16))) __bf16 bf16x16;
#define LDS __attribute__((address_space(3)))
typedef LDS char* lptr;
#define WPOS 1
#define W4POS 27
#define W5V 1
#define D2 4
#define D3 4
#define D5 4
#define U4 7
#define U5 6
#define PF0 768
#define NEXTPF 1
#define LFIRST 1
#define DEV __device__ __forceinline__

constexpr int SEQ = 2048, DM = 1024, FF = 3584, NMOD = 6 * DM;
constexpr size_t WS_MODP = 0;
constexpr size_t WS_UTP = 98304;
constexpr size_t WS_MODF = 212992;
constexpr size_t WS_MODH = 237568;
constexpr size_t WS_STAT = 262144;
constexpr size_t WS_KV = 786432;
constexpr size_t WS_UTF = (size_t)48 << 20;
constexpr size_t WS_H = (size_t)1 << 20;
constexpr size_t WS_O = (size_t)5 << 20;
constexpr size_t WS_X1 = (size_t)9 << 20;
constexpr size_t WS_Y = (size_t)13 << 20;
constexpr size_t WS_G = (size_t)17 << 20;
constexpr size_t WS_WQP = (size_t)36 << 20;
constexpr size_t WS_WOP = (size_t)38 << 20;
constexpr size_t WS_W2P = (size_t)40 << 20;
constexpr size_t WS_XCH = (size_t)32 << 20;

DEV unsigned pk(float a, float b) { return __builtin_bit_cast(unsigned, __builtin_convertvector((f32x2){a, b}, bf16x2)); }
DEV float rbf(float a) { return __uint_as_float(pk(a, 0.f) << 16); }
DEV float blo(unsigned u) { return __uint_as_float(u << 16); }
DEV float bhi(unsigned u) { return __uint_as_float(u & 0xffff0000u); }
DEV float silu(float z) { return z * __builtin_amdgcn_rcpf(1.f + __builtin_amdgcn_exp2f(-1.4426950408889634f * z)); }
DEV f32x16 mfma32(bf16x8 a, bf16x8 b, f32x16 c) { return __builtin_amdgcn_mfma_f32_32x32x16_bf16(a, b, c, 0, 0, 0); }
DEV f32x4 mfma16(bf16x8 a, bf16x8 b, f32x4 c) { return __builtin_amdgcn_mfma_f32_16x16x32_bf16(a, b, c, 0, 0, 0); }
DEV bf16x8 asb(u32x4 v) { return __builtin_bit_cast(bf16x8, v); }
DEV bf16x8 ldsA(lptr p) { return *(const LDS bf16x8*)p; }
DEV bf16x8 ldsT(lptr p, int second) {
  s16x4 a = __builtin_amdgcn_ds_read_tr16_b64_v4i16((LDS s16x4*)p);
  s16x4 b = __builtin_amdgcn_ds_read_tr16_b64_v4i16((LDS s16x4*)(p + second));
  return __builtin_bit_cast(bf16x8, __builtin_shufflevector(a, b, 0, 1, 2, 3, 4, 5, 6, 7));
}
DEV s16x4 ldsT1(lptr p) { return __builtin_amdgcn_ds_read_tr16_b64_v4i16((LDS s16x4*)p); }
DEV bf16x8 cat(s16x4 a, s16x4 b) { return __builtin_bit_cast(bf16x8, __builtin_shufflevector(a, b, 0, 1, 2, 3, 4, 5, 6, 7)); }
template <int C> DEV float dppadd(float v) { return v + __int_as_float(__builtin_amdgcn_update_dpp(0, __float_as_int(v), C, 0xf, 0xf, true)); }
DEV float swap32add(float v) { auto r = __builtin_amdgcn_permlane32_swap(__float_as_uint(v), __float_as_uint(v), false, false); return __uint_as_float(r[0]) + __uint_as_float(r[1]); }
DEV float swap16add(float v) { auto r = __builtin_amdgcn_permlane16_swap(__float_as_uint(v), __float_as_uint(v), false, false); return __uint_as_float(r[0]) + __uint_as_float(r[1]); }
DEV float swap32max(float v) { auto r = __builtin_amdgcn_permlane32_swap(__float_as_uint(v), __float_as_uint(v), false, false); return __builtin_fmaxf(__uint_as_float(r[0]), __uint_as_float(r[1])); }
DEV float wavesum(float v) { v = dppadd<0xB1>(v); v = dppadd<0x4E>(v); v = dppadd<0x141>(v); v = dppadd<0x140>(v); v = swap16add(v); return swap32add(v); }

#define SETM0(x) asm volatile("s_mov_b32 m0, %0\n\ts_nop 0" ::"s"(x) : "memory")
template <int OFF> DEV void glds(unsigned voff, const void* base) { asm volatile("global_load_lds_dwordx4 %0, %1 offset:%2" ::"v"(voff), "s"(base), "i"(OFF) : "memory"); }
template <int N> DEV void waitbar() { asm volatile("s_waitcnt vmcnt(%0) lgkmcnt(0)\n\ts_barrier" ::"i"(N) : "memory"); }
template <int OFF> DEV u32x4 gload(unsigned voff, const void* base) { u32x4 r; asm volatile("global_load_dwordx4 %0, %1, %2 offset:%3" : "=v"(r) : "v"(voff), "s"(base), "i"(OFF) : "memory"); return r; }
template <int OFF> DEV u32x2 gload2(unsigned voff, const void* base) { u32x2 r; asm volatile("global_load_dwordx2 %0, %1, %2 offset:%3" : "=v"(r) : "v"(voff), "s"(base), "i"(OFF) : "memory"); return r; }
template <int OFF> DEV unsigned gload1(unsigned voff, const void* base) { unsigned r; asm volatile("global_load_dword %0, %1, %2 offset:%3" : "=v"(r) : "v"(voff), "s"(base), "i"(OFF) : "memory"); return r; }
template <int OFF> DEV u32x4 gloadd(unsigned voff, const void* base) { u32x4 r; asm volatile("global_load_dwordx4 %0, %1, %2 offset:%3 sc1" : "=v"(r) : "v"(voff), "s"(base), "i"(OFF) : "memory"); return r; }
template <int OFF> DEV void gstore2(unsigned voff, void* base, u32x2 v) { asm volatile("global_store_dwordx2 %0, %1, %2 offset:%3 sc1" ::"v"(voff), "v"(v), "s"(base), "i"(OFF) : "memory"); }
template <int OFF> DEV void gstore(unsigned voff, void* base, u32x4 v) { asm volatile("global_store_dwordx4 %0, %1, %2 offset:%3 sc1\n\ts_nop 1" ::"v"(voff), "v"(v), "s"(base), "i"(OFF) : "memory"); }
#define CODE_RANGE(A, B, base, size) const char* base; unsigned size; { unsigned long long pc_; int o_; \
  asm volatile("s_getpc_b64 %0\n.Lcr%=:\n\ts_mov_b32 %1, " #A "-.Lcr%=\n\ts_mov_b32 %2, " #B "-" #A : "=s"(pc_), "=s"(o_), "=s"(size)); base = (const char*)(pc_ + o_); }
#define SB() __builtin_amdgcn_sched_barrier(0)
extern __shared__ __attribute__((aligned(16))) char smem_raw[];

struct Stamps {};
#define ST_INIT() Stamps S_
#define ST(i)
#define IT_INIT()
#define IT_A()
#define IT_B()
#define IT_END(k)
#define ST_END(k)

constexpr int K1_W = 0, K1_H = 65536, K1_MV = K1_H + 64 * 528, K1_RED = K1_MV + 8192, K1_LDS = K1_RED + 32768;
extern "C" __global__ __launch_bounds__(512, 2) void k_ln1kv(const char* __restrict__ x, const char* __restrict__ wk, const char* __restrict__ wv,
                                                  const char* __restrict__ modp, float* __restrict__ modf, uint16_t* __restrict__ modh,
                                                  char* __restrict__ hbuf, char* __restrict__ kv) {
  const lptr sm = (lptr)smem_raw;
  ST_INIT();
  const int b = blockIdx.x, t = threadIdx.x, l = t & 63, w = __builtin_amdgcn_readfirstlane(t >> 6);
  const int which = (b >> 3) & 1, rb = (b & 7) * 16 + (b >> 4);
  u32x4 xv[4], mp[4];
  { const char* xb = x + (size_t)(rb * 16 + 2 * w) * 2048; const unsigned vo = l * 32;
    xv[0] = gload<0>(vo, xb); xv[1] = gload<16>(vo, xb); xv[2] = gload<2048>(vo, xb); xv[3] = gload<2064>(vo, xb);
#pragma unroll
    for (int s = 0; s < 4; ++s) mp[s] = gload<0>(t * 16, modp + s * NMOD * 4); }
  { const char* wsrc = (which ? wv : wk) + w * 8192;
#pragma unroll
    for (int g = 0; g < 2; ++g) { SETM0(K1_W + w * 8192 + g * 4096); const char* s = wsrc + g * 4096;
      glds<0>(l * 16, s); glds<1024>(l * 16, s); glds<2048>(l * 16, s); glds<3072>(l * 16, s); } }
  asm volatile("s_waitcnt vmcnt(8)" : "+v"(xv[0]), "+v"(xv[1]), "+v"(xv[2]), "+v"(xv[3]), "+v"(mp[0]), "+v"(mp[1]), "+v"(mp[2]), "+v"(mp[3])::"memory");
  ST(1);
  { f32x4 o;
#pragma unroll
    for (int e = 0; e < 4; ++e) { const float v = rbf((__uint_as_float(mp[0][e]) + __uint_as_float(mp[1][e])) + (__uint_as_float(mp[2][e]) + __uint_as_float(mp[3][e])));
      o[e] = t >= 256 ? rbf(1.f + v) : v; }
    *(LDS f32x4*)(sm + K1_MV + (t & 256) * 16 + (t & 3) * 1024 + ((t & 255) >> 2) * 16) = o; }
  f32x2 v[2][8]; float rs[2];
#pragma unroll
  for (int r = 0; r < 2; ++r) { f32x2 s = {0.f, 0.f};
#pragma unroll
    for (int e = 0; e < 8; ++e) { const unsigned u = xv[2 * r + (e >> 2)][e & 3]; v[r][e] = (f32x2){blo(u), bhi(u)}; s += v[r][e]; }
    const float m = wavesum(s[0] + s[1]) * (1.f / 1024.f); f32x2 q = {0.f, 0.f};
#pragma unroll
    for (int e = 0; e < 8; ++e) { v[r][e] -= m; q += v[r][e] * v[r][e]; }
    rs[r] = __builtin_amdgcn_rsqf(wavesum(q[0] + q[1]) * (1.f / 1024.f) + 1e-5f); }
  asm volatile("s_waitcnt lgkmcnt(0)\n\ts_barrier" ::: "memory");
  { unsigned u[2][8];
#pragma unroll
    for (int e = 0; e < 4; ++e) { const f32x4 a = *(const LDS f32x4*)(sm + K1_MV + e * 1024 + l * 16), cc = *(const LDS f32x4*)(sm + K1_MV + 4096 + e * 1024 + l * 16);
#pragma unroll
      for (int r = 0; r < 2; ++r)
#pragma unroll
        for (int i = 0; i < 2; ++i) { const f32x2 h = v[r][2 * e + i] * rs[r] * (f32x2){cc[2 * i], cc[2 * i + 1]} + (f32x2){a[2 * i], a[2 * i + 1]}; u[r][2 * e + i] = pk(h[0], h[1]); } }
#pragma unroll
    for (int r = 0; r < 2; ++r) { const lptr d = sm + K1_H + l * 528 + (2 * w + r) * 16;
      *(LDS u32x4*)d = (u32x4){u[r][0], u[r][1], u[r][4], u[r][5]}; *(LDS u32x4*)(d + 256) = (u32x4){u[r][2], u[r][3], u[r][6], u[r][7]}; } }
  ST(2); waitbar<0>(); ST(3);
  f32x16 acc = {};
  { const int hi = l >> 5; const lptr hp = sm + K1_H + w * 8 * 528 + hi * 256 + (l & 15) * 16;
    const lptr wp = sm + K1_W + (w * 128 + 4 * hi + ((l >> 2) & 3)) * 64 + ((l >> 4) & 1) * 32 + (l & 3) * 8;
    bf16x8 fa[8], fb[8];
#pragma unroll
    for (int i = 0; i < 8; ++i) { fa[i] = ldsA(hp + i * 528); fb[i] = ldsT(wp + i * 1024, 512); }
#pragma unroll
    for (int i = 0; i < 2; ++i) { const int id = t + 512 * i, tok = id & 15, h2 = (id >> 4) & 1, kb = which * 32 + (id >> 5);
      gstore<0>(kb * 1024 + (h2 * 32 + tok) * 16, hbuf + (size_t)(rb >> 1) * 65536 + (rb & 1) * 256, *(const LDS u32x4*)(sm + K1_H + kb * 528 + h2 * 256 + tok * 16)); }
    if (which == 0) {
#pragma unroll
      for (int i = 0; i < 8; ++i) acc = mfma32(fb[i], fa[i], acc);
    } else {
#pragma unroll
      for (int i = 0; i < 8; ++i) acc = mfma32(fa[i], fb[i], acc);
    } }
#pragma unroll
  for (int q = 0; q < 4; ++q) *(LDS f32x4*)(sm + K1_RED + w * 4096 + q * 1024 + l * 16) = (f32x4){acc[4 * q], acc[4 * q + 1], acc[4 * q + 2], acc[4 * q + 3]};
  asm volatile("s_waitcnt lgkmcnt(0)\n\ts_barrier" ::: "memory");
  if (w == 0) {
    const lptr rp = sm + K1_RED + (which ? l * 16 : (l >> 5) * 2048 + (((l >> 4) & 1) * 32 + (l & 15)) * 16);
    f32x4 a = {}, c = {};
#pragma unroll
    for (int u = 0; u < 8; ++u) { a += *(const LDS f32x4*)(rp + u * 4096); c += *(const LDS f32x4*)(rp + u * 4096 + 1024); }
    char* base = kv + (rb >> 1) * 4096;
    char* d = which ? base + 2048 + (rb & 1) * 1024 + l * 16 : base + (l >> 5) * 1024 + (((l >> 4) & 1) * 32 + (rb & 1) * 16 + (l & 15)) * 16;
    *(u32x4*)d = (u32x4){pk(a[0], a[1]), pk(a[2], a[3]), pk(c[0], c[1]), pk(c[2], c[3])};
  } else if (b < 64 && w == 1) {
    const int col = 2048 + b * 64 + l; const float* mpf = (const float*)modp;
    float v2 = rbf((mpf[col] + mpf[NMOD + col]) + (mpf[2 * NMOD + col] + mpf[3 * NMOD + col]));
    if ((col >> 10) == 4) v2 = rbf(1.f + v2);
    modf[col] = v2; modh[col] = (uint16_t)(__float_as_uint(v2) >> 16);
  }
  ST_END(1);
}

DEV void mfma_done() { SB(); asm volatile("s_nop 7\n\ts_nop 7\n\ts_nop 7"); SB(); }
DEV void ldswait() { __builtin_amdgcn_s_waitcnt(0xC07F); }
template <int N> DEV void ldsw() { __builtin_amdgcn_s_waitcnt(0xC07F | (N << 8)); }
DEV void sbar() { asm volatile("s_barrier" ::: "memory"); }
DEV void bar() { asm volatile("" ::: "memory"); ldswait(); asm volatile("s_barrier" ::: "memory"); }
template <int N, int G, int K, int LN, int LP, class L> DEV void loader_tail(L&& late) {
  waitbar<G * N + LP * (K < LN ? K : LN)>(); if constexpr (K < LN) late(std::integral_constant<int, K>{});
  if constexpr (N > 0) loader_tail<N - 1, G, K + 1, LN, LP>(late); }
constexpr int younger(int t, int D, int EN) { const int a = t - D + 2 < 1 ? 1 : t - D + 2, b = t < EN ? t : EN; return b >= a ? b - a + 1 : 0; }
template <int T, int T0, int S, int D, int G, int STAGE, int EN, int EP, class I, class E> DEV void loader_ramp(I&& issue, E&& early) {
#pragma unroll
  for (int s = T == 0 ? 0 : D + T - 2; s < D + T - 1; ++s) issue(s, (s % S) * STAGE);
  if constexpr (T >= 1 && T <= EN) early(std::integral_constant<int, T - 1>{});
  waitbar<G*(D - 2) + EP * younger(T, D, EN)>();
  if constexpr (T < T0) loader_ramp<T + 1, T0, S, D, G, STAGE, EN, EP>(issue, early);
}
template <int KT, int S, int D, int G, int STAGE, int EN, int EP, int LN, int LP, class I, class E, class L>
DEV void loader(I&& issue, E&& early, L&& late) {
  static_assert(D <= S && EN + D - 2 <= KT - D, "");
  loader_ramp<0, KT - D, S, D, G, STAGE, EN, EP>(issue, early);
  issue(KT - 1, ((KT - 1) % S) * STAGE);
  loader_tail<D - 2, G, 0, LN, LP>(late);
}
constexpr int GA_STAGE = 24576;
DEV unsigned imgA(int l) { return (l >> 3) * (DM * 2) + (((l & 7) ^ (((l >> 4) & 1) << 2)) * 16); }
template <int KT, int S, int D, int EN, int EP, int LN, int LP, class E, class L>
DEV void gemmA_load(const char* act, const char* wgt, int l, int lw, E&& early, L&& late) {
  const char* ap = act + (size_t)lw * (KT * 4096);
  const char* wp = wgt + lw * 2048;
  const unsigned va = l * 16;
  loader<KT, S, D, 6, GA_STAGE, EN, EP, LN, LP>([&](int t, int slot) {
    SETM0(slot + lw * 4096); const char* a = ap + (size_t)t * 4096; glds<0>(va, a); glds<1024>(va, a); glds<2048>(va, a); glds<3072>(va, a);
    SETM0(slot + 16384 + lw * 2048); const char* s = wp + t * 8192; glds<0>(va, s); glds<1024>(va, s); }, early, late);
}
struct FragA { bf16x8 a0[4], a1[4], b[4]; };
template <int KT, int S>
DEV void gemmA_compute(const lptr sm, int l, int w, f32x16& c0, f32x16& c1, Stamps& S_) {
  const int q = (l >> 2) & 3, p = l & 3;
  const lptr ra = sm + (w & 1) * 8192 + l * 16;
  const lptr rb = sm + 16384 + (4 * (l >> 5) + q) * 128 + ((((w >> 1) * 4 + ((l >> 4) & 1) * 2 + (p >> 1)) ^ ((q >> 1) << 2)) * 16) + (p & 1) * 8;
  c0 = (f32x16){}; c1 = (f32x16){};
  FragA f, g; int slot = 0; IT_INIT();
  auto rd = [&](FragA& d, int k) { d.a0[k] = ldsA(ra + slot + k * 1024); d.b[k] = ldsT(rb + slot + k * 2048, 1024); d.a1[k] = ldsA(ra + slot + 4096 + k * 1024); };
  auto step = [&](FragA& cur, FragA& nxt) { IT_A(); IT_B(); bar(); slot = slot == (S - 1) * GA_STAGE ? 0 : slot + GA_STAGE;
#pragma unroll
    for (int k = 0; k < 4; ++k) { if (k < 2) rd(nxt, 2 * k); c0 = mfma32(cur.b[k], cur.a0[k], c0); SB(); if (k < 2) rd(nxt, 2 * k + 1); c1 = mfma32(cur.b[k], cur.a1[k], c1); SB(); } };
  bar(); ST(2);
#pragma unroll
  for (int k = 0; k < 4; ++k) rd(f, k);
  for (int t = 0; t < KT - 2; t += 2) { step(f, g); step(g, f); }
  step(f, g);
#pragma unroll
  for (int k = 0; k < 4; ++k) { c0 = mfma32(g.b[k], g.a0[k], c0); c1 = mfma32(g.b[k], g.a1[k], c1); }
  mfma_done();
  ST(3); IT_END(S == 4 ? 2 : 3);
}

constexpr int K2_KV0 = 98304, K2_KV1 = 131072, K2_KV2 = 0, K2_UW = 32768, K2_Q = 65536, K2_RED = 73728, K2_DUMP = 81920, K2_LDS = 163840;
DEV void pack16(const f32x16& s, u32x4& a, u32x4& b) {
  a = (u32x4){pk(s[0], s[1]), pk(s[2], s[3]), pk(s[4], s[5]), pk(s[6], s[7])};
  b = (u32x4){pk(s[8], s[9]), pk(s[10], s[11]), pk(s[12], s[13]), pk(s[14], s[15])}; }
DEV float max16(const f32x16& s) { float m = s[0];
#pragma unroll
  for (int i = 1; i < 16; ++i) m = __builtin_fmaxf(m, s[i]);
  return swap32max(m); }
#define EX2(v) __builtin_amdgcn_exp2f(v)
#define PIN(x) asm volatile("" : "+v"(x))

extern "C" __global__ __launch_bounds__(512, 2) void k_qattn(const char* __restrict__ hbuf, const char* __restrict__ wq, const char* __restrict__ kv, char* __restrict__ obuf,
                                                  const char* __restrict__ w1, const char* __restrict__ modh, float* __restrict__ utp,
                                                  const char* __restrict__ wo, const char* __restrict__ w2, char* __restrict__ xch, char* __restrict__ wop, char* __restrict__ w2p) {
  const lptr sm = (lptr)smem_raw;
  ST_INIT();
  const int b = blockIdx.x, l = threadIdx.x & 63, w = __builtin_amdgcn_readfirstlane(threadIdx.x >> 6);
  const int xcd = b & 7, j = b >> 3, tb = (xcd >> 1) * 4 + (j & 3), hp = (xcd & 1) * 8 + (j >> 2);
  const unsigned l16 = l * 16;
  if (w >= 4) { const int lw = w - 4;
    auto kvload = [&](int st, int slot) {
#pragma unroll
      for (int g = 0; g < 2; ++g) { SETM0(slot + lw * 8192 + g * 4096); const char* s = kv + st * 32768 + lw * 8192 + g * 4096;
        glds<0>(l16, s); glds<1024>(l16, s); glds<2048>(l16, s); glds<3072>(l16, s); } };
    gemmA_load<16, 4, D2, 8, 1, 2, 4>(hbuf + (size_t)tb * 4 * 64 * 1024, wq + hp * 131072, l, lw,
      [&](auto i) { SETM0(K2_KV0 + lw * 8192 + i * 1024); glds<0>(l16, kv + lw * 8192 + i * 1024); },
      [&](auto g) { SETM0(K2_KV1 + lw * 8192 + g * 4096); const char* s = kv + 32768 + lw * 8192 + g * 4096; glds<0>(l16, s); glds<1024>(l16, s); glds<2048>(l16, s); glds<3072>(l16, s); });
    waitbar<8>();
    const int ub = b < 224 ? b : b - 224, cg = ub % 56, ks = ub / 56;
    u32x2 vf[8];
    waitbar<0>(); kvload(2, K2_KV2);
    { const unsigned vo = (l >> 3) * (FF * 2) + (((l & 7) ^ (((l >> 4) & 1) << 2)) * 16);
#pragma unroll
      for (int g = 0; g < 2; ++g) { SETM0(K2_UW + lw * 8192 + g * 4096); const char* s = w1 + (size_t)(ks * 256 + lw * 64 + g * 32) * (FF * 2) + cg * 128;
        glds<0>(vo, s); glds<1024>(vo, s + 8 * FF * 2 - 1024); glds<2048>(vo, s + 16 * FF * 2 - 2048); glds<3072>(vo, s + 24 * FF * 2 - 3072); }
      const char* mv = modh + (ks * 256 + lw * 64) * 2; const unsigned vm = ((4 - (l & 1)) * DM + 4 * (l >> 5)) * 2;
      vf[0] = gload2<0>(vm, mv); vf[1] = gload2<16>(vm, mv); vf[2] = gload2<32>(vm, mv); vf[3] = gload2<48>(vm, mv);
      vf[4] = gload2<64>(vm, mv); vf[5] = gload2<80>(vm, mv); vf[6] = gload2<96>(vm, mv); vf[7] = gload2<112>(vm, mv); }
    waitbar<16>(); kvload(3, K2_KV0);
    u32x4 rp[9];
    { const char* s = wo + (size_t)((b & 15) * 64 + lw * 16) * 2048 + (b >> 4) * 128; rp[7] = gload<0>(imgA(l), s); rp[8] = gload<0>(imgA(l), s + 16384);
      const int r = ((b & 31) * 4 + lw) * 7; const unsigned v0 = (l >> 4) * 2048 + ((l & 15) ^ ((l >> 4) * 2)) * 16;
#pragma unroll
      for (int i = 0; i < 7; ++i) rp[i] = gload<0>(v0 ^ (((r + i) & 1) * 128), w2 + (size_t)(r + i) * 8192 + (b >> 5) * 256); }
    waitbar<9>(); kvload(4, K2_KV1);
    { const int q = (l >> 2) & 3, p = l & 3; f32x16 d0 = {}, d1 = {};
      const lptr rb = sm + K2_UW + (lw * 64 + 4 * (l >> 5) + q) * 128 + (((((l >> 4) & 1) * 2 + (p >> 1)) ^ ((q >> 1) << 2)) * 16) + (p & 1) * 8;
      const lptr rc = sm + K2_UW + (lw * 64 + 4 * (l >> 5) + q) * 128 + (((4 + ((l >> 4) & 1) * 2 + (p >> 1)) ^ ((q >> 1) << 2)) * 16) + (p & 1) * 8;
#pragma unroll
      for (int kk = 0; kk < 4; ++kk) { PIN(vf[2 * kk]); PIN(vf[2 * kk + 1]); const bf16x8 vv = asb((u32x4){vf[2 * kk][0], vf[2 * kk][1], vf[2 * kk + 1][0], vf[2 * kk + 1][1]});
        d0 = mfma32(ldsT(rb + kk * 2048, 1024), vv, d0); d1 = mfma32(ldsT(rc + kk * 2048, 1024), vv, d1); }
      if ((l & 31) < 2) { const lptr d = sm + K2_RED + lw * 512 + (l & 31) * 256 + (l >> 5) * 16;
#pragma unroll
        for (int i = 0; i < 4; ++i) { *(LDS f32x4*)(d + i * 32) = (f32x4){d0[4 * i], d0[4 * i + 1], d0[4 * i + 2], d0[4 * i + 3]};
          *(LDS f32x4*)(d + 128 + i * 32) = (f32x4){d1[4 * i], d1[4 * i + 1], d1[4 * i + 2], d1[4 * i + 3]}; } } }
    waitbar<0>(); kvload(5, K2_KV2);
    if (b < 224 && lw < 2) { float v = 0.f;
#pragma unroll
      for (int u = 0; u < 4; ++u) v += *(const LDS float*)(sm + K2_RED + u * 512 + lw * 256 + l * 4);
      utp[(ks * 2 + lw) * FF + cg * 64 + l] = v; }
    waitbar<0>(); kvload(6, K2_KV0);
    { char* d = xch + b * 16384 + lw * 4096; const u32x4 e = {~0u, ~0u, ~0u, ~0u}; gstore<0>(l16, d, e); gstore<1024>(l16, d, e); gstore<2048>(l16, d, e); gstore<3072>(l16, d, e);
#pragma unroll
      for (int i = 0; i < 9; ++i) PIN(rp[i]);
      char* o = wop + b * 8192 + lw * 2048; gstore<0>(l16, o, rp[7]); gstore<1024>(l16, o, rp[8]);
#pragma unroll
      for (int i = 0; i < 7; ++i) gstore<0>(l16, w2p + (size_t)(b * 4 + lw) * 7168 + i * 1024, rp[i]); }
    waitbar<13>(); kvload(7, K2_KV1);
    { CODE_RANGE(k_oproj, k_mod, cb, cs) const unsigned o = (j & 15) * 4096 + lw * 1024 + l16; SETM0(K2_DUMP + lw * 1024); glds<0>(o < cs ? o : 0, cb); }
    waitbar<1>();
    asm volatile("s_waitcnt vmcnt(0)" ::: "memory");
    return; }
  bf16x8 qA0, qA1, qB0, qB1;
  { f32x16 c0, c1; gemmA_compute<16, 4>(sm, l, w, c0, c1, S_);
    constexpr float QS = 0.17677669529663687f * 1.4426950408889634f;
    c0 *= QS; c1 *= QS;
    u32x4 a, c; pack16(c0, a, c); qA0 = asb(a); qA1 = asb(c); pack16(c1, a, c); qB0 = asb(a); qB1 = asb(c); }
  bar();
  const unsigned onea = (l & 6) == 0 ? 0x3f803f80u : 0u, oneb = (l & 6) == 4 ? 0x3f803f80u : 0u;
  const bf16x8 ones = asb((u32x4){onea, onea, oneb, oneb}); const int sel = (l & 1) ? 0xEEEEEEEE : 0x44444444;
  auto rowsum = [&](u32x8 p, f32x4 a) { return __builtin_amdgcn_smfmac_f32_16x16x64_bf16(ones, __builtin_bit_cast(bf16x16, p), a, sel, 0, 0); };
  auto lo = [](u32x8 p) { return asb(__builtin_shufflevector(p, p, 0, 1, 2, 3)); };
  auto hi = [](u32x8 p) { return asb(__builtin_shufflevector(p, p, 4, 5, 6, 7)); };
  const lptr kp0 = sm + K2_KV0 + l16, kp1 = sm + K2_KV1 + l16, kp2 = sm + K2_KV2 + l16;
  bf16x8 k0a = ldsA(kp0), k1a = ldsA(kp0 + 1024), v0a = ldsA(kp0 + 2048), v1a = ldsA(kp0 + 3072), k0b = ldsA(kp0 + 4096), k1b = ldsA(kp0 + 5120), v0b, v1b = k0a;
  f32x16 nA, nB, sA, sB, oA = {}, oB = {}; f32x4 lA = {}, lB = {};
  { f32x16 z = {}, y = {}; z = mfma32(k0a, qA0, z); y = mfma32(k0a, qB0, y); z = mfma32(k1a, qA1, z); y = mfma32(k1a, qB1, y); const float m = -max16(z), n = -max16(y);
#pragma unroll
    for (int i = 0; i < 16; ++i) { nA[i] = m; nB[i] = n; } }
  PIN(nA); PIN(nB);
  sA = mfma32(k0a, qA0, nA); sA = mfma32(k1a, qA1, sA);
  u32x8 pA = {}, pB = {};
#define HALF(S, N, Q0, Q1, NEG, OS, ON, LN, PS, PN, K0, V1, K1, V0, W, R0, R1)                                                   \
  { S[0] = EX2(S[0]); S[1] = EX2(S[1]); PIN(S); SB();                                                                 \
    if (WPOS == 3) { W; } SB(); N = mfma32(K0, Q0, NEG); SB(); R0; SB();                                                                          \
    S[2] = EX2(S[2]); S[3] = EX2(S[3]); PS[0] = pk(S[0], S[1]); PS[1] = pk(S[2], S[3]); PIN(PS); SB();                \
    ON = mfma32(V1, hi(PN), ON); SB(); R1; SB();                                                                      \
    S[4] = EX2(S[4]); S[5] = EX2(S[5]); S[6] = EX2(S[6]); S[7] = EX2(S[7]); PS[2] = pk(S[4], S[5]); PS[3] = pk(S[6], S[7]); PIN(PS); SB(); \
    if (WPOS == 4) { W; } SB(); LN = rowsum(PN, LN); SB();                                                                                        \
    S[8] = EX2(S[8]); PIN(S); SB();                                                                                   \
    if (WPOS == 2) { W; } SB(); N = mfma32(K1, Q1, N); SB(); if (WPOS == 0) { W; } SB();                                                                             \
    S[9] = EX2(S[9]); S[10] = EX2(S[10]); S[11] = EX2(S[11]); PS[4] = pk(S[8], S[9]); PS[5] = pk(S[10], S[11]); PIN(PS); SB(); \
    S[12] = EX2(S[12]); S[13] = EX2(S[13]); PIN(S); SB();                                                             \
    if (WPOS == 1) { W; } SB(); OS = mfma32(V0, lo(PS), OS); SB();                                                                                \
    S[14] = EX2(S[14]); S[15] = EX2(S[15]); PS[6] = pk(S[12], S[13]); PS[7] = pk(S[14], S[15]); PIN(PS); SB(); }
#define TILE(X, Y, C, N)                                                                                               \
  HALF(sA, sB, qB0, qB1, nB, oA, oB, lB, pA, pB, k0##C, v1##N, k1##C, v0##C, __builtin_amdgcn_s_waitcnt(0xC07F), , )   \
  HALF(sB, sA, qA0, qA1, nA, oB, oA, lA, pB, pA, k0##N, v1##C, k1##N, v0##C, , (v0##N = ldsA((X) + 2048), v1##N = ldsA((X) + 3072)), (k0##C = ldsA(Y), k1##C = ldsA((Y) + 1024)))
#define TILE2(T, X) TILE(T + 4096, X, a, b) TILE(X, X + 4096, b, a)
#define STAGE(P, Q, BAR) TILE2(P, P + 2 * 4096) TILE2(P + 2 * 4096, P + 4 * 4096) TILE2(P + 4 * 4096, P + 6 * 4096) BAR TILE2(P + 6 * 4096, Q)
  ST(4);
  { lptr c = kp0, n = kp1, f = kp2;
    for (int st = 0; st < 7; ++st) { STAGE(c, n, bar();) const lptr t = c; c = n; n = f; f = t; }
    STAGE(c, n, ) }
  ST(5);
  oB = mfma32(v1b, hi(pB), oB); lB = rowsum(pB, lB);
  mfma_done();
  auto finish = [&](f32x16& o, float lsum, bf16x8 q0, bf16x8 q1, int i) {
    float chk = lsum;
#pragma unroll
    for (int e = 0; e < 16; ++e) chk = __builtin_fmaxf(chk, __builtin_fabsf(o[e]) * 0x1p-40f);
    bool bad = !(chk < 0x1p80f);
#pragma unroll
    for (int e = 0; e < 16; ++e) bad |= (o[e] != o[e]);
    bad |= (lsum != lsum);
    if (__any(bad)) {
      float m = -INFINITY; lsum = 0.f; o = (f32x16){};
      for (int t = 0; t < 64; ++t) { const bf16x8* g = (const bf16x8*)(kv + t * 4096) + l;
        f32x16 s = {}; s = mfma32(g[0], q0, s); s = mfma32(g[64], q1, s); mfma_done();
        const float mn = __builtin_fmaxf(m, max16(s)), al = EX2(m - mn); m = mn; float rsum = 0.f;
#pragma unroll
        for (int e = 0; e < 16; ++e) { s[e] = rbf(EX2(s[e] - mn)); rsum += s[e]; o[e] *= al; }
        lsum = lsum * al + swap32add(rsum);
        u32x4 a, c; pack16(s, a, c); o = mfma32(g[128], asb(a), o); o = mfma32(g[192], asb(c), o); mfma_done(); } }
    o *= 1.f / lsum;
    u32x4 o0, o1; pack16(o, o0, o1);
    char* d = obuf + (size_t)((tb * 4 + 2 * (w & 1) + i) * 64 + (hp * 2 + (w >> 1)) * 2) * 1024;
    gstore<0>(l16, d, o0); gstore<1024>(l16, d, o1); };
  finish(oA, lA[0] + lA[1], qA0, qA1, 0); finish(oB, lB[0] + lB[1], qB0, qB1, 1);
  ST_END(2);
}

constexpr int K3_S = 6, K3_X = K3_S * GA_STAGE, K3_MV = 4 * GA_STAGE, K3_LDS = K3_X + 16384;
extern "C" __global__ __launch_bounds__(512, 2) void k_oproj(const char* __restrict__ obuf, const char* __restrict__ wo, const char* __restrict__ x, const float* __restrict__ modf,
                                                  char* __restrict__ x1b, char* __restrict__ yb, f32x2* __restrict__ stat,
                                                  const float* __restrict__ utp, float* __restrict__ utf) {
  const lptr sm = (lptr)smem_raw;
  ST_INIT();
  const int b = blockIdx.x, l = threadIdx.x & 63, w = __builtin_amdgcn_readfirstlane(threadIdx.x >> 6);
  const int xcd = b & 7, j = b >> 3, tb = (xcd >> 1) * 4 + (j & 3), nt = (xcd & 1) * 8 + (j >> 2);
  if (__builtin_expect(w >= 4, LFIRST)) { u32x4 e; const int lw = w - 4;
    gemmA_load<16, K3_S, D3, 0, 0, 2, 3>(obuf + (size_t)tb * 4 * 64 * 1024, wo + nt * 131072, l, lw, [](auto) {}, [&](auto g) {
      const char* s = x + (size_t)(tb * 128 + lw * 32 + g * 16) * 2048 + nt * 128; const unsigned vx = (l >> 3) * 2048 + ((l & 7) ^ (l >> 3)) * 16;
      SETM0(K3_X + lw * 4096 + g * 2048); glds<0>(vx, s); glds<1024>(vx, s + 16384 - 1024);
      if constexpr (g == 0) { SETM0(K3_MV + lw * 1024); glds<0>((l & 16) * 512 + (l & 15) * 16, (const char*)(modf + 2 * DM + nt * 64)); }
      else { CODE_RANGE(k_ffn1, k_ffn2, cb, cs) const unsigned o = (j & 7) * 4096 + lw * 1024 + l * 16; e = gload<0>(o < cs ? o : 0, cb); } });
    waitbar<1>();
    if (b < 28) { const int i = b * 256 + lw * 64 + l; const float v = (utp[i] + utp[2 * FF + i]) + (utp[4 * FF + i] + utp[6 * FF + i]);
      utf[i] = i < FF ? v : -1.4426950408889634f * v; }
    asm volatile("s_waitcnt vmcnt(0)" : "+v"(e)::"memory"); return; }
  const int hi = l >> 5, tb32 = tb * 4 + 2 * (w & 1), n0 = nt * 64 + (w >> 1) * 32;
  f32x16 acc[2]; gemmA_compute<16, K3_S>(sm, l, w, acc[0], acc[1], S_);
  bar();
  f32x4 ga[4], cb[4];
#pragma unroll
  for (int q = 0; q < 4; ++q) { const lptr p = sm + K3_MV + w * 1024 + (w >> 1) * 128 + q * 32 + hi * 16; ga[q] = *(const LDS f32x4*)p; cb[q] = *(const LDS f32x4*)(p + 256); }
  u32x2 xr[2][4];
#pragma unroll
  for (int i = 0; i < 2; ++i)
#pragma unroll
    for (int q = 0; q < 4; ++q) xr[i][q] = *(const LDS u32x2*)(sm + K3_X + ((w & 1) * 64 + i * 32 + (l & 31)) * 128 + (((w >> 1) * 4 + q) ^ (l & 7)) * 16 + hi * 8);
#pragma unroll
  for (int i = 0; i < 2; ++i) { unsigned xo[8], yo[8]; float x1[16], s = 0.f;
#pragma unroll
    for (int h = 0; h < 2; ++h) { const u32x4 xs = {xr[i][2 * h][0], xr[i][2 * h][1], xr[i][2 * h + 1][0], xr[i][2 * h + 1][1]};
#pragma unroll
      for (int e = 0; e < 4; ++e) { const int r = 8 * h + 2 * e, q = r >> 2, c = r & 3;
        const unsigned u = pk(blo(xs[e]) + ga[q][c] * acc[i][r], bhi(xs[e]) + ga[q][c + 1] * acc[i][r + 1]);
        x1[r] = blo(u); x1[r + 1] = bhi(u); xo[4 * h + e] = u; yo[4 * h + e] = pk(x1[r] * cb[q][c], x1[r + 1] * cb[q][c + 1]); s += x1[r] + x1[r + 1]; } }
    const size_t off = (size_t)((tb32 + i) * 64 + (n0 >> 4)) * 1024;
    gstore<0>(l * 16, yb + off, (u32x4){yo[0], yo[1], yo[2], yo[3]}); gstore<1024>(l * 16, yb + off, (u32x4){yo[4], yo[5], yo[6], yo[7]});
    gstore<0>(l * 16, x1b + off, (u32x4){xo[0], xo[1], xo[2], xo[3]}); gstore<1024>(l * 16, x1b + off, (u32x4){xo[4], xo[5], xo[6], xo[7]});
    const float mean = swap32add(s) * (1.f / 32.f); float m2 = 0.f;
#pragma unroll
    for (int e = 0; e < 16; ++e) m2 += (x1[e] - mean) * (x1[e] - mean);
    m2 = swap32add(m2);
    if (hi == 0) stat[((tb32 + i) * 32 + (n0 >> 5)) * 32 + l] = (f32x2){mean, m2}; }
  ST_END(3);
}

constexpr int K4_STAGE = 45056, K4_UT = 3 * K4_STAGE, K4_RS = K4_UT + 2048, K4_LDS = K4_RS + 1024;
struct FragB { bf16x8 a[4]; s16x4 b[14]; };
extern "C" __global__ __launch_bounds__(512, 2) void k_ffn1(const char* __restrict__ yb, const char* __restrict__ w1, const char* __restrict__ stat,
                                                            const char* __restrict__ utp, char* __restrict__ gb) {
  const lptr sm = (lptr)smem_raw;
  ST_INIT();
  const int b = blockIdx.x, t = threadIdx.x, l = t & 63, w = __builtin_amdgcn_readfirstlane(t >> 6);
  const int xcd = b & 7, j = b >> 3, tb = (xcd >> 2) * 8 + (j & 7), nt = (xcd & 3) * 4 + (j >> 3);
  constexpr float NL2E = -1.4426950408889634f;
  if (__builtin_expect(w >= 4, LFIRST)) { const int lw = w - 4;
    const char* ap = yb + (size_t)(tb * 4 + lw) * 64 * 1024; const char* wp = w1 + nt * 448;
    unsigned vb[7];
#pragma unroll
    for (int i = 0; i < 7; ++i) { const int c = (lw * 7 + i) * 64 + l, r = c / 28; vb[i] = r * (FF * 2) + ((c - r * 28) ^ ((r >> 1) & 2)) * 16 + 3072 - i * 1024; }
    const unsigned va = l * 16;
    u32x4 st[8], e;
    loader<16, 3, 3, 11, K4_STAGE, 8, 1, 1, 2>([&](int tt, int slot) {
      SETM0(slot + lw * 4096); const char* a = ap + (size_t)tt * 4096;
      glds<0>(va, a); glds<1024>(va, a); glds<2048>(va, a); glds<3072>(va, a);
      SETM0(slot + 16384 + lw * 7168 + 3072); const char* s = wp + (size_t)tt * (64 * FF * 2);
      glds<-3072>(vb[0], s); glds<-2048>(vb[1], s); glds<-1024>(vb[2], s); glds<0>(vb[3], s); glds<1024>(vb[4], s); glds<2048>(vb[5], s); glds<3072>(vb[6], s); },
      [&](auto i) { st[i] = gload<0>(va, stat + (size_t)(tb * 4 + lw) * 8192 + i * 1024); },
      [&](auto) { SETM0(K4_UT + (lw & 1) * 1024); glds<0>(l < 56 ? va : 0, utp + ((lw & 1) * FF + nt * 224) * 4);
        CODE_RANGE(k_ffn2, k_mod, cb, cs) const unsigned o = (j & 3) * 4096 + lw * 1024 + l * 16; e = gload<0>(o < cs ? o : 0, cb); });
#pragma unroll
    for (int i = 0; i < 8; ++i) asm volatile("" : "+v"(st[i]));
    f32x4 o;
#pragma unroll
    for (int k = 0; k < 2; ++k) { float mean = 0.f, m2 = 0.f;
#pragma unroll
      for (int i = 0; i < 8; ++i) mean += __uint_as_float(st[i][2 * k]);
      mean = swap32add(swap16add(mean)) * (1.f / 32.f);
#pragma unroll
      for (int i = 0; i < 8; ++i) { const float d = __uint_as_float(st[i][2 * k]) - mean; m2 += __uint_as_float(st[i][2 * k + 1]) + 32.f * d * d; }
      const float rs = NL2E * __builtin_amdgcn_rsqf(swap32add(swap16add(m2)) * (1.f / 1024.f) + 1e-5f); o[2 * k] = rs; o[2 * k + 1] = -rs * mean; }
    if (l < 16) *(LDS f32x4*)(sm + K4_RS + (lw * 32 + 2 * l) * 8) = o;
    waitbar<1>();
    asm volatile("s_waitcnt vmcnt(0)" : "+v"(e)::"memory");
    return; }
  const int g = l >> 4, th = w & 1, nh = w >> 1, sw = (g & 1) * 32;
  const lptr ra = sm + th * 8192 + (g >> 1) * 1024 + (g & 1) * 512 + (l & 15) * 16;
  const lptr rb = sm + 16384 + (16 * (g >> 1) + 4 * (g & 1) + ((l >> 2) & 3)) * 448 + (l & 3) * 8;
  lptr base[4] = {ra, rb + nh * 256 + sw, rb + nh * 256 - sw, rb + (((6 + nh) * 32) ^ sw)};
  PIN(base[0]); PIN(base[1]); PIN(base[2]); PIN(base[3]);
  lptr p[4] = {base[0], base[1], base[2], base[3]};
  f32x4 acc[7][4];
  FragB f, h; int slot = 0; IT_INIT();
  auto rd = [&](FragB& d, int i, int ks) {
    if (i < 4) d.a[i] = ldsA(p[0] + (i >> 1) * 4096 + (i & 1) * 256 + ks * 2048);
    else { const int n = (i - 4) >> 1; d.b[i - 4] = ldsT1((n == 6 ? p[3] : p[1 + (n & 1)] + n * 32) + ks * (32 * 448) + (i & 1) * (8 * 448)); } };
  auto kstep = [&](FragB& cur, FragB& nxt, int ks, auto first) {
#pragma unroll
    for (int n = 0; n < 7; ++n)
#pragma unroll
      for (int i = 0; i < 4; ++i) { const int m = n * 4 + i;
        acc[n][i] = mfma16(cat(cur.b[2 * n], cur.b[2 * n + 1]), cur.a[i], first ? (f32x4){} : acc[n][i]); SB();
        if (m < 18) rd(nxt, m, ks); else if (ks == 1 && m < 22) { p[m - 18] = base[m - 18] + slot; PIN(p[m - 18]); } else if (m == W4POS) { ldswait(); IT_B(); } else if (m == 22) IT_A();
        SB(); } };
  bar(); ST(2);
#pragma unroll
  for (int i = 0; i < 18; ++i) rd(f, i, 0);
  slot = K4_STAGE; kstep(f, h, 1, std::true_type{}); sbar(); kstep(h, f, 0, std::false_type{});
  ST(4);
#pragma unroll U4
  for (int tt = 1; tt < 15; ++tt) { slot = slot == 2 * K4_STAGE ? 0 : slot + K4_STAGE; kstep(f, h, 1, std::false_type{}); sbar(); kstep(h, f, 0, std::false_type{});
  }
  ST(7);
  kstep(f, h, 1, std::false_type{});
#pragma unroll
  for (int n = 0; n < 7; ++n)
#pragma unroll
    for (int i = 0; i < 4; ++i) acc[n][i] = mfma16(cat(h.b[2 * n], h.b[2 * n + 1]), h.a[i], acc[n][i]);
  mfma_done(); bar();
  ST(3);
  f32x2 rn[4];
#pragma unroll
  for (int i = 0; i < 4; ++i) rn[i] = *(const LDS f32x2*)(sm + K4_RS + (th * 64 + i * 16 + (l & 15)) * 8);
  struct UT { f32x4 u, t; };
  auto ut = [&](int n) { const int col = ((n < 6 ? 8 * nh + n : 6 + nh) * 16 + 4 * g) * 4; return UT{*(const LDS f32x4*)(sm + K4_UT + col), *(const LDS f32x4*)(sm + K4_UT + 1024 + col)}; };
  auto act = [&](int n, int i, const UT& c) { u32x2 o;
#pragma unroll
    for (int e = 0; e < 2; ++e) { const f32x2 z = rn[i][0] * (f32x2){acc[n][i][2 * e], acc[n][i][2 * e + 1]} + (rn[i][1] * (f32x2){c.u[2 * e], c.u[2 * e + 1]} + (f32x2){c.t[2 * e], c.t[2 * e + 1]});
      const f32x2 dn = NL2E * (f32x2){EX2(z[0]), EX2(z[1])} + NL2E;
      const f32x2 r = z * (f32x2){__builtin_amdgcn_rcpf(dn[0]), __builtin_amdgcn_rcpf(dn[1])}; o[e] = pk(r[0], r[1]); }
    return o; };
  auto dst = [&](int gi, int i) { const int kk = nt * 7 + gi; return gb + (size_t)(((tb * 56 + (kk >> 1)) * 8 + th * 4 + i) * 2 + (kk & 1)) * 1024; };
  const unsigned vo = l * 16;
  UT c[7];
#pragma unroll
  for (int n = 0; n < 2; ++n) c[n] = ut(n);
#pragma unroll
  for (int p = 0; p < 3; ++p) { c[2 * p + 2] = ut(2 * p + 2); if (p < 2) c[2 * p + 3] = ut(2 * p + 3);
#pragma unroll
    for (int i = 0; i < 4; ++i) { const u32x2 lo = act(2 * p, i, c[2 * p]), hi = act(2 * p + 1, i, c[2 * p + 1]); gstore<0>(vo, dst(4 * nh + p, i), (u32x4){lo[0], lo[1], hi[0], hi[1]}); } }
#pragma unroll
  for (int i = 0; i < 4; ++i) gstore2<0>(vo, dst(3, i) + nh * 8, act(6, i, c[6]));
  IT_END(0); ST_END(4);
}

constexpr int K5_STAGE = 32768, K5_S = 5, K5_X = 3 * K5_STAGE, K5_LDS = K5_S * K5_STAGE;
struct FragC { bf16x8 a[4]; s16x4 b[8]; };
extern "C" __global__ __launch_bounds__(512, 2) void k_ffn2(const char* __restrict__ gb, const char* __restrict__ w2, const char* __restrict__ x1b,
                                                            const float* __restrict__ modf, char* __restrict__ out, char* xch) {
  const lptr sm = (lptr)smem_raw;
  ST_INIT();
  const int b = blockIdx.x, l = threadIdx.x & 63, w = __builtin_amdgcn_readfirstlane(threadIdx.x >> 6);
  const int kh = b & 1, j = b >> 3, tb = ((b & 7) >> 1) * 4 + (j & 3), nt = j >> 2;
  if (__builtin_expect(w >= 4, LFIRST)) { const int lw = w - 4;
    const char* ap = gb + (size_t)(tb * 56 + kh * 28) * 16384 + lw * 4096; const char* wp = w2 + (size_t)(nt * 56 + kh * 28) * 16384 + lw * 4096;
    const unsigned va = l * 16;
    loader<28, K5_S, D5, 8, K5_STAGE, 0, 0, 1, 5>([&](int t, int slot) {
      SETM0(slot + lw * 4096); const char* a = ap + (size_t)t * 16384; glds<0>(va, a); glds<1024>(va, a); glds<2048>(va, a); glds<3072>(va, a);
      SETM0(slot + 16384 + lw * 4096); const char* s = wp + (size_t)t * 16384;
      glds<0>(va, s); glds<1024>(va, s); glds<2048>(va, s); glds<3072>(va, s); }, [](auto) {},
      [&](auto) {
        const char* s = x1b + (size_t)((tb * 4 + (lw & 1) * 2 + kh) * 64 + nt * 8 + (lw >> 1) * 4) * 1024;
        SETM0(K5_X + lw * 4096); glds<0>(va, s); glds<1024>(va, s); glds<2048>(va, s); glds<3072>(va, s);
        SETM0(K5_X + 16384 + lw * 1024); glds<0>((l & 15) * 16, (const char*)(modf + 5 * DM + nt * 128 + (lw >> 1) * 64)); });
    waitbar<0>();
    return; }
  const int g = l >> 4, th = w & 1, nh = w >> 1, rx = 4 * (g & 1) + ((l >> 2) & 3);
  const int tok0 = tb * 128 + th * 64 + kh * 32, n0 = nt * 128 + nh * 64;
  u32x2 xr[2][4]; f32x4 gv[4];
  lptr base[6], p[6];
  base[4] = sm + th * 8192 + kh * 4096 + l * 16; base[5] = sm + th * 8192 + (kh ^ 1) * 4096 + l * 16;
#pragma unroll
  for (int n = 0; n < 4; ++n) base[n] = sm + 16384 + (4 * g + ((l >> 2) & 3)) * 256 + ((4 * nh + n) ^ rx) * 32 + (l & 3) * 8;
#pragma unroll
  for (int n = 0; n < 6; ++n) { PIN(base[n]); p[n] = base[n]; }
  f32x4 acc[4][4];
  FragC f, h; int slot = 0; IT_INIT();
  auto rd = [&](FragC& d, int i, int ks) {
    if (i < 4) d.a[i] = ldsA(p[4 + (i >> 1)] + (i & 1) * 2048 + ks * 1024); else d.b[i - 4] = ldsT1(p[(i - 4) >> 1] + ks * 8192 + (i & 1) * 4096); };
  auto kstep = [&](FragC& cur, FragC& nxt, int ks, auto first) {
#pragma unroll
    for (int n = 0; n < 4; ++n)
#pragma unroll
      for (int i = 0; i < 4; ++i) { const int m = n * 4 + i;
        acc[n][i] = mfma16(cat(cur.b[2 * n], cur.b[2 * n + 1]), cur.a[i], first ? (f32x4){} : acc[n][i]); SB();
        if (m < 12) rd(nxt, m, ks);
        else if (ks == 1) { p[m - 12] = base[m - 12] + slot; PIN(p[m - 12]); if (m < 14) { p[m - 8] = base[m - 8] + slot; PIN(p[m - 8]); } }
        if (W5V == 0 || ks == 1) { if (m == 15) { ldswait(); IT_B(); } else if (m == 12) IT_A(); } else if (m == 15) ldsw<4>();
        if (W5V == 1 && ks == 1 && m == 7) ldsw<8>();
        SB(); } };
  bar(); ST(2);
#pragma unroll
  for (int i = 0; i < 12; ++i) rd(f, i, 0);
  slot = K5_STAGE; kstep(f, h, 1, std::true_type{}); sbar(); kstep(h, f, 0, std::false_type{});
  ST(1);
#pragma unroll U5
  for (int tt = 1; tt < 25; ++tt) { slot = slot == (K5_S - 1) * K5_STAGE ? 0 : slot + K5_STAGE; kstep(f, h, 1, std::false_type{}); sbar(); kstep(h, f, 0, std::false_type{});
  }
  ST(7);
  auto hstep = [&](FragC& cur, FragC& nxt, int half, int nhalf, int ns) {
    const int off = (ns >> 1) * K5_STAGE + (ns & 1) * 1024, offw = (ns >> 1) * K5_STAGE + (ns & 1) * 8192;
#pragma unroll
    for (int m = 0; m < 8; ++m) { const int n = m >> 1, i = 2 * half + (m & 1);
      acc[n][i] = mfma16(cat(cur.b[2 * n], cur.b[2 * n + 1]), cur.a[i], acc[n][i]); SB();
      if (ns >= 0) { if (m < 2) nxt.a[2 * nhalf + m] = ldsA(base[4 + nhalf] + off + m * 2048); nxt.b[m] = ldsT1(base[m >> 1] + offw + (m & 1) * 4096); }
      SB(); } };
  hstep(f, h, 1, 1, 1); bar(); hstep(h, f, 1, 1, 2); hstep(f, h, 1, 1, 3); bar(); hstep(h, f, 1, 1, 4); hstep(f, h, 1, 1, 5); hstep(h, f, 1, 0, 0);
  ST(3);
  hstep(f, h, 0, 0, 1);
  { char* d = xch + (size_t)(b * 4 + w) * 4096;
#pragma unroll
    for (int i = 0; i < 2; ++i)
#pragma unroll
      for (int q = 0; q < 2; ++q) { const f32x4 u = acc[2 * q][2 + i], v = acc[2 * q + 1][2 + i];
        gstore<0>(l * 16, d + (i * 2 + q) * 1024, (u32x4){min(pk(u[0], u[1]), ~1u), min(pk(u[2], u[3]), ~1u), min(pk(v[0], v[1]), ~1u), min(pk(v[2], v[3]), ~1u)}); } }
  SB();
  hstep(h, f, 0, 0, 2); hstep(f, h, 0, 0, 3); hstep(h, f, 0, 0, 4); hstep(f, h, 0, 0, 5); hstep(h, f, 0, 0, -1);
  ST(4);
  sbar();
#pragma unroll
  for (int n = 0; n < 4; ++n) { gv[n] = *(const LDS f32x4*)(sm + K5_X + 16384 + w * 1024 + n * 64 + g * 16);
#pragma unroll
    for (int i = 0; i < 2; ++i) xr[i][n] = *(const LDS u32x2*)(sm + K5_X + w * 4096 + n * 1024 + (g & 1) * 512 + (i * 16 + (l & 15)) * 16 + (g >> 1) * 8); }
  u32x4 pr[4];
  { const char* s = xch + (size_t)((b ^ 1) * 4 + w) * 4096; const unsigned vo = l * 16;
    for (int spin = 0; spin < (1 << 16); ++spin) {
      pr[0] = gloadd<0>(vo, s); pr[1] = gloadd<1024>(vo, s); pr[2] = gloadd<2048>(vo, s); pr[3] = gloadd<3072>(vo, s);
      asm volatile("s_waitcnt vmcnt(0)" : "+v"(pr[0]), "+v"(pr[1]), "+v"(pr[2]), "+v"(pr[3])::"memory");
      unsigned m = 0;
#pragma unroll
      for (int i = 0; i < 4; ++i) m = max(max(max(m, pr[i][0]), max(pr[i][1], pr[i][2])), pr[i][3]);
      if (!__any(m == ~0u)) break;
    } }
  ST(6);
#pragma unroll
  for (int i = 0; i < 2; ++i)
#pragma unroll
    for (int p = 0; p < 2; ++p) { unsigned o[2][2];
#pragma unroll
      for (int q = 0; q < 2; ++q) { const int n = 2 * p + q; const unsigned u0 = pr[2 * i + p][2 * q], u1 = pr[2 * i + p][2 * q + 1];
        const f32x4 a = acc[n][i] + (f32x4){blo(u0), bhi(u0), blo(u1), bhi(u1)};
        o[q][0] = pk(blo(xr[i][n][0]) + gv[n][0] * a[0], bhi(xr[i][n][0]) + gv[n][1] * a[1]); o[q][1] = pk(blo(xr[i][n][1]) + gv[n][2] * a[2], bhi(xr[i][n][1]) + gv[n][3] * a[3]); }
      auto r0 = __builtin_amdgcn_permlane16_swap(o[0][0], o[1][0], false, false); auto r1 = __builtin_amdgcn_permlane16_swap(o[0][1], o[1][1], false, false);
      gstore<0>((l & 15) * 2048 + (g & 1) * 32 + (g >> 1) * 16, out + (size_t)(tok0 + i * 16) * 2048 + (n0 + 32 * p) * 2, (u32x4){r0[0], r1[0], r0[1], r1[1]}); }
  IT_END(1); ST_END(5);
}

extern "C" __global__ __launch_bounds__(256) void k_mod(const uint16_t* __restrict__ c, const char* __restrict__ wmod, float* __restrict__ modp,
                                             const char* __restrict__ x, const char* __restrict__ wq, const char* __restrict__ wk, const char* __restrict__ wv, char* __restrict__ wqp) {
  __shared__ float red[4][32];
  ST_INIT();
  const int t = threadIdx.x;
  if (blockIdx.x >= PF0 && blockIdx.x < PF0 + 256) { const int p = blockIdx.x - PF0; u32x4 d[7];
    const char* xb = x + (size_t)((p & 7) * 32 + (p >> 3)) * 16384;
    { const char* s = wq + (size_t)(p & 15) * 131072 + (p >> 4) * 128; const unsigned v = imgA(t & 63) + (t >> 6) * 32768; d[4] = gload<0>(v, s); d[5] = gload<0>(v, s + 16384); }
#pragma unroll
    for (int i = 0; i < 4; ++i) d[i] = gload<0>(t * 16, xb + i * 4096);
    d[6] = gload<0>((t & 31) * 16, (p & 128 ? wv : wk) + (p & 127) * 512);
    { CODE_RANGE(k_ln1kv, k_mod, cb, cs) const unsigned o = (p >> 3) * 2048 + t * 16; u32x4 e = gload<0>(t < 128 && o < cs ? o : 0, cb);
      asm volatile("s_waitcnt vmcnt(6)" : "+v"(d[4]), "+v"(d[5])::"memory");
      const unsigned v = (t & 63) * 16 + (t >> 6) * 2048; gstore<0>(v, wqp + p * 8192, d[4]); gstore<1024>(v, wqp + p * 8192, d[5]);
      asm volatile("s_waitcnt vmcnt(0)" : "+v"(e)::"memory"); }
    asm volatile("s_waitcnt vmcnt(0)" : "+v"(d[0]), "+v"(d[1]), "+v"(d[2]), "+v"(d[3]), "+v"(d[4]), "+v"(d[5]), "+v"(d[6])::"memory");
    return; }
  const int b = blockIdx.x < PF0 ? blockIdx.x : blockIdx.x - 256, xcd = b & 7, j = b >> 3;
  const int cg = (j & 1) + 2 * (xcd + 8 * ((j >> 1) % 12)), ks = j / 24;
  const int row0 = ks * 256 + (t >> 2);
  const char* p = wmod + (size_t)row0 * (NMOD * 2) + cg * 64 + (t & 3) * 16;
  u32x4 w[4]; float s[4];
#pragma unroll
  for (int i = 0; i < 4; ++i) { w[i] = *(const u32x4*)(p + (size_t)i * 64 * NMOD * 2); s[i] = __uint_as_float((unsigned)c[row0 + 64 * i] << 16); }
  float a[8] = {0, 0, 0, 0, 0, 0, 0, 0};
#pragma unroll
  for (int i = 0; i < 4; ++i) { const float f = rbf(silu(s[i]));
#pragma unroll
    for (int e = 0; e < 4; ++e) { a[2 * e] += f * blo(w[i][e]); a[2 * e + 1] += f * bhi(w[i][e]); } }
#pragma unroll
  for (int e = 0; e < 8; ++e) { float v = a[e]; v += __shfl_xor(v, 4); v += __shfl_xor(v, 8); v = swap16add(v); a[e] = swap32add(v); }
  if ((t & 63) < 4) {
#pragma unroll
    for (int e = 0; e < 8; ++e) red[t >> 6][(t & 3) * 8 + e] = a[e]; }
  __syncthreads();
  if (t < 32) modp[ks * NMOD + cg * 32 + t] = (red[0][t] + red[1][t]) + (red[2][t] + red[3][t]);
  ST_END(0);
}

extern "C" void kernel_launch(void* const* d_in, const int* in_sizes, int n_in, void* d_out, int out_size, void* d_ws, size_t ws_size, hipStream_t stream) {
  const char* x = (const char*)d_in[0]; const char* wq = (const char*)d_in[1]; const char* wk = (const char*)d_in[2]; const char* wv = (const char*)d_in[3];
  const char* wo = (const char*)d_in[4]; const char* w1 = (const char*)d_in[6]; const char* w2 = (const char*)d_in[7];
  const uint16_t* c = (const uint16_t*)d_in[8]; const char* wmod = (const char*)d_in[9];
  char* ws = (char*)d_ws;
  (void)hipFuncSetAttribute((const void*)k_ln1kv, hipFuncAttributeMaxDynamicSharedMemorySize, K1_LDS);
  (void)hipFuncSetAttribute((const void*)k_qattn, hipFuncAttributeMaxDynamicSharedMemorySize, K2_LDS);
  (void)hipFuncSetAttribute((const void*)k_oproj, hipFuncAttributeMaxDynamicSharedMemorySize, K3_LDS);
  (void)hipFuncSetAttribute((const void*)k_ffn1, hipFuncAttributeMaxDynamicSharedMemorySize, K4_LDS);
  (void)hipFuncSetAttribute((const void*)k_ffn2, hipFuncAttributeMaxDynamicSharedMemorySize, K5_LDS);
  float* modp = (float*)(ws + WS_MODP); float* modf = (float*)(ws + WS_MODF);
  k_mod<<<1024, 256, 0, stream>>>(c, wmod, modp, x, wq, wk, wv, ws + WS_WQP);
  k_ln1kv<<<256, 512, K1_LDS, stream>>>(x, wk, wv, (const char*)modp, modf, (uint16_t*)(ws + WS_MODH), ws + WS_H, ws + WS_KV);
  k_qattn<<<256, 512, K2_LDS, stream>>>(ws + WS_H, ws + WS_WQP, ws + WS_KV, ws + WS_O, w1, ws + WS_MODH, (float*)(ws + WS_UTP), wo, w2, ws + WS_XCH, ws + WS_WOP, ws + WS_W2P);
  k_oproj<<<256, 512, K3_LDS, stream>>>(ws + WS_O, ws + WS_WOP, x, modf, ws + WS_X1, ws + WS_Y, (f32x2*)(ws + WS_STAT), (const float*)(ws + WS_UTP), (float*)(ws + WS_UTF));
  k_ffn1<<<256, 512, K4_LDS, stream>>>(ws + WS_Y, w1, ws + WS_STAT, ws + WS_UTF, ws + WS_G);
  k_ffn2<<<256, 512, K5_LDS, stream>>>(ws + WS_G, ws + WS_W2P, ws + WS_X1, modf, (char*)d_out, ws + WS_XCH);
}
```

```cpp
#include <hip/hip_runtime.h>
#include <hip/hip_bf16.h>
#include <stdint.h>
#include <type_traits>

typedef __attribute__((ext_vector_type(2))) float f32x2;
typedef __attribute__((ext_vector_type(4))) float f32x4;
typedef __attribute__((ext_vector_type(16))) float f32x16;
typedef __attribute__((ext_vector_type(2))) __bf16 bf16x2;
typedef __attribute__((ext_vector_type(8))) __bf16 bf16x8;
typedef __attribute__((ext_vector_type(4))) short s16x4;
typedef __attribute__((ext_vector_type(2))) unsigned u32x2;
typedef __attribute__((ext_vector_type(4))) unsigned u32x4;
typedef __attribute__((ext_vector_type(8))) unsigned u32x8;
typedef __attribute__((ext_vector_type(16))) __bf16 bf16x16;
#define LDS __attribute__((address_space(3)))
typedef LDS char* lptr;
#define WPOS 1
#define W4POS 27
#define W5V 1
#define D2 4
#define D3 4
#define D5 4
#define U4 7
#define U5 6
#define WL 2
#define MPL 1
#define HP 0
#define PF0 384
#define NEXTPF 1
#define LFIRST 1
#define DEV __device__ __forceinline__

constexpr int SEQ = 2048, DM = 1024, FF = 3584, NMOD = 6 * DM;
constexpr size_t WS_MODP = 0;
constexpr size_t WS_UTP = 98304;
constexpr size_t WS_MODF = 212992;
constexpr size_t WS_MODH = 237568;
constexpr size_t WS_STAT = 262144;
constexpr size_t WS_KV = 786432;
constexpr size_t WS_UTF = (size_t)48 << 20;
constexpr size_t WS_H = (size_t)1 << 20;
constexpr size_t WS_O = (size_t)5 << 20;
constexpr size_t WS_X1 = (size_t)9 << 20;
constexpr size_t WS_Y = (size_t)13 << 20;
constexpr size_t WS_G = (size_t)17 << 20;
constexpr size_t WS_WQP = (size_t)36 << 20;
constexpr size_t WS_WOP = (size_t)38 << 20;
constexpr size_t WS_W2P = (size_t)40 << 20;
constexpr size_t WS_XCH = (size_t)32 << 20;

DEV unsigned pk(float a, float b) { return __builtin_bit_cast(unsigned, __builtin_convertvector((f32x2){a, b}, bf16x2)); }
DEV float rbf(float a) { return __uint_as_float(pk(a, 0.f) << 16); }
DEV float blo(unsigned u) { return __uint_as_float(u << 16); }
DEV float bhi(unsigned u) { return __uint_as_float(u & 0xffff0000u); }
DEV float silu(float z) { return z * __builtin_amdgcn_rcpf(1.f + __builtin_amdgcn_exp2f(-1.4426950408889634f * z)); }
DEV f32x16 mfma32(bf16x8 a, bf16x8 b, f32x16 c) { return __builtin_amdgcn_mfma_f32_32x32x16_bf16(a, b, c, 0, 0, 0); }
DEV f32x4 mfma16(bf16x8 a, bf16x8 b, f32x4 c) { return __builtin_amdgcn_mfma_f32_16x16x32_bf16(a, b, c, 0, 0, 0); }
DEV bf16x8 asb(u32x4 v) { return __builtin_bit_cast(bf16x8, v); }
DEV bf16x8 ldsA(lptr p) { return *(const LDS bf16x8*)p; }
DEV bf16x8 ldsT(lptr p, int second) {
  s16x4 a = __builtin_amdgcn_ds_read_tr16_b64_v4i16((LDS s16x4*)p);
  s16x4 b = __builtin_amdgcn_ds_read_tr16_b64_v4i16((LDS s16x4*)(p + second));
  return __builtin_bit_cast(bf16x8, __builtin_shufflevector(a, b, 0, 1, 2, 3, 4, 5, 6, 7));
}
DEV s16x4 ldsT1(lptr p) { return __builtin_amdgcn_ds_read_tr16_b64_v4i16((LDS s16x4*)p); }
DEV bf16x8 cat(s16x4 a, s16x4 b) { return __builtin_bit_cast(bf16x8, __builtin_shufflevector(a, b, 0, 1, 2, 3, 4, 5, 6, 7)); }
template <int C> DEV float dppadd(float v) { return v + __int_as_float(__builtin_amdgcn_update_dpp(0, __float_as_int(v), C, 0xf, 0xf, true)); }
DEV float swap32add(float v) { auto r = __builtin_amdgcn_permlane32_swap(__float_as_uint(v), __float_as_uint(v), false, false); return __uint_as_float(r[0]) + __uint_as_float(r[1]); }
DEV float swap16add(float v) { auto r = __builtin_amdgcn_permlane16_swap(__float_as_uint(v), __float_as_uint(v), false, false); return __uint_as_float(r[0]) + __uint_as_float(r[1]); }
DEV float swap32max(float v) { auto r = __builtin_amdgcn_permlane32_swap(__float_as_uint(v), __float_as_uint(v), false, false); return __builtin_fmaxf(__uint_as_float(r[0]), __uint_as_float(r[1])); }
DEV float wavesum(float v) { v = dppadd<0xB1>(v); v = dppadd<0x4E>(v); v = dppadd<0x141>(v); v = dppadd<0x140>(v); v = swap16add(v); return swap32add(v); }

#define SETM0(x) asm volatile("s_mov_b32 m0, %0\n\ts_nop 0" ::"s"(x) : "memory")
template <int OFF> DEV void glds(unsigned voff, const void* base) { asm volatile("global_load_lds_dwordx4 %0, %1 offset:%2" ::"v"(voff), "s"(base), "i"(OFF) : "memory"); }
template <int N> DEV void waitbar() { asm volatile("s_waitcnt vmcnt(%0) lgkmcnt(0)\n\ts_barrier" ::"i"(N) : "memory"); }
template <int OFF> DEV u32x4 gload(unsigned voff, const void* base) { u32x4 r; asm volatile("global_load_dwordx4 %0, %1, %2 offset:%3" : "=v"(r) : "v"(voff), "s"(base), "i"(OFF) : "memory"); return r; }
template <int OFF> DEV u32x2 gload2(unsigned voff, const void* base) { u32x2 r; asm volatile("global_load_dwordx2 %0, %1, %2 offset:%3" : "=v"(r) : "v"(voff), "s"(base), "i"(OFF) : "memory"); return r; }
template <int OFF> DEV unsigned gload1(unsigned voff, const void* base) { unsigned r; asm volatile("global_load_dword %0, %1, %2 offset:%3" : "=v"(r) : "v"(voff), "s"(base), "i"(OFF) : "memory"); return r; }
template <int OFF> DEV u32x4 gloadd(unsigned voff, const void* base) { u32x4 r; asm volatile("global_load_dwordx4 %0, %1, %2 offset:%3 sc1" : "=v"(r) : "v"(voff), "s"(base), "i"(OFF) : "memory"); return r; }
template <int OFF> DEV void gstore2(unsigned voff, void* base, u32x2 v) { asm volatile("global_store_dwordx2 %0, %1, %2 offset:%3 sc1" ::"v"(voff), "v"(v), "s"(base), "i"(OFF) : "memory"); }
template <int OFF> DEV void gstore(unsigned voff, void* base, u32x4 v) { asm volatile("global_store_dwordx4 %0, %1, %2 offset:%3 sc1\n\ts_nop 1" ::"v"(voff), "v"(v), "s"(base), "i"(OFF) : "memory"); }
#define CODE_RANGE(A, B, base, size) const char* base; unsigned size; { unsigned long long pc_; int o_; \
  asm volatile("s_getpc_b64 %0\n.Lcr%=:\n\ts_mov_b32 %1, " #A "-.Lcr%=\n\ts_mov_b32 %2, " #B "-" #A : "=s"(pc_), "=s"(o_), "=s"(size)); base = (const char*)(pc_ + o_); }
#define SB() __builtin_amdgcn_sched_barrier(0)
#define PIN(x) asm volatile("" : "+v"(x))
extern __shared__ __attribute__((aligned(16))) char smem_raw[];

struct Stamps {};
#define ST_INIT() Stamps S_
#define ST(i)
#define IT_INIT()
#define IT_A()
#define IT_B()
#define IT_END(k)
#define ST_END(k)

constexpr int K1_W = 0, K1_H = 65536, K1_MV = K1_H + 64 * 528, K1_RED = K1_MV + 8192, K1_LDS = K1_RED + 32768;
extern "C" __global__ __launch_bounds__(512, 2) void k_ln1kv(const char* __restrict__ x, const char* __restrict__ wk, const char* __restrict__ wv,
                                                  const char* __restrict__ modp, float* __restrict__ modf, uint16_t* __restrict__ modh,
                                                  char* __restrict__ hbuf, char* __restrict__ kv) {
  const lptr sm = (lptr)smem_raw;
  ST_INIT();
  const int b = blockIdx.x, t = threadIdx.x, l = t & 63, w = __builtin_amdgcn_readfirstlane(t >> 6);
  const int which = (b >> 3) & 1, rb = (b & 7) * 16 + (b >> 4);
  u32x4 xv[4], mp[4];
  { const char* xb = x + (size_t)(rb * 16 + 2 * w) * 2048; const unsigned vo = l * 32;
    xv[0] = gload<0>(vo, xb); xv[1] = gload<16>(vo, xb); xv[2] = gload<2048>(vo, xb); xv[3] = gload<2064>(vo, xb);
    if (MPL) asm volatile("s_waitcnt vmcnt(0)" ::: "memory");
#pragma unroll
    for (int s = 0; s < 4; ++s) mp[s] = gload<0>(t * 16, modp + s * NMOD * 4); }
  asm volatile("s_waitcnt vmcnt(4)" : "+v"(xv[0]), "+v"(xv[1]), "+v"(xv[2]), "+v"(xv[3])::"memory");
  ST(1);
  const char* wsrc = (which ? wv : wk) + w * 8192;
  auto wload = [&](int g) { SETM0(K1_W + w * 8192 + g * 2048); glds<0>(l * 16, wsrc + g * 2048); glds<1024>(l * 16, wsrc + g * 2048); };
  float rs[2], nm[2];
  { float s[4] = {0.f, 0.f, 0.f, 0.f}; const bf16x2 one = __builtin_bit_cast(bf16x2, 0x3f803f80u);
#pragma unroll
    for (int e = 0; e < 8; ++e)
#pragma unroll
      for (int r = 0; r < 2; ++r) { const unsigned xx = xv[2 * r + (e >> 2)][e & 3]; const bf16x2 u = __builtin_bit_cast(bf16x2, xx);
        s[r] = __builtin_amdgcn_fdot2_f32_bf16(u, one, s[r], false); s[2 + r] = __builtin_amdgcn_fdot2_f32_bf16(u, u, s[2 + r], false); }
#pragma unroll
    for (int i = 0; i < 4; ++i) s[i] = wavesum(s[i]) * (1.f / 1024.f);
#pragma unroll
    for (int r = 0; r < 2; ++r) { rs[r] = __builtin_amdgcn_rsqf(__builtin_fmaxf(s[2 + r] - s[r] * s[r], 0.f) + 1e-5f); nm[r] = -s[r] * rs[r]; } }
  PIN(rs[0]); PIN(rs[1]);
  asm volatile("s_waitcnt vmcnt(0)" : "+v"(mp[0]), "+v"(mp[1]), "+v"(mp[2]), "+v"(mp[3])::"memory");
  { f32x4 o;
#pragma unroll
    for (int e = 0; e < 4; ++e) { const float v = rbf((__uint_as_float(mp[0][e]) + __uint_as_float(mp[1][e])) + (__uint_as_float(mp[2][e]) + __uint_as_float(mp[3][e])));
      o[e] = rbf(v + (w >= 4 ? 1.f : 0.f)); }
    *(LDS f32x4*)(sm + K1_MV + (t & 256) * 16 + (t & 3) * 1024 + ((t & 255) >> 2) * 16) = o; }
  if (WL == 1) { wload(0); } if (WL == 2) { wload(0); wload(1); }
  ST(4);
  asm volatile("s_waitcnt lgkmcnt(0)\n\ts_barrier" ::: "memory");
  ST(5);
  { unsigned u[2][8];
#pragma unroll
    for (int e = 0; e < 4; ++e) { const f32x4 a = *(const LDS f32x4*)(sm + K1_MV + e * 1024 + l * 16), cc = *(const LDS f32x4*)(sm + K1_MV + 4096 + e * 1024 + l * 16);
      if (e + WL < 4) { SB(); wload(e + WL); SB(); }
#pragma unroll
      for (int r = 0; r < 2; ++r)
#pragma unroll
        for (int i = 0; i < 2; ++i) { const unsigned xx = xv[2 * r + (e >> 1)][(2 * e + i) & 3];
          const f32x2 h = ((f32x2){blo(xx), bhi(xx)} * rs[r] + nm[r]) * (f32x2){cc[2 * i], cc[2 * i + 1]} + (f32x2){a[2 * i], a[2 * i + 1]}; u[r][2 * e + i] = pk(h[0], h[1]); } }
#pragma unroll
    for (int r = 0; r < 2; ++r) { const lptr d = sm + K1_H + l * 528 + (2 * w + r) * 16;
      *(LDS u32x4*)d = (u32x4){u[r][0], u[r][1], u[r][4], u[r][5]}; *(LDS u32x4*)(d + 256) = (u32x4){u[r][2], u[r][3], u[r][6], u[r][7]}; } }
  ST(2); waitbar<0>(); ST(3);
  auto hst = [&](int) {
#pragma unroll
    for (int i = 0; i < 2; ++i) { const int id = t + 512 * i, tok = id & 15, h2 = (id >> 4) & 1, kb = which * 32 + (id >> 5);
      gstore<0>(kb * 1024 + (h2 * 32 + tok) * 16, hbuf + (size_t)(rb >> 1) * 65536 + (rb & 1) * 256, *(const LDS u32x4*)(sm + K1_H + kb * 528 + h2 * 256 + tok * 16)); }
  };
  f32x16 acc = {};
  { const int hi = l >> 5; const lptr hp = sm + K1_H + w * 8 * 528 + hi * 256 + (l & 15) * 16;
    const lptr wp = sm + K1_W + (w * 128 + 4 * hi + ((l >> 2) & 3)) * 64 + ((l >> 4) & 1) * 32 + (l & 3) * 8;
    bf16x8 fa[8], fb[8];
#pragma unroll
    for (int i = 0; i < 8; ++i) { fa[i] = ldsA(hp + i * 528); fb[i] = ldsT(wp + i * 1024, 512); }
    if (HP == 0) hst(0);
    if (which == 0) {
#pragma unroll
      for (int i = 0; i < 8; ++i) acc = mfma32(fb[i], fa[i], acc);
    } else {
#pragma unroll
      for (int i = 0; i < 8; ++i) acc = mfma32(fa[i], fb[i], acc);
    } }
#pragma unroll
  for (int q = 0; q < 4; ++q) *(LDS f32x4*)(sm + K1_RED + w * 4096 + q * 1024 + l * 16) = (f32x4){acc[4 * q], acc[4 * q + 1], acc[4 * q + 2], acc[4 * q + 3]};
  if (HP == 1) hst(0);
  ST(6);
  asm volatile("s_waitcnt lgkmcnt(0)\n\ts_barrier" ::: "memory");
  ST(7);

  if (w == 0) {
    const lptr rp = sm + K1_RED + (which ? l * 16 : (l >> 5) * 2048 + (((l >> 4) & 1) * 32 + (l & 15)) * 16);
    f32x4 a = {}, c = {};
#pragma unroll
    for (int u = 0; u < 8; ++u) { a += *(const LDS f32x4*)(rp + u * 4096); c += *(const LDS f32x4*)(rp + u * 4096 + 1024); }
    char* base = kv + (rb >> 1) * 4096;
    char* d = which ? base + 2048 + (rb & 1) * 1024 + l * 16 : base + (l >> 5) * 1024 + (((l >> 4) & 1) * 32 + (rb & 1) * 16 + (l & 15)) * 16;
    *(u32x4*)d = (u32x4){pk(a[0], a[1]), pk(a[2], a[3]), pk(c[0], c[1]), pk(c[2], c[3])};
  } else if (b < 64 && w == 1) {
    const int col = 2048 + b * 64 + l; const float* mpf = (const float*)modp;
    float v2 = rbf((mpf[col] + mpf[NMOD + col]) + (mpf[2 * NMOD + col] + mpf[3 * NMOD + col]));
    if ((col >> 10) == 4) v2 = rbf(1.f + v2);
    modf[col] = v2; modh[col] = (uint16_t)(__float_as_uint(v2) >> 16);
  }
  ST_END(1);
}

DEV void mfma_done() { SB(); asm volatile("s_nop 7\n\ts_nop 7\n\ts_nop 7"); SB(); }
DEV void ldswait() { __builtin_amdgcn_s_waitcnt(0xC07F); }
template <int N> DEV void ldsw() { __builtin_amdgcn_s_waitcnt(0xC07F | (N << 8)); }
DEV void sbar() { asm volatile("s_barrier" ::: "memory"); }
DEV void bar() { asm volatile("" ::: "memory"); ldswait(); asm volatile("s_barrier" ::: "memory"); }
template <int N, int G, int K, int LN, int LP, class L> DEV void loader_tail(L&& late) {
  waitbar<G * N + LP * (K < LN ? K : LN)>(); if constexpr (K < LN) late(std::integral_constant<int, K>{});
  if constexpr (N > 0) loader_tail<N - 1, G, K + 1, LN, LP>(late); }
constexpr int younger(int t, int D, int EN) { const int a = t - D + 2 < 1 ? 1 : t - D + 2, b = t < EN ? t : EN; return b >= a ? b - a + 1 : 0; }
template <int T, int T0, int S, int D, int G, int STAGE, int EN, int EP, class I, class E> DEV void loader_ramp(I&& issue, E&& early) {
#pragma unroll
  for (int s = T == 0 ? 0 : D + T - 2; s < D + T - 1; ++s) issue(s, (s % S) * STAGE);
  if constexpr (T >= 1 && T <= EN) early(std::integral_constant<int, T - 1>{});
  waitbar<G*(D - 2) + EP * younger(T, D, EN)>();
  if constexpr (T < T0) loader_ramp<T + 1, T0, S, D, G, STAGE, EN, EP>(issue, early);
}
template <int KT, int S, int D, int G, int STAGE, int EN, int EP, int LN, int LP, class I, class E, class L>
DEV void loader(I&& issue, E&& early, L&& late) {
  static_assert(D <= S && EN + D - 2 <= KT - D, "");
  loader_ramp<0, KT - D, S, D, G, STAGE, EN, EP>(issue, early);
  issue(KT - 1, ((KT - 1) % S) * STAGE);
  loader_tail<D - 2, G, 0, LN, LP>(late);
}
constexpr int GA_STAGE = 24576;
DEV unsigned imgA(int l) { return (l >> 3) * (DM * 2) + (((l & 7) ^ (((l >> 4) & 1) << 2)) * 16); }
template <int KT, int S, int D, int EN, int EP, int LN, int LP, class E, class L>
DEV void gemmA_load(const char* act, const char* wgt, int l, int lw, E&& early, L&& late) {
  const char* ap = act + (size_t)lw * (KT * 4096);
  const char* wp = wgt + lw * 2048;
  const unsigned va = l * 16;
  loader<KT, S, D, 6, GA_STAGE, EN, EP, LN, LP>([&](int t, int slot) {
    SETM0(slot + lw * 4096); const char* a = ap + (size_t)t * 4096; glds<0>(va, a); glds<1024>(va, a); glds<2048>(va, a); glds<3072>(va, a);
    SETM0(slot + 16384 + lw * 2048); const char* s = wp + t * 8192; glds<0>(va, s); glds<1024>(va, s); }, early, late);
}
struct FragA { bf16x8 a0[4], a1[4], b[4]; };
template <int KT, int S>
DEV void gemmA_compute(const lptr sm, int l, int w, f32x16& c0, f32x16& c1, Stamps& S_) {
  const int q = (l >> 2) & 3, p = l & 3;
  const lptr ra = sm + (w & 1) * 8192 + l * 16;
  const lptr rb = sm + 16384 + (4 * (l >> 5) + q) * 128 + ((((w >> 1) * 4 + ((l >> 4) & 1) * 2 + (p >> 1)) ^ ((q >> 1) << 2)) * 16) + (p & 1) * 8;
  c0 = (f32x16){}; c1 = (f32x16){};
  FragA f, g; int slot = 0; IT_INIT();
  auto rd = [&](FragA& d, int k) { d.a0[k] = ldsA(ra + slot + k * 1024); d.b[k] = ldsT(rb + slot + k * 2048, 1024); d.a1[k] = ldsA(ra + slot + 4096 + k * 1024); };
  auto step = [&](FragA& cur, FragA& nxt) { IT_A(); IT_B(); bar(); slot = slot == (S - 1) * GA_STAGE ? 0 : slot + GA_STAGE;
#pragma unroll
    for (int k = 0; k < 4; ++k) { if (k < 2) rd(nxt, 2 * k); c0 = mfma32(cur.b[k], cur.a0[k], c0); SB(); if (k < 2) rd(nxt, 2 * k + 1); c1 = mfma32(cur.b[k], cur.a1[k], c1); SB(); } };
  bar(); ST(2);
#pragma unroll
  for (int k = 0; k < 4; ++k) rd(f, k);
  for (int t = 0; t < KT - 2; t += 2) { step(f, g); step(g, f); }
  step(f, g);
#pragma unroll
  for (int k = 0; k < 4; ++k) { c0 = mfma32(g.b[k], g.a0[k], c0); c1 = mfma32(g.b[k], g.a1[k], c1); }
  mfma_done();
  ST(3); IT_END(S == 4 ? 2 : 3);
}

constexpr int K2_KV0 = 98304, K2_KV1 = 131072, K2_KV2 = 0, K2_UW = 32768, K2_Q = 65536, K2_RED = 73728, K2_DUMP = 81920, K2_LDS = 163840;
DEV void pack16(const f32x16& s, u32x4& a, u32x4& b) {
  a = (u32x4){pk(s[0], s[1]), pk(s[2], s[3]), pk(s[4], s[5]), pk(s[6], s[7])};
  b = (u32x4){pk(s[8], s[9]), pk(s[10], s[11]), pk(s[12], s[13]), pk(s[14], s[15])}; }
DEV float max16(const f32x16& s) { float m = s[0];
#pragma unroll
  for (int i = 1; i < 16; ++i) m = __builtin_fmaxf(m, s[i]);
  return swap32max(m); }
#define EX2(v) __builtin_amdgcn_exp2f(v)

extern "C" __global__ __launch_bounds__(512, 2) void k_qattn(const char* __restrict__ hbuf, const char* __restrict__ wq, const char* __restrict__ kv, char* __restrict__ obuf,
                                                  const char* __restrict__ w1, const char* __restrict__ modh, float* __restrict__ utp,
                                                  const char* __restrict__ wo, const char* __restrict__ w2, char* __restrict__ xch, char* __restrict__ wop, char* __restrict__ w2p) {
  const lptr sm = (lptr)smem_raw;
  ST_INIT();
  const int b = blockIdx.x, l = threadIdx.x & 63, w = __builtin_amdgcn_readfirstlane(threadIdx.x >> 6);
  const int xcd = b & 7, j = b >> 3, tb = (xcd >> 1) * 4 + (j & 3), hp = (xcd & 1) * 8 + (j >> 2);
  const unsigned l16 = l * 16;
  if (w >= 4) { const int lw = w - 4;
    auto kvload = [&](int st, int slot) {
#pragma unroll
      for (int g = 0; g < 2; ++g) { SETM0(slot + lw * 8192 + g * 4096); const char* s = kv + st * 32768 + lw * 8192 + g * 4096;
        glds<0>(l16, s); glds<1024>(l16, s); glds<2048>(l16, s); glds<3072>(l16, s); } };
    gemmA_load<16, 4, D2, 8, 1, 2, 4>(hbuf + (size_t)tb * 4 * 64 * 1024, wq + hp * 131072, l, lw,
      [&](auto i) { SETM0(K2_KV0 + lw * 8192 + i * 1024); glds<0>(l16, kv + lw * 8192 + i * 1024); },
      [&](auto g) { SETM0(K2_KV1 + lw * 8192 + g * 4096); const char* s = kv + 32768 + lw * 8192 + g * 4096; glds<0>(l16, s); glds<1024>(l16, s); glds<2048>(l16, s); glds<3072>(l16, s); });
    waitbar<8>();
    const int ub = b < 224 ? b : b - 224, cg = ub % 56, ks = ub / 56;
    u32x2 vf[8];
    waitbar<0>(); kvload(2, K2_KV2);
    { const unsigned vo = (l >> 3) * (FF * 2) + (((l & 7) ^ (((l >> 4) & 1) << 2)) * 16);
#pragma unroll
      for (int g = 0; g < 2; ++g) { SETM0(K2_UW + lw * 8192 + g * 4096); const char* s = w1 + (size_t)(ks * 256 + lw * 64 + g * 32) * (FF * 2) + cg * 128;
        glds<0>(vo, s); glds<1024>(vo, s + 8 * FF * 2 - 1024); glds<2048>(vo, s + 16 * FF * 2 - 2048); glds<3072>(vo, s + 24 * FF * 2 - 3072); }
      const char* mv = modh + (ks * 256 + lw * 64) * 2; const unsigned vm = ((4 - (l & 1)) * DM + 4 * (l >> 5)) * 2;
      vf[0] = gload2<0>(vm, mv); vf[1] = gload2<16>(vm, mv); vf[2] = gload2<32>(vm, mv); vf[3] = gload2<48>(vm, mv);
      vf[4] = gload2<64>(vm, mv); vf[5] = gload2<80>(vm, mv); vf[6] = gload2<96>(vm, mv); vf[7] = gload2<112>(vm, mv); }
    waitbar<16>(); kvload(3, K2_KV0);
    u32x4 rp[9];
    { const char* s = wo + (size_t)((b & 15) * 64 + lw * 16) * 2048 + (b >> 4) * 128; rp[7] = gload<0>(imgA(l), s); rp[8] = gload<0>(imgA(l), s + 16384);
      const int r = ((b & 31) * 4 + lw) * 7; const unsigned v0 = (l >> 4) * 2048 + ((l & 15) ^ ((l >> 4) * 2)) * 16;
#pragma unroll
      for (int i = 0; i < 7; ++i) rp[i] = gload<0>(v0 ^ (((r + i) & 1) * 128), w2 + (size_t)(r + i) * 8192 + (b >> 5) * 256); }
    waitbar<9>(); kvload(4, K2_KV1);
    { const int q = (l >> 2) & 3, p = l & 3; f32x16 d0 = {}, d1 = {};
      const lptr rb = sm + K2_UW + (lw * 64 + 4 * (l >> 5) + q) * 128 + (((((l >> 4) & 1) * 2 + (p >> 1)) ^ ((q >> 1) << 2)) * 16) + (p & 1) * 8;
      const lptr rc = sm + K2_UW + (lw * 64 + 4 * (l >> 5) + q) * 128 + (((4 + ((l >> 4) & 1) * 2 + (p >> 1)) ^ ((q >> 1) << 2)) * 16) + (p & 1) * 8;
#pragma unroll
      for (int kk = 0; kk < 4; ++kk) { PIN(vf[2 * kk]); PIN(vf[2 * kk + 1]); const bf16x8 vv = asb((u32x4){vf[2 * kk][0], vf[2 * kk][1], vf[2 * kk + 1][0], vf[2 * kk + 1][1]});
        d0 = mfma32(ldsT(rb + kk * 2048, 1024), vv, d0); d1 = mfma32(ldsT(rc + kk * 2048, 1024), vv, d1); }
      if ((l & 31) < 2) { const lptr d = sm + K2_RED + lw * 512 + (l & 31) * 256 + (l >> 5) * 16;
#pragma unroll
        for (int i = 0; i < 4; ++i) { *(LDS f32x4*)(d + i * 32) = (f32x4){d0[4 * i], d0[4 * i + 1], d0[4 * i + 2], d0[4 * i + 3]};
          *(LDS f32x4*)(d + 128 + i * 32) = (f32x4){d1[4 * i], d1[4 * i + 1], d1[4 * i + 2], d1[4 * i + 3]}; } } }
    waitbar<0>(); kvload(5, K2_KV2);
    if (b < 224 && lw < 2) { float v = 0.f;
#pragma unroll
      for (int u = 0; u < 4; ++u) v += *(const LDS float*)(sm + K2_RED + u * 512 + lw * 256 + l * 4);
      utp[(ks * 2 + lw) * FF + cg * 64 + l] = v; }
    waitbar<0>(); kvload(6, K2_KV0);
    { char* d = xch + b * 16384 + lw * 4096; const u32x4 e = {~0u, ~0u, ~0u, ~0u}; gstore<0>(l16, d, e); gstore<1024>(l16, d, e); gstore<2048>(l16, d, e); gstore<3072>(l16, d, e);
#pragma unroll
      for (int i = 0; i < 9; ++i) PIN(rp[i]);
      char* o = wop + b * 8192 + lw * 2048; gstore<0>(l16, o, rp[7]); gstore<1024>(l16, o, rp[8]);
#pragma unroll
      for (int i = 0; i < 7; ++i) gstore<0>(l16, w2p + (size_t)(b * 4 + lw) * 7168 + i * 1024, rp[i]); }
    waitbar<13>(); kvload(7, K2_KV1);
    { CODE_RANGE(k_oproj, k_mod, cb, cs) const unsigned o = (j & 15) * 4096 + lw * 1024 + l16; SETM0(K2_DUMP + lw * 1024); glds<0>(o < cs ? o : 0, cb); }
    waitbar<1>();
    asm volatile("s_waitcnt vmcnt(0)" ::: "memory");
    return; }
  bf16x8 qA0, qA1, qB0, qB1;
  { f32x16 c0, c1; gemmA_compute<16, 4>(sm, l, w, c0, c1, S_);
    constexpr float QS = 0.17677669529663687f * 1.4426950408889634f;
    c0 *= QS; c1 *= QS;
    u32x4 a, c; pack16(c0, a, c); qA0 = asb(a); qA1 = asb(c); pack16(c1, a, c); qB0 = asb(a); qB1 = asb(c); }
  bar();
  const unsigned onea = (l & 6) == 0 ? 0x3f803f80u : 0u, oneb = (l & 6) == 4 ? 0x3f803f80u : 0u;
  const bf16x8 ones = asb((u32x4){onea, onea, oneb, oneb}); const int sel = (l & 1) ? 0xEEEEEEEE : 0x44444444;
  auto rowsum = [&](u32x8 p, f32x4 a) { return __builtin_amdgcn_smfmac_f32_16x16x64_bf16(ones, __builtin_bit_cast(bf16x16, p), a, sel, 0, 0); };
  auto lo = [](u32x8 p) { return asb(__builtin_shufflevector(p, p, 0, 1, 2, 3)); };
  auto hi = [](u32x8 p) { return asb(__builtin_shufflevector(p, p, 4, 5, 6, 7)); };
  const lptr kp0 = sm + K2_KV0 + l16, kp1 = sm + K2_KV1 + l16, kp2 = sm + K2_KV2 + l16;
  bf16x8 k0a = ldsA(kp0), k1a = ldsA(kp0 + 1024), v0a = ldsA(kp0 + 2048), v1a = ldsA(kp0 + 3072), k0b = ldsA(kp0 + 4096), k1b = ldsA(kp0 + 5120), v0b, v1b = k0a;
  f32x16 nA, nB, sA, sB, oA = {}, oB = {}; f32x4 lA = {}, lB = {};
  { f32x16 z = {}, y = {}; z = mfma32(k0a, qA0, z); y = mfma32(k0a, qB0, y); z = mfma32(k1a, qA1, z); y = mfma32(k1a, qB1, y); const float m = -max16(z), n = -max16(y);
#pragma unroll
    for (int i = 0; i < 16; ++i) { nA[i] = m; nB[i] = n; } }
  PIN(nA); PIN(nB);
  sA = mfma32(k0a, qA0, nA); sA = mfma32(k1a, qA1, sA);
  u32x8 pA = {}, pB = {};
#define HALF(S, N, Q0, Q1, NEG, OS, ON, LN, PS, PN, K0, V1, K1, V0, W, R0, R1)                                                   \
  { S[0] = EX2(S[0]); S[1] = EX2(S[1]); PIN(S); SB();                                                                 \
    if (WPOS == 3) { W; } SB(); N = mfma32(K0, Q0, NEG); SB(); R0; SB();                                                                          \
    S[2] = EX2(S[2]); S[3] = EX2(S[3]); PS[0] = pk(S[0], S[1]); PS[1] = pk(S[2], S[3]); PIN(PS); SB();                \
    ON = mfma32(V1, hi(PN), ON); SB(); R1; SB();                                                                      \
    S[4] = EX2(S[4]); S[5] = EX2(S[5]); S[6] = EX2(S[6]); S[7] = EX2(S[7]); PS[2] = pk(S[4], S[5]); PS[3] = pk(S[6], S[7]); PIN(PS); SB(); \
    if (WPOS == 4) { W; } SB(); LN = rowsum(PN, LN); SB();                                                                                        \
    S[8] = EX2(S[8]); PIN(S); SB();                                                                                   \
    if (WPOS == 2) { W; } SB(); N = mfma32(K1, Q1, N); SB(); if (WPOS == 0) { W; } SB();                                                                             \
    S[9] = EX2(S[9]); S[10] = EX2(S[10]); S[11] = EX2(S[11]); PS[4] = pk(S[8], S[9]); PS[5] = pk(S[10], S[11]); PIN(PS); SB(); \
    S[12] = EX2(S[12]); S[13] = EX2(S[13]); PIN(S); SB();                                                             \
    if (WPOS == 1) { W; } SB(); OS = mfma32(V0, lo(PS), OS); SB();                                                                                \
    S[14] = EX2(S[14]); S[15] = EX2(S[15]); PS[6] = pk(S[12], S[13]); PS[7] = pk(S[14], S[15]); PIN(PS); SB(); }
#define TILE(X, Y, C, N)                                                                                               \
  HALF(sA, sB, qB0, qB1, nB, oA, oB, lB, pA, pB, k0##C, v1##N, k1##C, v0##C, __builtin_amdgcn_s_waitcnt(0xC07F), , )   \
  HALF(sB, sA, qA0, qA1, nA, oB, oA, lA, pB, pA, k0##N, v1##C, k1##N, v0##C, , (v0##N = ldsA((X) + 2048), v1##N = ldsA((X) + 3072)), (k0##C = ldsA(Y), k1##C = ldsA((Y) + 1024)))
#define TILE2(T, X) TILE(T + 4096, X, a, b) TILE(X, X + 4096, b, a)
#define STAGE(P, Q, BAR) TILE2(P, P + 2 * 4096) TILE2(P + 2 * 4096, P + 4 * 4096) TILE2(P + 4 * 4096, P + 6 * 4096) BAR TILE2(P + 6 * 4096, Q)
  ST(4);
  { lptr c = kp0, n = kp1, f = kp2;
    for (int st = 0; st < 7; ++st) { STAGE(c, n, bar();) const lptr t = c; c = n; n = f; f = t; }
    STAGE(c, n, ) }
  ST(5);
  oB = mfma32(v1b, hi(pB), oB); lB = rowsum(pB, lB);
  mfma_done();
  auto finish = [&](f32x16& o, float lsum, bf16x8 q0, bf16x8 q1, int i) {
    float chk = lsum;
#pragma unroll
    for (int e = 0; e < 16; ++e) chk = __builtin_fmaxf(chk, __builtin_fabsf(o[e]) * 0x1p-40f);
    bool bad = !(chk < 0x1p80f);
#pragma unroll
    for (int e = 0; e < 16; ++e) bad |= (o[e] != o[e]);
    bad |= (lsum != lsum);
    if (__any(bad)) {
      float m = -INFINITY; lsum = 0.f; o = (f32x16){};
      for (int t = 0; t < 64; ++t) { const bf16x8* g = (const bf16x8*)(kv + t * 4096) + l;
        f32x16 s = {}; s = mfma32(g[0], q0, s); s = mfma32(g[64], q1, s); mfma_done();
        const float mn = __builtin_fmaxf(m, max16(s)), al = EX2(m - mn); m = mn; float rsum = 0.f;
#pragma unroll
        for (int e = 0; e < 16; ++e) { s[e] = rbf(EX2(s[e] - mn)); rsum += s[e]; o[e] *= al; }
        lsum = lsum * al + swap32add(rsum);
        u32x4 a, c; pack16(s, a, c); o = mfma32(g[128], asb(a), o); o = mfma32(g[192], asb(c), o); mfma_done(); } }
    o *= 1.f / lsum;
    u32x4 o0, o1; pack16(o, o0, o1);
    char* d = obuf + (size_t)((tb * 4 + 2 * (w & 1) + i) * 64 + (hp * 2 + (w >> 1)) * 2) * 1024;
    gstore<0>(l16, d, o0); gstore<1024>(l16, d, o1); };
  finish(oA, lA[0] + lA[1], qA0, qA1, 0); finish(oB, lB[0] + lB[1], qB0, qB1, 1);
  ST_END(2);
}

constexpr int K3_S = 6, K3_X = K3_S * GA_STAGE, K3_MV = 4 * GA_STAGE, K3_LDS = K3_X + 16384;
extern "C" __global__ __launch_bounds__(512, 2) void k_oproj(const char* __restrict__ obuf, const char* __restrict__ wo, const char* __restrict__ x, const float* __restrict__ modf,
                                                  char* __restrict__ x1b, char* __restrict__ yb, f32x2* __restrict__ stat,
                                                  const float* __restrict__ utp, float* __restrict__ utf) {
  const lptr sm = (lptr)smem_raw;
  ST_INIT();
  const int b = blockIdx.x, l = threadIdx.x & 63, w = __builtin_amdgcn_readfirstlane(threadIdx.x >> 6);
  const int xcd = b & 7, j = b >> 3, tb = (xcd >> 1) * 4 + (j & 3), nt = (xcd & 1) * 8 + (j >> 2);
  if (__builtin_expect(w >= 4, LFIRST)) { u32x4 e; const int lw = w - 4;
    gemmA_load<16, K3_S, D3, 0, 0, 2, 3>(obuf + (size_t)tb * 4 * 64 * 1024, wo + nt * 131072, l, lw, [](auto) {}, [&](auto g) {
      const char* s = x + (size_t)(tb * 128 + lw * 32 + g * 16) * 2048 + nt * 128; const unsigned vx = (l >> 3) * 2048 + ((l & 7) ^ (l >> 3)) * 16;
      SETM0(K3_X + lw * 4096 + g * 2048); glds<0>(vx, s); glds<1024>(vx, s + 16384 - 1024);
      if constexpr (g == 0) { SETM0(K3_MV + lw * 1024); glds<0>((l & 16) * 512 + (l & 15) * 16, (const char*)(modf + 2 * DM + nt * 64)); }
      else { CODE_RANGE(k_ffn1, k_ffn2, cb, cs) const unsigned o = (j & 7) * 4096 + lw * 1024 + l * 16; e = gload<0>(o < cs ? o : 0, cb); } });
    waitbar<1>();
    if (b < 28) { const int i = b * 256 + lw * 64 + l; const float v = (utp[i] + utp[2 * FF + i]) + (utp[4 * FF + i] + utp[6 * FF + i]);
      utf[i] = i < FF ? v : -1.4426950408889634f * v; }
    asm volatile("s_waitcnt vmcnt(0)" : "+v"(e)::"memory"); return; }
  const int hi = l >> 5, tb32 = tb * 4 + 2 * (w & 1), n0 = nt * 64 + (w >> 1) * 32;
  f32x16 acc[2]; gemmA_compute<16, K3_S>(sm, l, w, acc[0], acc[1], S_);
  bar();
  f32x4 ga[4], cb[4];
#pragma unroll
  for (int q = 0; q < 4; ++q) { const lptr p = sm + K3_MV + w * 1024 + (w >> 1) * 128 + q * 32 + hi * 16; ga[q] = *(const LDS f32x4*)p; cb[q] = *(const LDS f32x4*)(p + 256); }
  u32x2 xr[2][4];
#pragma unroll
  for (int i = 0; i < 2; ++i)
#pragma unroll
    for (int q = 0; q < 4; ++q) xr[i][q] = *(const LDS u32x2*)(sm + K3_X + ((w & 1) * 64 + i * 32 + (l & 31)) * 128 + (((w >> 1) * 4 + q) ^ (l & 7)) * 16 + hi * 8);
#pragma unroll
  for (int i = 0; i < 2; ++i) { unsigned xo[8], yo[8]; float x1[16], s = 0.f;
#pragma unroll
    for (int h = 0; h < 2; ++h) { const u32x4 xs = {xr[i][2 * h][0], xr[i][2 * h][1], xr[i][2 * h + 1][0], xr[i][2 * h + 1][1]};
#pragma unroll
      for (int e = 0; e < 4; ++e) { const int r = 8 * h + 2 * e, q = r >> 2, c = r & 3;
        const unsigned u = pk(blo(xs[e]) + ga[q][c] * acc[i][r], bhi(xs[e]) + ga[q][c + 1] * acc[i][r + 1]);
        x1[r] = blo(u); x1[r + 1] = bhi(u); xo[4 * h + e] = u; yo[4 * h + e] = pk(x1[r] * cb[q][c], x1[r + 1] * cb[q][c + 1]); s += x1[r] + x1[r + 1]; } }
    const size_t off = (size_t)((tb32 + i) * 64 + (n0 >> 4)) * 1024;
    gstore<0>(l * 16, yb + off, (u32x4){yo[0], yo[1], yo[2], yo[3]}); gstore<1024>(l * 16, yb + off, (u32x4){yo[4], yo[5], yo[6], yo[7]});
    gstore<0>(l * 16, x1b + off, (u32x4){xo[0], xo[1], xo[2], xo[3]}); gstore<1024>(l * 16, x1b + off, (u32x4){xo[4], xo[5], xo[6], xo[7]});
    const float mean = swap32add(s) * (1.f / 32.f); float m2 = 0.f;
#pragma unroll
    for (int e = 0; e < 16; ++e) m2 += (x1[e] - mean) * (x1[e] - mean);
    m2 = swap32add(m2);
    if (hi == 0) stat[((tb32 + i) * 32 + (n0 >> 5)) * 32 + l] = (f32x2){mean, m2}; }
  ST_END(3);
}

constexpr int K4_STAGE = 45056, K4_UT = 3 * K4_STAGE, K4_RS = K4_UT + 2048, K4_LDS = K4_RS + 1024;
struct FragB { bf16x8 a[4]; s16x4 b[14]; };
extern "C" __global__ __launch_bounds__(512, 2) void k_ffn1(const char* __restrict__ yb, const char* __restrict__ w1, const char* __restrict__ stat,
                                                            const char* __restrict__ utp, char* __restrict__ gb) {
  const lptr sm = (lptr)smem_raw;
  ST_INIT();
  const int b = blockIdx.x, t = threadIdx.x, l = t & 63, w = __builtin_amdgcn_readfirstlane(t >> 6);
  const int xcd = b & 7, j = b >> 3, tb = (xcd >> 2) * 8 + (j & 7), nt = (xcd & 3) * 4 + (j >> 3);
  constexpr float NL2E = -1.4426950408889634f;
  if (__builtin_expect(w >= 4, LFIRST)) { const int lw = w - 4;
    const char* ap = yb + (size_t)(tb * 4 + lw) * 64 * 1024; const char* wp = w1 + nt * 448;
    unsigned vb[7];
#pragma unroll
    for (int i = 0; i < 7; ++i) { const int c = (lw * 7 + i) * 64 + l, r = c / 28; vb[i] = r * (FF * 2) + ((c - r * 28) ^ ((r >> 1) & 2)) * 16 + 3072 - i * 1024; }
    const unsigned va = l * 16;
    u32x4 st[8], e;
    loader<16, 3, 3, 11, K4_STAGE, 8, 1, 1, 2>([&](int tt, int slot) {
      SETM0(slot + lw * 4096); const char* a = ap + (size_t)tt * 4096;
      glds<0>(va, a); glds<1024>(va, a); glds<2048>(va, a); glds<3072>(va, a);
      SETM0(slot + 16384 + lw * 7168 + 3072); const char* s = wp + (size_t)tt * (64 * FF * 2);
      glds<-3072>(vb[0], s); glds<-2048>(vb[1], s); glds<-1024>(vb[2], s); glds<0>(vb[3], s); glds<1024>(vb[4], s); glds<2048>(vb[5], s); glds<3072>(vb[6], s); },
      [&](auto i) { st[i] = gload<0>(va, stat + (size_t)(tb * 4 + lw) * 8192 + i * 1024); },
      [&](auto) { SETM0(K4_UT + (lw & 1) * 1024); glds<0>(l < 56 ? va : 0, utp + ((lw & 1) * FF + nt * 224) * 4);
        CODE_RANGE(k_ffn2, k_mod, cb, cs) const unsigned o = (j & 3) * 4096 + lw * 1024 + l * 16; e = gload<0>(o < cs ? o : 0, cb); });
#pragma unroll
    for (int i = 0; i < 8; ++i) asm volatile("" : "+v"(st[i]));
    f32x4 o;
#pragma unroll
    for (int k = 0; k < 2; ++k) { float mean = 0.f, m2 = 0.f;
#pragma unroll
      for (int i = 0; i < 8; ++i) mean += __uint_as_float(st[i][2 * k]);
      mean = swap32add(swap16add(mean)) * (1.f / 32.f);
#pragma unroll
      for (int i = 0; i < 8; ++i) { const float d = __uint_as_float(st[i][2 * k]) - mean; m2 += __uint_as_float(st[i][2 * k + 1]) + 32.f * d * d; }
      const float rs = NL2E * __builtin_amdgcn_rsqf(swap32add(swap16add(m2)) * (1.f / 1024.f) + 1e-5f); o[2 * k] = rs; o[2 * k + 1] = -rs * mean; }
    if (l < 16) *(LDS f32x4*)(sm + K4_RS + (lw * 32 + 2 * l) * 8) = o;
    waitbar<1>();
    asm volatile("s_waitcnt vmcnt(0)" : "+v"(e)::"memory");
    return; }
  const int g = l >> 4, th = w & 1, nh = w >> 1, sw = (g & 1) * 32;
  const lptr ra = sm + th * 8192 + (g >> 1) * 1024 + (g & 1) * 512 + (l & 15) * 16;
  const lptr rb = sm + 16384 + (16 * (g >> 1) + 4 * (g & 1) + ((l >> 2) & 3)) * 448 + (l & 3) * 8;
  lptr base[4] = {ra, rb + nh * 256 + sw, rb + nh * 256 - sw, rb + (((6 + nh) * 32) ^ sw)};
  PIN(base[0]); PIN(base[1]); PIN(base[2]); PIN(base[3]);
  lptr p[4] = {base[0], base[1], base[2], base[3]};
  f32x4 acc[7][4];
  FragB f, h; int slot = 0; IT_INIT();
  auto rd = [&](FragB& d, int i, int ks) {
    if (i < 4) d.a[i] = ldsA(p[0] + (i >> 1) * 4096 + (i & 1) * 256 + ks * 2048);
    else { const int n = (i - 4) >> 1; d.b[i - 4] = ldsT1((n == 6 ? p[3] : p[1 + (n & 1)] + n * 32) + ks * (32 * 448) + (i & 1) * (8 * 448)); } };
  auto kstep = [&](FragB& cur, FragB& nxt, int ks, auto first) {
#pragma unroll
    for (int n = 0; n < 7; ++n)
#pragma unroll
      for (int i = 0; i < 4; ++i) { const int m = n * 4 + i;
        acc[n][i] = mfma16(cat(cur.b[2 * n], cur.b[2 * n + 1]), cur.a[i], first ? (f32x4){} : acc[n][i]); SB();
        if (m < 18) rd(nxt, m, ks); else if (ks == 1 && m < 22) { p[m - 18] = base[m - 18] + slot; PIN(p[m - 18]); } else if (m == W4POS) { ldswait(); IT_B(); } else if (m == 22) IT_A();
        SB(); } };
  bar(); ST(2);
#pragma unroll
  for (int i = 0; i < 18; ++i) rd(f, i, 0);
  slot = K4_STAGE; kstep(f, h, 1, std::true_type{}); sbar(); kstep(h, f, 0, std::false_type{});
  ST(4);
#pragma unroll U4
  for (int tt = 1; tt < 15; ++tt) { slot = slot == 2 * K4_STAGE ? 0 : slot + K4_STAGE; kstep(f, h, 1, std::false_type{}); sbar(); kstep(h, f, 0, std::false_type{});
  }
  ST(7);
  kstep(f, h, 1, std::false_type{});
#pragma unroll
  for (int n = 0; n < 7; ++n)
#pragma unroll
    for (int i = 0; i < 4; ++i) acc[n][i] = mfma16(cat(h.b[2 * n], h.b[2 * n + 1]), h.a[i], acc[n][i]);
  mfma_done(); bar();
  ST(3);
  f32x2 rn[4];
#pragma unroll
  for (int i = 0; i < 4; ++i) rn[i] = *(const LDS f32x2*)(sm + K4_RS + (th * 64 + i * 16 + (l & 15)) * 8);
  struct UT { f32x4 u, t; };
  auto ut = [&](int n) { const int col = ((n < 6 ? 8 * nh + n : 6 + nh) * 16 + 4 * g) * 4; return UT{*(const LDS f32x4*)(sm + K4_UT + col), *(const LDS f32x4*)(sm + K4_UT + 1024 + col)}; };
  auto act = [&](int n, int i, const UT& c) { u32x2 o;
#pragma unroll
    for (int e = 0; e < 2; ++e) { const f32x2 z = rn[i][0] * (f32x2){acc[n][i][2 * e], acc[n][i][2 * e + 1]} + (rn[i][1] * (f32x2){c.u[2 * e], c.u[2 * e + 1]} + (f32x2){c.t[2 * e], c.t[2 * e + 1]});
      const f32x2 dn = NL2E * (f32x2){EX2(z[0]), EX2(z[1])} + NL2E;
      const f32x2 r = z * (f32x2){__builtin_amdgcn_rcpf(dn[0]), __builtin_amdgcn_rcpf(dn[1])}; o[e] = pk(r[0], r[1]); }
    return o; };
  auto dst = [&](int gi, int i) { const int kk = nt * 7 + gi; return gb + (size_t)(((tb * 56 + (kk >> 1)) * 8 + th * 4 + i) * 2 + (kk & 1)) * 1024; };
  const unsigned vo = l * 16;
  UT c[7];
#pragma unroll
  for (int n = 0; n < 2; ++n) c[n] = ut(n);
#pragma unroll
  for (int p = 0; p < 3; ++p) { c[2 * p + 2] = ut(2 * p + 2); if (p < 2) c[2 * p + 3] = ut(2 * p + 3);
#pragma unroll
    for (int i = 0; i < 4; ++i) { const u32x2 lo = act(2 * p, i, c[2 * p]), hi = act(2 * p + 1, i, c[2 * p + 1]); gstore<0>(vo, dst(4 * nh + p, i), (u32x4){lo[0], lo[1], hi[0], hi[1]}); } }
#pragma unroll
  for (int i = 0; i < 4; ++i) gstore2<0>(vo, dst(3, i) + nh * 8, act(6, i, c[6]));
  IT_END(0); ST_END(4);
}

constexpr int K5_STAGE = 32768, K5_S = 5, K5_X = 3 * K5_STAGE, K5_LDS = K5_S * K5_STAGE;
struct FragC { bf16x8 a[4]; s16x4 b[8]; };
extern "C" __global__ __launch_bounds__(512, 2) void k_ffn2(const char* __restrict__ gb, const char* __restrict__ w2, const char* __restrict__ x1b,
                                                            const float* __restrict__ modf, char* __restrict__ out, char* xch) {
  const lptr sm = (lptr)smem_raw;
  ST_INIT();
  const int b = blockIdx.x, l = threadIdx.x & 63, w = __builtin_amdgcn_readfirstlane(threadIdx.x >> 6);
  const int kh = b & 1, j = b >> 3, tb = ((b & 7) >> 1) * 4 + (j & 3), nt = j >> 2;
  if (__builtin_expect(w >= 4, LFIRST)) { const int lw = w - 4;
    const char* ap = gb + (size_t)(tb * 56 + kh * 28) * 16384 + lw * 4096; const char* wp = w2 + (size_t)(nt * 56 + kh * 28) * 16384 + lw * 4096;
    const unsigned va = l * 16;
    loader<28, K5_S, D5, 8, K5_STAGE, 0, 0, 1, 5>([&](int t, int slot) {
      SETM0(slot + lw * 4096); const char* a = ap + (size_t)t * 16384; glds<0>(va, a); glds<1024>(va, a); glds<2048>(va, a); glds<3072>(va, a);
      SETM0(slot + 16384 + lw * 4096); const char* s = wp + (size_t)t * 16384;
      glds<0>(va, s); glds<1024>(va, s); glds<2048>(va, s); glds<3072>(va, s); }, [](auto) {},
      [&](auto) {
        const char* s = x1b + (size_t)((tb * 4 + (lw & 1) * 2 + kh) * 64 + nt * 8 + (lw >> 1) * 4) * 1024;
        SETM0(K5_X + lw * 4096); glds<0>(va, s); glds<1024>(va, s); glds<2048>(va, s); glds<3072>(va, s);
        SETM0(K5_X + 16384 + lw * 1024); glds<0>((l & 15) * 16, (const char*)(modf + 5 * DM + nt * 128 + (lw >> 1) * 64)); });
    waitbar<0>();
    return; }
  const int g = l >> 4, th = w & 1, nh = w >> 1, rx = 4 * (g & 1) + ((l >> 2) & 3);
  const int tok0 = tb * 128 + th * 64 + kh * 32, n0 = nt * 128 + nh * 64;
  u32x2 xr[2][4]; f32x4 gv[4];
  lptr base[6], p[6];
  base[4] = sm + th * 8192 + kh * 4096 + l * 16; base[5] = sm + th * 8192 + (kh ^ 1) * 4096 + l * 16;
#pragma unroll
  for (int n = 0; n < 4; ++n) base[n] = sm + 16384 + (4 * g + ((l >> 2) & 3)) * 256 + ((4 * nh + n) ^ rx) * 32 + (l & 3) * 8;
#pragma unroll
  for (int n = 0; n < 6; ++n) { PIN(base[n]); p[n] = base[n]; }
  f32x4 acc[4][4];
  FragC f, h; int slot = 0; IT_INIT();
  auto rd = [&](FragC& d, int i, int ks) {
    if (i < 4) d.a[i] = ldsA(p[4 + (i >> 1)] + (i & 1) * 2048 + ks * 1024); else d.b[i - 4] = ldsT1(p[(i - 4) >> 1] + ks * 8192 + (i & 1) * 4096); };
  auto kstep = [&](FragC& cur, FragC& nxt, int ks, auto first) {
#pragma unroll
    for (int n = 0; n < 4; ++n)
#pragma unroll
      for (int i = 0; i < 4; ++i) { const int m = n * 4 + i;
        acc[n][i] = mfma16(cat(cur.b[2 * n], cur.b[2 * n + 1]), cur.a[i], first ? (f32x4){} : acc[n][i]); SB();
        if (m < 12) rd(nxt, m, ks);
        else if (ks == 1) { p[m - 12] = base[m - 12] + slot; PIN(p[m - 12]); if (m < 14) { p[m - 8] = base[m - 8] + slot; PIN(p[m - 8]); } }
        if (W5V == 0 || ks == 1) { if (m == 15) { ldswait(); IT_B(); } else if (m == 12) IT_A(); } else if (m == 15) ldsw<4>();
        if (W5V == 1 && ks == 1 && m == 7) ldsw<8>();
        SB(); } };
  bar(); ST(2);
#pragma unroll
  for (int i = 0; i < 12; ++i) rd(f, i, 0);
  slot = K5_STAGE; kstep(f, h, 1, std::true_type{}); sbar(); kstep(h, f, 0, std::false_type{});
  ST(1);
#pragma unroll U5
  for (int tt = 1; tt < 25; ++tt) { slot = slot == (K5_S - 1) * K5_STAGE ? 0 : slot + K5_STAGE; kstep(f, h, 1, std::false_type{}); sbar(); kstep(h, f, 0, std::false_type{});
  }
  ST(7);
  auto hstep = [&](FragC& cur, FragC& nxt, int half, int nhalf, int ns) {
    const int off = (ns >> 1) * K5_STAGE + (ns & 1) * 1024, offw = (ns >> 1) * K5_STAGE + (ns & 1) * 8192;
#pragma unroll
    for (int m = 0; m < 8; ++m) { const int n = m >> 1, i = 2 * half + (m & 1);
      acc[n][i] = mfma16(cat(cur.b[2 * n], cur.b[2 * n + 1]), cur.a[i], acc[n][i]); SB();
      if (ns >= 0) { if (m < 2) nxt.a[2 * nhalf + m] = ldsA(base[4 + nhalf] + off + m * 2048); nxt.b[m] = ldsT1(base[m >> 1] + offw + (m & 1) * 4096); }
      SB(); } };
  hstep(f, h, 1, 1, 1); bar(); hstep(h, f, 1, 1, 2); hstep(f, h, 1, 1, 3); bar(); hstep(h, f, 1, 1, 4); hstep(f, h, 1, 1, 5); hstep(h, f, 1, 0, 0);
  ST(3);
  hstep(f, h, 0, 0, 1);
  { char* d = xch + (size_t)(b * 4 + w) * 4096;
#pragma unroll
    for (int i = 0; i < 2; ++i)
#pragma unroll
      for (int q = 0; q < 2; ++q) { const f32x4 u = acc[2 * q][2 + i], v = acc[2 * q + 1][2 + i];
        gstore<0>(l * 16, d + (i * 2 + q) * 1024, (u32x4){min(pk(u[0], u[1]), ~1u), min(pk(u[2], u[3]), ~1u), min(pk(v[0], v[1]), ~1u), min(pk(v[2], v[3]), ~1u)}); } }
  SB();
  hstep(h, f, 0, 0, 2); hstep(f, h, 0, 0, 3); hstep(h, f, 0, 0, 4); hstep(f, h, 0, 0, 5); hstep(h, f, 0, 0, -1);
  ST(4);
  sbar();
#pragma unroll
  for (int n = 0; n < 4; ++n) { gv[n] = *(const LDS f32x4*)(sm + K5_X + 16384 + w * 1024 + n * 64 + g * 16);
#pragma unroll
    for (int i = 0; i < 2; ++i) xr[i][n] = *(const LDS u32x2*)(sm + K5_X + w * 4096 + n * 1024 + (g & 1) * 512 + (i * 16 + (l & 15)) * 16 + (g >> 1) * 8); }
  u32x4 pr[4];
  { const char* s = xch + (size_t)((b ^ 1) * 4 + w) * 4096; const unsigned vo = l * 16;
    for (int spin = 0; spin < (1 << 16); ++spin) {
      pr[0] = gloadd<0>(vo, s); pr[1] = gloadd<1024>(vo, s); pr[2] = gloadd<2048>(vo, s); pr[3] = gloadd<3072>(vo, s);
      asm volatile("s_waitcnt vmcnt(0)" : "+v"(pr[0]), "+v"(pr[1]), "+v"(pr[2]), "+v"(pr[3])::"memory");
      unsigned m = 0;
#pragma unroll
      for (int i = 0; i < 4; ++i) m = max(max(max(m, pr[i][0]), max(pr[i][1], pr[i][2])), pr[i][3]);
      if (!__any(m == ~0u)) break;
    } }
  ST(6);
#pragma unroll
  for (int i = 0; i < 2; ++i)
#pragma unroll
    for (int p = 0; p < 2; ++p) { unsigned o[2][2];
#pragma unroll
      for (int q = 0; q < 2; ++q) { const int n = 2 * p + q; const unsigned u0 = pr[2 * i + p][2 * q], u1 = pr[2 * i + p][2 * q + 1];
        const f32x4 a = acc[n][i] + (f32x4){blo(u0), bhi(u0), blo(u1), bhi(u1)};
        o[q][0] = pk(blo(xr[i][n][0]) + gv[n][0] * a[0], bhi(xr[i][n][0]) + gv[n][1] * a[1]); o[q][1] = pk(blo(xr[i][n][1]) + gv[n][2] * a[2], bhi(xr[i][n][1]) + gv[n][3] * a[3]); }
      auto r0 = __builtin_amdgcn_permlane16_swap(o[0][0], o[1][0], false, false); auto r1 = __builtin_amdgcn_permlane16_swap(o[0][1], o[1][1], false, false);
      gstore<0>((l & 15) * 2048 + (g & 1) * 32 + (g >> 1) * 16, out + (size_t)(tok0 + i * 16) * 2048 + (n0 + 32 * p) * 2, (u32x4){r0[0], r1[0], r0[1], r1[1]}); }
  IT_END(1); ST_END(5);
}

extern "C" __global__ __launch_bounds__(512) void k_mod(const uint16_t* __restrict__ c, const char* __restrict__ wmod, float* __restrict__ modp,
                                             const char* __restrict__ x, const char* __restrict__ wq, const char* __restrict__ wk, const char* __restrict__ wv, char* __restrict__ wqp) {
  __shared__ __attribute__((aligned(16))) float red[8][64];
  ST_INIT();
  const int t = threadIdx.x;
  if (blockIdx.x >= PF0 && blockIdx.x < PF0 + 128) { const int p = blockIdx.x - PF0; u32x4 d[7];
    const char* xb = x + (size_t)((p & 7) * 16 + (p >> 3)) * 32768;
    { const char* s = wq + (size_t)(p & 7) * 262144 + (p >> 3) * 128; const unsigned v = imgA(t & 63) + (t >> 6) * 32768; d[4] = gload<0>(v, s); d[5] = gload<0>(v, s + 16384); }
#pragma unroll
    for (int i = 0; i < 4; ++i) d[i] = gload<0>(t * 16, xb + i * 8192);
    d[6] = gload<0>((t & 63) * 16, (p & 64 ? wv : wk) + (p & 63) * 1024);
    { CODE_RANGE(k_ln1kv, k_mod, cb, cs) const unsigned o = (p >> 3) * 8192 + t * 16; u32x4 e = gload<0>(o < cs ? o : 0, cb);
      asm volatile("s_waitcnt vmcnt(6)" : "+v"(d[4]), "+v"(d[5])::"memory");
      const unsigned v = (t & 63) * 16 + (t >> 6) * 2048; char* o2 = wqp + ((p >> 3) * 8 + (p & 7)) * 16384; gstore<0>(v, o2, d[4]); gstore<1024>(v, o2, d[5]);
      asm volatile("s_waitcnt vmcnt(0)" : "+v"(e)::"memory"); }
    asm volatile("s_waitcnt vmcnt(0)" : "+v"(d[0]), "+v"(d[1]), "+v"(d[2]), "+v"(d[3]), "+v"(d[4]), "+v"(d[5]), "+v"(d[6])::"memory");
    return; }
  const int b = blockIdx.x < PF0 ? blockIdx.x : blockIdx.x - 128, cg = b % 96, ks = b / 96;
  const int row0 = ks * 256 + (t >> 3) * 4;
  const char* p = wmod + (size_t)row0 * (NMOD * 2) + cg * 128 + (t & 7) * 16;
  u32x4 w[4]; const u32x2 cv = *(const u32x2*)(c + row0);
#pragma unroll
  for (int i = 0; i < 4; ++i) w[i] = *(const u32x4*)(p + (size_t)i * NMOD * 2);
  float a[8] = {0, 0, 0, 0, 0, 0, 0, 0};
#pragma unroll
  for (int i = 0; i < 4; ++i) { const float f = rbf(silu(i & 1 ? bhi(cv[i >> 1]) : blo(cv[i >> 1])));
#pragma unroll
    for (int e = 0; e < 4; ++e) { a[2 * e] += f * blo(w[i][e]); a[2 * e + 1] += f * bhi(w[i][e]); } }
#pragma unroll
  for (int e = 0; e < 8; ++e) a[e] = swap32add(swap16add(dppadd<0x128>(a[e])));
  if ((t & 63) < 8) { *(f32x4*)&red[t >> 6][(t & 7) * 8] = (f32x4){a[0], a[1], a[2], a[3]}; *(f32x4*)&red[t >> 6][(t & 7) * 8 + 4] = (f32x4){a[4], a[5], a[6], a[7]}; }
  __syncthreads();
  if (t < 64) modp[ks * NMOD + cg * 64 + t] = ((red[0][t] + red[1][t]) + (red[2][t] + red[3][t])) + ((red[4][t] + red[5][t]) + (red[6][t] + red[7][t]));
  ST_END(0);
}

extern "C" void kernel_launch(void* const* d_in, const int* in_sizes, int n_in, void* d_out, int out_size, void* d_ws, size_t ws_size, hipStream_t stream) {
  const char* x = (const char*)d_in[0]; const char* wq = (const char*)d_in[1]; const char* wk = (const char*)d_in[2]; const char* wv = (const char*)d_in[3];
  const char* wo = (const char*)d_in[4]; const char* w1 = (const char*)d_in[6]; const char* w2 = (const char*)d_in[7];
  const uint16_t* c = (const uint16_t*)d_in[8]; const char* wmod = (const char*)d_in[9];
  char* ws = (char*)d_ws;
  (void)hipFuncSetAttribute((const void*)k_ln1kv, hipFuncAttributeMaxDynamicSharedMemorySize, K1_LDS);
  (void)hipFuncSetAttribute((const void*)k_qattn, hipFuncAttributeMaxDynamicSharedMemorySize, K2_LDS);
  (void)hipFuncSetAttribute((const void*)k_oproj, hipFuncAttributeMaxDynamicSharedMemorySize, K3_LDS);
  (void)hipFuncSetAttribute((const void*)k_ffn1, hipFuncAttributeMaxDynamicSharedMemorySize, K4_LDS);
  (void)hipFuncSetAttribute((const void*)k_ffn2, hipFuncAttributeMaxDynamicSharedMemorySize, K5_LDS);
  float* modp = (float*)(ws + WS_MODP); float* modf = (float*)(ws + WS_MODF);
  k_mod<<<512, 512, 0, stream>>>(c, wmod, modp, x, wq, wk, wv, ws + WS_WQP);
  k_ln1kv<<<256, 512, K1_LDS, stream>>>(x, wk, wv, (const char*)modp, modf, (uint16_t*)(ws + WS_MODH), ws + WS_H, ws + WS_KV);
  k_qattn<<<256, 512, K2_LDS, stream>>>(ws + WS_H, ws + WS_WQP, ws + WS_KV, ws + WS_O, w1, ws + WS_MODH, (float*)(ws + WS_UTP), wo, w2, ws + WS_XCH, ws + WS_WOP, ws + WS_W2P);
  k_oproj<<<256, 512, K3_LDS, stream>>>(ws + WS_O, ws + WS_WOP, x, modf, ws + WS_X1, ws + WS_Y, (f32x2*)(ws + WS_STAT), (const float*)(ws + WS_UTP), (float*)(ws + WS_UTF));
  k_ffn1<<<256, 512, K4_LDS, stream>>>(ws + WS_Y, w1, ws + WS_STAT, ws + WS_UTF, ws + WS_G);
  k_ffn2<<<256, 512, K5_LDS, stream>>>(ws + WS_G, ws + WS_W2P, ws + WS_X1, modf, (char*)d_out, ws + WS_XCH);
}
```

```cpp
#include <hip/hip_runtime.h>
#include <hip/hip_bf16.h>
#include <stdint.h>
#include <type_traits>

typedef __attribute__((ext_vector_type(2))) float f32x2;
typedef __attribute__((ext_vector_type(4))) float f32x4;
typedef __attribute__((ext_vector_type(16))) float f32x16;
typedef __attribute__((ext_vector_type(2))) __bf16 bf16x2;
typedef __attribute__((ext_vector_type(8))) __bf16 bf16x8;
typedef __attribute__((ext_vector_type(4))) short s16x4;
typedef __attribute__((ext_vector_type(2))) unsigned u32x2;
typedef __attribute__((ext_vector_type(4))) unsigned u32x4;
typedef __attribute__((ext_vector_type(8))) unsigned u32x8;
typedef __attribute__((ext_vector_type(16))) __bf16 bf16x16;
#define LDS __attribute__((address_space(3)))
typedef LDS char* lptr;
#define WPOS 1
#define W4POS 27
#define W5V 1
#define D2 4
#define D3 4
#define D5 4
#define U4 7
#define U5 6
#define WL 2
#define MPL 1
#define HP 0
#define SELFPF 1
#define TLBW 0
#define PF0 384
#define NEXTPF 1
#define LFIRST 1
#define DEV __device__ __forceinline__

constexpr int SEQ = 2048, DM = 1024, FF = 3584, NMOD = 6 * DM;
constexpr size_t WS_MODP = 0;
constexpr size_t WS_UTP = 98304;
constexpr size_t WS_MODF = 212992;
constexpr size_t WS_MODH = 237568;
constexpr size_t WS_STAT = 262144;
constexpr size_t WS_KV = 786432;
constexpr size_t WS_UTF = (size_t)48 << 20;
constexpr size_t WS_H = (size_t)1 << 20;
constexpr size_t WS_O = (size_t)5 << 20;
constexpr size_t WS_X1 = (size_t)9 << 20;
constexpr size_t WS_Y = (size_t)13 << 20;
constexpr size_t WS_G = (size_t)17 << 20;
constexpr size_t WS_WQP = (size_t)36 << 20;
constexpr size_t WS_WOP = (size_t)38 << 20;
constexpr size_t WS_W2P = (size_t)40 << 20;
constexpr size_t WS_XCH = (size_t)32 << 20;

DEV unsigned pk(float a, float b) { return __builtin_bit_cast(unsigned, __builtin_convertvector((f32x2){a, b}, bf16x2)); }
DEV float rbf(float a) { return __uint_as_float(pk(a, 0.f) << 16); }
DEV float blo(unsigned u) { return __uint_as_float(u << 16); }
DEV float bhi(unsigned u) { return __uint_as_float(u & 0xffff0000u); }
DEV float silu(float z) { return z * __builtin_amdgcn_rcpf(1.f + __builtin_amdgcn_exp2f(-1.4426950408889634f * z)); }
DEV f32x16 mfma32(bf16x8 a, bf16x8 b, f32x16 c) { return __builtin_amdgcn_mfma_f32_32x32x16_bf16(a, b, c, 0, 0, 0); }
DEV f32x4 mfma16(bf16x8 a, bf16x8 b, f32x4 c) { return __builtin_amdgcn_mfma_f32_16x16x32_bf16(a, b, c, 0, 0, 0); }
DEV bf16x8 asb(u32x4 v) { return __builtin_bit_cast(bf16x8, v); }
DEV bf16x8 ldsA(lptr p) { return *(const LDS bf16x8*)p; }
DEV bf16x8 ldsT(lptr p, int second) {
  s16x4 a = __builtin_amdgcn_ds_read_tr16_b64_v4i16((LDS s16x4*)p);
  s16x4 b = __builtin_amdgcn_ds_read_tr16_b64_v4i16((LDS s16x4*)(p + second));
  return __builtin_bit_cast(bf16x8, __builtin_shufflevector(a, b, 0, 1, 2, 3, 4, 5, 6, 7));
}
DEV s16x4 ldsT1(lptr p) { return __builtin_amdgcn_ds_read_tr16_b64_v4i16((LDS s16x4*)p); }
DEV bf16x8 cat(s16x4 a, s16x4 b) { return __builtin_bit_cast(bf16x8, __builtin_shufflevector(a, b, 0, 1, 2, 3, 4, 5, 6, 7)); }
template <int C> DEV float dppadd(float v) { return v + __int_as_float(__builtin_amdgcn_update_dpp(0, __float_as_int(v), C, 0xf, 0xf, true)); }
DEV float swap32add(float v) { auto r = __builtin_amdgcn_permlane32_swap(__float_as_uint(v), __float_as_uint(v), false, false); return __uint_as_float(r[0]) + __uint_as_float(r[1]); }
DEV float swap16add(float v) { auto r = __builtin_amdgcn_permlane16_swap(__float_as_uint(v), __float_as_uint(v), false, false); return __uint_as_float(r[0]) + __uint_as_float(r[1]); }
DEV float swap32max(float v) { auto r = __builtin_amdgcn_permlane32_swap(__float_as_uint(v), __float_as_uint(v), false, false); return __builtin_fmaxf(__uint_as_float(r[0]), __uint_as_float(r[1])); }
DEV float wavesum(float v) { v = dppadd<0xB1>(v); v = dppadd<0x4E>(v); v = dppadd<0x141>(v); v = dppadd<0x140>(v); v = swap16add(v); return swap32add(v); }

#define SETM0(x) asm volatile("s_mov_b32 m0, %0\n\ts_nop 0" ::"s"(x) : "memory")
template <int OFF> DEV void glds(unsigned voff, const void* base) { asm volatile("global_load_lds_dwordx4 %0, %1 offset:%2" ::"v"(voff), "s"(base), "i"(OFF) : "memory"); }
template <int N> DEV void waitbar() { asm volatile("s_waitcnt vmcnt(%0) lgkmcnt(0)\n\ts_barrier" ::"i"(N) : "memory"); }
template <int OFF> DEV u32x4 gload(unsigned voff, const void* base) { u32x4 r; asm volatile("global_load_dwordx4 %0, %1, %2 offset:%3" : "=v"(r) : "v"(voff), "s"(base), "i"(OFF) : "memory"); return r; }
template <int OFF> DEV u32x2 gload2(unsigned voff, const void* base) { u32x2 r; asm volatile("global_load_dwordx2 %0, %1, %2 offset:%3" : "=v"(r) : "v"(voff), "s"(base), "i"(OFF) : "memory"); return r; }
template <int OFF> DEV unsigned gload1(unsigned voff, const void* base) { unsigned r; asm volatile("global_load_dword %0, %1, %2 offset:%3" : "=v"(r) : "v"(voff), "s"(base), "i"(OFF) : "memory"); return r; }
template <int OFF> DEV u32x4 gloadd(unsigned voff, const void* base) { u32x4 r; asm volatile("global_load_dwordx4 %0, %1, %2 offset:%3 sc1" : "=v"(r) : "v"(voff), "s"(base), "i"(OFF) : "memory"); return r; }
template <int OFF> DEV void gstore2(unsigned voff, void* base, u32x2 v) { asm volatile("global_store_dwordx2 %0, %1, %2 offset:%3 sc1" ::"v"(voff), "v"(v), "s"(base), "i"(OFF) : "memory"); }
template <int OFF> DEV void gstore(unsigned voff, void* base, u32x4 v) { asm volatile("global_store_dwordx4 %0, %1, %2 offset:%3 sc1\n\ts_nop 1" ::"v"(voff), "v"(v), "s"(base), "i"(OFF) : "memory"); }
#define CODE_RANGE(A, B, base, size) const char* base; unsigned size; { unsigned long long pc_; int o_; \
  asm volatile("s_getpc_b64 %0\n.Lcr%=:\n\ts_mov_b32 %1, " #A "-.Lcr%=\n\ts_mov_b32 %2, " #B "-" #A : "=s"(pc_), "=s"(o_), "=s"(size)); base = (const char*)(pc_ + o_); }
#define SB() __builtin_amdgcn_sched_barrier(0)
#define PIN(x) asm volatile("" : "+v"(x))
extern __shared__ __attribute__((aligned(16))) char smem_raw[];

struct Stamps {};
#define ST_INIT() Stamps S_
#define ST(i)
#define IT_INIT()
#define IT_A()
#define IT_B()
#define IT_END(k)
#define ST_END(k)

constexpr int K1_W = 0, K1_H = 65536, K1_MV = K1_H + 64 * 528, K1_RED = K1_MV + 8192, K1_LDS = K1_RED + 32768;
extern "C" __global__ __launch_bounds__(512, 2) void k_ln1kv(const char* __restrict__ x, const char* __restrict__ wk, const char* __restrict__ wv,
                                                  const char* __restrict__ modp, float* __restrict__ modf, uint16_t* __restrict__ modh,
                                                  char* __restrict__ hbuf, char* __restrict__ kv) {
  const lptr sm = (lptr)smem_raw;
  ST_INIT();
  const int b = blockIdx.x, t = threadIdx.x, l = t & 63, w = __builtin_amdgcn_readfirstlane(t >> 6);
  const int which = (b >> 3) & 1, rb = (b & 7) * 16 + (b >> 4);
  u32x4 xv[4], mp[4];
  { const char* xb = x + (size_t)(rb * 16 + 2 * w) * 2048; const unsigned vo = l * 32;
    xv[0] = gload<0>(vo, xb); xv[1] = gload<16>(vo, xb); xv[2] = gload<2048>(vo, xb); xv[3] = gload<2064>(vo, xb);
    if (MPL) asm volatile("s_waitcnt vmcnt(0)" ::: "memory");
#pragma unroll
    for (int s = 0; s < 4; ++s) mp[s] = gload<0>(t * 16, modp + s * NMOD * 4); }
  asm volatile("s_waitcnt vmcnt(4)" : "+v"(xv[0]), "+v"(xv[1]), "+v"(xv[2]), "+v"(xv[3])::"memory");
  ST(1);
  const char* wsrc = (which ? wv : wk) + w * 8192;
  auto wload = [&](int g) { SETM0(K1_W + w * 8192 + g * 2048); glds<0>(l * 16, wsrc + g * 2048); glds<1024>(l * 16, wsrc + g * 2048); };
  float rs[2], nm[2];
  { float s[4] = {0.f, 0.f, 0.f, 0.f}; const bf16x2 one = __builtin_bit_cast(bf16x2, 0x3f803f80u);
#pragma unroll
    for (int e = 0; e < 8; ++e)
#pragma unroll
      for (int r = 0; r < 2; ++r) { const unsigned xx = xv[2 * r + (e >> 2)][e & 3]; const bf16x2 u = __builtin_bit_cast(bf16x2, xx);
        s[r] = __builtin_amdgcn_fdot2_f32_bf16(u, one, s[r], false); s[2 + r] = __builtin_amdgcn_fdot2_f32_bf16(u, u, s[2 + r], false); }
#pragma unroll
    for (int i = 0; i < 4; ++i) s[i] = wavesum(s[i]) * (1.f / 1024.f);
#pragma unroll
    for (int r = 0; r < 2; ++r) { rs[r] = __builtin_amdgcn_rsqf(__builtin_fmaxf(s[2 + r] - s[r] * s[r], 0.f) + 1e-5f); nm[r] = -s[r] * rs[r]; } }
  PIN(rs[0]); PIN(rs[1]);
  asm volatile("s_waitcnt vmcnt(0)" : "+v"(mp[0]), "+v"(mp[1]), "+v"(mp[2]), "+v"(mp[3])::"memory");
  { f32x4 o;
#pragma unroll
    for (int e = 0; e < 4; ++e) { const float v = rbf((__uint_as_float(mp[0][e]) + __uint_as_float(mp[1][e])) + (__uint_as_float(mp[2][e]) + __uint_as_float(mp[3][e])));
      o[e] = rbf(v + (w >= 4 ? 1.f : 0.f)); }
    *(LDS f32x4*)(sm + K1_MV + (t & 256) * 16 + (t & 3) * 1024 + ((t & 255) >> 2) * 16) = o; }
  if (WL == 1) { wload(0); } if (WL == 2) { wload(0); wload(1); }
  ST(4);
  asm volatile("s_waitcnt lgkmcnt(0)\n\ts_barrier" ::: "memory");
  ST(5);
  { unsigned u[2][8];
#pragma unroll
    for (int e = 0; e < 4; ++e) { const f32x4 a = *(const LDS f32x4*)(sm + K1_MV + e * 1024 + l * 16), cc = *(const LDS f32x4*)(sm + K1_MV + 4096 + e * 1024 + l * 16);
      if (e + WL < 4) { SB(); wload(e + WL); SB(); }
#pragma unroll
      for (int r = 0; r < 2; ++r)
#pragma unroll
        for (int i = 0; i < 2; ++i) { const unsigned xx = xv[2 * r + (e >> 1)][(2 * e + i) & 3];
          const f32x2 h = ((f32x2){blo(xx), bhi(xx)} * rs[r] + nm[r]) * (f32x2){cc[2 * i], cc[2 * i + 1]} + (f32x2){a[2 * i], a[2 * i + 1]}; u[r][2 * e + i] = pk(h[0], h[1]); } }
#pragma unroll
    for (int r = 0; r < 2; ++r) { const lptr d = sm + K1_H + l * 528 + (2 * w + r) * 16;
      *(LDS u32x4*)d = (u32x4){u[r][0], u[r][1], u[r][4], u[r][5]}; *(LDS u32x4*)(d + 256) = (u32x4){u[r][2], u[r][3], u[r][6], u[r][7]}; } }
  ST(2); waitbar<0>(); ST(3);
  auto hst = [&](int) {
#pragma unroll
    for (int i = 0; i < 2; ++i) { const int id = t + 512 * i, tok = id & 15, h2 = (id >> 4) & 1, kb = which * 32 + (id >> 5);
      gstore<0>(kb * 1024 + (h2 * 32 + tok) * 16, hbuf + (size_t)(rb >> 1) * 65536 + (rb & 1) * 256, *(const LDS u32x4*)(sm + K1_H + kb * 528 + h2 * 256 + tok * 16)); }
  };
  f32x16 acc = {};
  { const int hi = l >> 5; const lptr hp = sm + K1_H + w * 8 * 528 + hi * 256 + (l & 15) * 16;
    const lptr wp = sm + K1_W + (w * 128 + 4 * hi + ((l >> 2) & 3)) * 64 + ((l >> 4) & 1) * 32 + (l & 3) * 8;
    bf16x8 fa[8], fb[8];
#pragma unroll
    for (int i = 0; i < 8; ++i) { fa[i] = ldsA(hp + i * 528); fb[i] = ldsT(wp + i * 1024, 512); }
    if (HP == 0) hst(0);
    if (which == 0) {
#pragma unroll
      for (int i = 0; i < 8; ++i) acc = mfma32(fb[i], fa[i], acc);
    } else {
#pragma unroll
      for (int i = 0; i < 8; ++i) acc = mfma32(fa[i], fb[i], acc);
    } }
#pragma unroll
  for (int q = 0; q < 4; ++q) *(LDS f32x4*)(sm + K1_RED + w * 4096 + q * 1024 + l * 16) = (f32x4){acc[4 * q], acc[4 * q + 1], acc[4 * q + 2], acc[4 * q + 3]};
  if (HP == 1) hst(0);
  ST(6);
  asm volatile("s_waitcnt lgkmcnt(0)\n\ts_barrier" ::: "memory");
  ST(7);

  if (w == 0) {
    const lptr rp = sm + K1_RED + (which ? l * 16 : (l >> 5) * 2048 + (((l >> 4) & 1) * 32 + (l & 15)) * 16);
    f32x4 a = {}, c = {};
#pragma unroll
    for (int u = 0; u < 8; ++u) { a += *(const LDS f32x4*)(rp + u * 4096); c += *(const LDS f32x4*)(rp + u * 4096 + 1024); }
    char* base = kv + (rb >> 1) * 4096;
    char* d = which ? base + 2048 + (rb & 1) * 1024 + l * 16 : base + (l >> 5) * 1024 + (((l >> 4) & 1) * 32 + (rb & 1) * 16 + (l & 15)) * 16;
    *(u32x4*)d = (u32x4){pk(a[0], a[1]), pk(a[2], a[3]), pk(c[0], c[1]), pk(c[2], c[3])};
  } else if (b < 64 && w == 1) {
    const int col = 2048 + b * 64 + l; const float* mpf = (const float*)modp;
    float v2 = rbf((mpf[col] + mpf[NMOD + col]) + (mpf[2 * NMOD + col] + mpf[3 * NMOD + col]));
    if ((col >> 10) == 4) v2 = rbf(1.f + v2);
    modf[col] = v2; modh[col] = (uint16_t)(__float_as_uint(v2) >> 16);
  }
  ST_END(1);
}

DEV void mfma_done() { SB(); asm volatile("s_nop 7\n\ts_nop 7\n\ts_nop 7"); SB(); }
DEV void ldswait() { __builtin_amdgcn_s_waitcnt(0xC07F); }
template <int N> DEV void ldsw() { __builtin_amdgcn_s_waitcnt(0xC07F | (N << 8)); }
DEV void sbar() { asm volatile("s_barrier" ::: "memory"); }
DEV void bar() { asm volatile("" ::: "memory"); ldswait(); asm volatile("s_barrier" ::: "memory"); }
template <int N, int G, int K, int LN, int LP, class L> DEV void loader_tail(L&& late) {
  waitbar<G * N + LP * (K < LN ? K : LN)>(); if constexpr (K < LN) late(std::integral_constant<int, K>{});
  if constexpr (N > 0) loader_tail<N - 1, G, K + 1, LN, LP>(late); }
constexpr int younger(int t, int D, int EN) { const int a = t - D + 2 < 1 ? 1 : t - D + 2, b = t < EN ? t : EN; return b >= a ? b - a + 1 : 0; }
template <int T, int T0, int S, int D, int G, int STAGE, int EN, int EP, class I, class E> DEV void loader_ramp(I&& issue, E&& early) {
#pragma unroll
  for (int s = T == 0 ? 0 : D + T - 2; s < D + T - 1; ++s) issue(s, (s % S) * STAGE);
  if constexpr (T >= 1 && T <= EN) early(std::integral_constant<int, T - 1>{});
  waitbar<G*(D - 2) + EP * younger(T, D, EN)>();
  if constexpr (T < T0) loader_ramp<T + 1, T0, S, D, G, STAGE, EN, EP>(issue, early);
}
template <int KT, int S, int D, int G, int STAGE, int EN, int EP, int LN, int LP, class I, class E, class L>
DEV void loader(I&& issue, E&& early, L&& late) {
  static_assert(D <= S && EN + D - 2 <= KT - D, "");
  loader_ramp<0, KT - D, S, D, G, STAGE, EN, EP>(issue, early);
  issue(KT - 1, ((KT - 1) % S) * STAGE);
  loader_tail<D - 2, G, 0, LN, LP>(late);
}
constexpr int GA_STAGE = 24576;
DEV unsigned imgA(int l) { return (l >> 3) * (DM * 2) + (((l & 7) ^ (((l >> 4) & 1) << 2)) * 16); }
template <int KT, int S, int D, int EN, int EP, int LN, int LP, class E, class L>
DEV void gemmA_load(const char* act, const char* wgt, int l, int lw, E&& early, L&& late) {
  const char* ap = act + (size_t)lw * (KT * 4096);
  const char* wp = wgt + lw * 2048;
  const unsigned va = l * 16;
  loader<KT, S, D, 6, GA_STAGE, EN, EP, LN, LP>([&](int t, int slot) {
    SETM0(slot + lw * 4096); const char* a = ap + (size_t)t * 4096; glds<0>(va, a); glds<1024>(va, a); glds<2048>(va, a); glds<3072>(va, a);
    SETM0(slot + 16384 + lw * 2048); const char* s = wp + t * 8192; glds<0>(va, s); glds<1024>(va, s); }, early, late);
}
struct FragA { bf16x8 a0[4], a1[4], b[4]; };
template <int KT, int S>
DEV void gemmA_compute(const lptr sm, int l, int w, f32x16& c0, f32x16& c1, Stamps& S_) {
  const int q = (l >> 2) & 3, p = l & 3;
  const lptr ra = sm + (w & 1) * 8192 + l * 16;
  const lptr rb = sm + 16384 + (4 * (l >> 5) + q) * 128 + ((((w >> 1) * 4 + ((l >> 4) & 1) * 2 + (p >> 1)) ^ ((q >> 1) << 2)) * 16) + (p & 1) * 8;
  c0 = (f32x16){}; c1 = (f32x16){};
  FragA f, g; int slot = 0; IT_INIT();
  auto rd = [&](FragA& d, int k) { d.a0[k] = ldsA(ra + slot + k * 1024); d.b[k] = ldsT(rb + slot + k * 2048, 1024); d.a1[k] = ldsA(ra + slot + 4096 + k * 1024); };
  auto step = [&](FragA& cur, FragA& nxt) { IT_A(); IT_B(); bar(); slot = slot == (S - 1) * GA_STAGE ? 0 : slot + GA_STAGE;
#pragma unroll
    for (int k = 0; k < 4; ++k) { if (k < 2) rd(nxt, 2 * k); c0 = mfma32(cur.b[k], cur.a0[k], c0); SB(); if (k < 2) rd(nxt, 2 * k + 1); c1 = mfma32(cur.b[k], cur.a1[k], c1); SB(); } };
  bar(); ST(2);
#pragma unroll
  for (int k = 0; k < 4; ++k) rd(f, k);
  for (int t = 0; t < KT - 2; t += 2) { step(f, g); step(g, f); }
  step(f, g);
#pragma unroll
  for (int k = 0; k < 4; ++k) { c0 = mfma32(g.b[k], g.a0[k], c0); c1 = mfma32(g.b[k], g.a1[k], c1); }
  mfma_done();
  ST(3); IT_END(S == 4 ? 2 : 3);
}

constexpr int K2_KV0 = 98304, K2_KV1 = 131072, K2_KV2 = 0, K2_UW = 32768, K2_Q = 65536, K2_RED = 73728, K2_DUMP = 81920, K2_LDS = 163840;
DEV void pack16(const f32x16& s, u32x4& a, u32x4& b) {
  a = (u32x4){pk(s[0], s[1]), pk(s[2], s[3]), pk(s[4], s[5]), pk(s[6], s[7])};
  b = (u32x4){pk(s[8], s[9]), pk(s[10], s[11]), pk(s[12], s[13]), pk(s[14], s[15])}; }
DEV float max16(const f32x16& s) { float m = s[0];
#pragma unroll
  for (int i = 1; i < 16; ++i) m = __builtin_fmaxf(m, s[i]);
  return swap32max(m); }
#define EX2(v) __builtin_amdgcn_exp2f(v)

extern "C" __global__ __launch_bounds__(512, 2) void k_qattn(const char* __restrict__ hbuf, const char* __restrict__ wq, const char* __restrict__ kv, char* __restrict__ obuf,
                                                  const char* __restrict__ w1, const char* __restrict__ modh, float* __restrict__ utp,
                                                  const char* __restrict__ wo, const char* __restrict__ w2, char* __restrict__ xch, char* __restrict__ wop, char* __restrict__ w2p, const char* __restrict__ outp) {
  const lptr sm = (lptr)smem_raw;
  ST_INIT();
  const int b = blockIdx.x, l = threadIdx.x & 63, w = __builtin_amdgcn_readfirstlane(threadIdx.x >> 6);
  const int xcd = b & 7, j = b >> 3, tb = (xcd >> 1) * 4 + (j & 3), hp = (xcd & 1) * 8 + (j >> 2);
  const unsigned l16 = l * 16;
  if (w >= 4) { const int lw = w - 4;
    auto kvload = [&](int st, int slot) {
#pragma unroll
      for (int g = 0; g < 2; ++g) { SETM0(slot + lw * 8192 + g * 4096); const char* s = kv + st * 32768 + lw * 8192 + g * 4096;
        glds<0>(l16, s); glds<1024>(l16, s); glds<2048>(l16, s); glds<3072>(l16, s); } };
    gemmA_load<16, 4, D2, 8, 1, 2, 4>(hbuf + (size_t)tb * 4 * 64 * 1024, wq + hp * 131072, l, lw,
      [&](auto i) { SETM0(K2_KV0 + lw * 8192 + i * 1024); glds<0>(l16, kv + lw * 8192 + i * 1024); },
      [&](auto g) { SETM0(K2_KV1 + lw * 8192 + g * 4096); const char* s = kv + 32768 + lw * 8192 + g * 4096; glds<0>(l16, s); glds<1024>(l16, s); glds<2048>(l16, s); glds<3072>(l16, s); });
    waitbar<8>();
    const int ub = b < 224 ? b : b - 224, cg = ub % 56, ks = ub / 56;
    u32x2 vf[8];
    waitbar<0>(); kvload(2, K2_KV2);
    { const unsigned vo = (l >> 3) * (FF * 2) + (((l & 7) ^ (((l >> 4) & 1) << 2)) * 16);
#pragma unroll
      for (int g = 0; g < 2; ++g) { SETM0(K2_UW + lw * 8192 + g * 4096); const char* s = w1 + (size_t)(ks * 256 + lw * 64 + g * 32) * (FF * 2) + cg * 128;
        glds<0>(vo, s); glds<1024>(vo, s + 8 * FF * 2 - 1024); glds<2048>(vo, s + 16 * FF * 2 - 2048); glds<3072>(vo, s + 24 * FF * 2 - 3072); }
      const char* mv = modh + (ks * 256 + lw * 64) * 2; const unsigned vm = ((4 - (l & 1)) * DM + 4 * (l >> 5)) * 2;
      vf[0] = gload2<0>(vm, mv); vf[1] = gload2<16>(vm, mv); vf[2] = gload2<32>(vm, mv); vf[3] = gload2<48>(vm, mv);
      vf[4] = gload2<64>(vm, mv); vf[5] = gload2<80>(vm, mv); vf[6] = gload2<96>(vm, mv); vf[7] = gload2<112>(vm, mv); }
    waitbar<16>(); kvload(3, K2_KV0);
    u32x4 rp[9];
    { const char* s = wo + (size_t)((b & 15) * 64 + lw * 16) * 2048 + (b >> 4) * 128; rp[7] = gload<0>(imgA(l), s); rp[8] = gload<0>(imgA(l), s + 16384);
      const int r = ((b & 31) * 4 + lw) * 7; const unsigned v0 = (l >> 4) * 2048 + ((l & 15) ^ ((l >> 4) * 2)) * 16;
#pragma unroll
      for (int i = 0; i < 7; ++i) rp[i] = gload<0>(v0 ^ (((r + i) & 1) * 128), w2 + (size_t)(r + i) * 8192 + (b >> 5) * 256); }
    waitbar<9>(); kvload(4, K2_KV1);
    { const int q = (l >> 2) & 3, p = l & 3; f32x16 d0 = {}, d1 = {};
      const lptr rb = sm + K2_UW + (lw * 64 + 4 * (l >> 5) + q) * 128 + (((((l >> 4) & 1) * 2 + (p >> 1)) ^ ((q >> 1) << 2)) * 16) + (p & 1) * 8;
      const lptr rc = sm + K2_UW + (lw * 64 + 4 * (l >> 5) + q) * 128 + (((4 + ((l >> 4) & 1) * 2 + (p >> 1)) ^ ((q >> 1) << 2)) * 16) + (p & 1) * 8;
#pragma unroll
      for (int kk = 0; kk < 4; ++kk) { PIN(vf[2 * kk]); PIN(vf[2 * kk + 1]); const bf16x8 vv = asb((u32x4){vf[2 * kk][0], vf[2 * kk][1], vf[2 * kk + 1][0], vf[2 * kk + 1][1]});
        d0 = mfma32(ldsT(rb + kk * 2048, 1024), vv, d0); d1 = mfma32(ldsT(rc + kk * 2048, 1024), vv, d1); }
      if ((l & 31) < 2) { const lptr d = sm + K2_RED + lw * 512 + (l & 31) * 256 + (l >> 5) * 16;
#pragma unroll
        for (int i = 0; i < 4; ++i) { *(LDS f32x4*)(d + i * 32) = (f32x4){d0[4 * i], d0[4 * i + 1], d0[4 * i + 2], d0[4 * i + 3]};
          *(LDS f32x4*)(d + 128 + i * 32) = (f32x4){d1[4 * i], d1[4 * i + 1], d1[4 * i + 2], d1[4 * i + 3]}; } } }
    waitbar<0>(); kvload(5, K2_KV2);
    if (b < 224 && lw < 2) { float v = 0.f;
#pragma unroll
      for (int u = 0; u < 4; ++u) v += *(const LDS float*)(sm + K2_RED + u * 512 + lw * 256 + l * 4);
      utp[(ks * 2 + lw) * FF + cg * 64 + l] = v; }
    waitbar<0>(); kvload(6, K2_KV0);
    { char* d = xch + b * 16384 + lw * 4096; const u32x4 e = {~0u, ~0u, ~0u, ~0u}; gstore<0>(l16, d, e); gstore<1024>(l16, d, e); gstore<2048>(l16, d, e); gstore<3072>(l16, d, e);
#pragma unroll
      for (int i = 0; i < 9; ++i) PIN(rp[i]);
      char* o = wop + b * 8192 + lw * 2048; gstore<0>(l16, o, rp[7]); gstore<1024>(l16, o, rp[8]);
#pragma unroll
      for (int i = 0; i < 7; ++i) gstore<0>(l16, w2p + (size_t)(b * 4 + lw) * 7168 + i * 1024, rp[i]); }
    waitbar<13>(); kvload(7, K2_KV1);
    { CODE_RANGE(k_oproj, k_mod, cb, cs) const unsigned o = (j & 15) * 4096 + lw * 1024 + l16; SETM0(K2_DUMP + lw * 1024); glds<0>(o < cs ? o : 0, cb); }
    waitbar<1>();
    asm volatile("s_waitcnt vmcnt(0)" ::: "memory");
    return; }
  bf16x8 qA0, qA1, qB0, qB1;
  { f32x16 c0, c1; gemmA_compute<16, 4>(sm, l, w, c0, c1, S_);
    constexpr float QS = 0.17677669529663687f * 1.4426950408889634f;
    c0 *= QS; c1 *= QS;
    u32x4 a, c; pack16(c0, a, c); qA0 = asb(a); qA1 = asb(c); pack16(c1, a, c); qB0 = asb(a); qB1 = asb(c); }
  bar();
  const unsigned onea = (l & 6) == 0 ? 0x3f803f80u : 0u, oneb = (l & 6) == 4 ? 0x3f803f80u : 0u;
  const bf16x8 ones = asb((u32x4){onea, onea, oneb, oneb}); const int sel = (l & 1) ? 0xEEEEEEEE : 0x44444444;
  auto rowsum = [&](u32x8 p, f32x4 a) { return __builtin_amdgcn_smfmac_f32_16x16x64_bf16(ones, __builtin_bit_cast(bf16x16, p), a, sel, 0, 0); };
  auto lo = [](u32x8 p) { return asb(__builtin_shufflevector(p, p, 0, 1, 2, 3)); };
  auto hi = [](u32x8 p) { return asb(__builtin_shufflevector(p, p, 4, 5, 6, 7)); };
  const lptr kp0 = sm + K2_KV0 + l16, kp1 = sm + K2_KV1 + l16, kp2 = sm + K2_KV2 + l16;
  bf16x8 k0a = ldsA(kp0), k1a = ldsA(kp0 + 1024), v0a = ldsA(kp0 + 2048), v1a = ldsA(kp0 + 3072), k0b = ldsA(kp0 + 4096), k1b = ldsA(kp0 + 5120), v0b, v1b = k0a;
  f32x16 nA, nB, sA, sB, oA = {}, oB = {}; f32x4 lA = {}, lB = {};
  { f32x16 z = {}, y = {}; z = mfma32(k0a, qA0, z); y = mfma32(k0a, qB0, y); z = mfma32(k1a, qA1, z); y = mfma32(k1a, qB1, y); const float m = -max16(z), n = -max16(y);
#pragma unroll
    for (int i = 0; i < 16; ++i) { nA[i] = m; nB[i] = n; } }
  PIN(nA); PIN(nB);
  sA = mfma32(k0a, qA0, nA); sA = mfma32(k1a, qA1, sA);
  u32x8 pA = {}, pB = {};
#define HALF(S, N, Q0, Q1, NEG, OS, ON, LN, PS, PN, K0, V1, K1, V0, W, R0, R1)                                                   \
  { S[0] = EX2(S[0]); S[1] = EX2(S[1]); PIN(S); SB();                                                                 \
    if (WPOS == 3) { W; } SB(); N = mfma32(K0, Q0, NEG); SB(); R0; SB();                                                                          \
    S[2] = EX2(S[2]); S[3] = EX2(S[3]); PS[0] = pk(S[0], S[1]); PS[1] = pk(S[2], S[3]); PIN(PS); SB();                \
    ON = mfma32(V1, hi(PN), ON); SB(); R1; SB();                                                                      \
    S[4] = EX2(S[4]); S[5] = EX2(S[5]); S[6] = EX2(S[6]); S[7] = EX2(S[7]); PS[2] = pk(S[4], S[5]); PS[3] = pk(S[6], S[7]); PIN(PS); SB(); \
    if (WPOS == 4) { W; } SB(); LN = rowsum(PN, LN); SB();                                                                                        \
    S[8] = EX2(S[8]); PIN(S); SB();                                                                                   \
    if (WPOS == 2) { W; } SB(); N = mfma32(K1, Q1, N); SB(); if (WPOS == 0) { W; } SB();                                                                             \
    S[9] = EX2(S[9]); S[10] = EX2(S[10]); S[11] = EX2(S[11]); PS[4] = pk(S[8], S[9]); PS[5] = pk(S[10], S[11]); PIN(PS); SB(); \
    S[12] = EX2(S[12]); S[13] = EX2(S[13]); PIN(S); SB();                                                             \
    if (WPOS == 1) { W; } SB(); OS = mfma32(V0, lo(PS), OS); SB();                                                                                \
    S[14] = EX2(S[14]); S[15] = EX2(S[15]); PS[6] = pk(S[12], S[13]); PS[7] = pk(S[14], S[15]); PIN(PS); SB(); }
#define TILE(X, Y, C, N)                                                                                               \
  HALF(sA, sB, qB0, qB1, nB, oA, oB, lB, pA, pB, k0##C, v1##N, k1##C, v0##C, __builtin_amdgcn_s_waitcnt(0xC07F), , )   \
  HALF(sB, sA, qA0, qA1, nA, oB, oA, lA, pB, pA, k0##N, v1##C, k1##N, v0##C, , (v0##N = ldsA((X) + 2048), v1##N = ldsA((X) + 3072)), (k0##C = ldsA(Y), k1##C = ldsA((Y) + 1024)))
#define TILE2(T, X) TILE(T + 4096, X, a, b) TILE(X, X + 4096, b, a)
#define STAGE(P, Q, BAR) TILE2(P, P + 2 * 4096) TILE2(P + 2 * 4096, P + 4 * 4096) TILE2(P + 4 * 4096, P + 6 * 4096) BAR TILE2(P + 6 * 4096, Q)
  ST(4);
  { lptr c = kp0, n = kp1, f = kp2;
    for (int st = 0; st < 7; ++st) { STAGE(c, n, bar();) const lptr t = c; c = n; n = f; f = t; }
    STAGE(c, n, ) }
  ST(5);
  oB = mfma32(v1b, hi(pB), oB); lB = rowsum(pB, lB);
  mfma_done();
  auto finish = [&](f32x16& o, float lsum, bf16x8 q0, bf16x8 q1, int i) {
    float chk = lsum;
#pragma unroll
    for (int e = 0; e < 16; ++e) chk = __builtin_fmaxf(chk, __builtin_fabsf(o[e]) * 0x1p-40f);
    bool bad = !(chk < 0x1p80f);
#pragma unroll
    for (int e = 0; e < 16; ++e) bad |= (o[e] != o[e]);
    bad |= (lsum != lsum);
    if (__any(bad)) {
      float m = -INFINITY; lsum = 0.f; o = (f32x16){};
      for (int t = 0; t < 64; ++t) { const bf16x8* g = (const bf16x8*)(kv + t * 4096) + l;
        f32x16 s = {}; s = mfma32(g[0], q0, s); s = mfma32(g[64], q1, s); mfma_done();
        const float mn = __builtin_fmaxf(m, max16(s)), al = EX2(m - mn); m = mn; float rsum = 0.f;
#pragma unroll
        for (int e = 0; e < 16; ++e) { s[e] = rbf(EX2(s[e] - mn)); rsum += s[e]; o[e] *= al; }
        lsum = lsum * al + swap32add(rsum);
        u32x4 a, c; pack16(s, a, c); o = mfma32(g[128], asb(a), o); o = mfma32(g[192], asb(c), o); mfma_done(); } }
    o *= 1.f / lsum;
    u32x4 o0, o1; pack16(o, o0, o1);
    char* d = obuf + (size_t)((tb * 4 + 2 * (w & 1) + i) * 64 + (hp * 2 + (w >> 1)) * 2) * 1024;
    gstore<0>(l16, d, o0); gstore<1024>(l16, d, o1); };
  finish(oA, lA[0] + lA[1], qA0, qA1, 0); finish(oB, lB[0] + lB[1], qB0, qB1, 1);
  ST_END(2);
}

constexpr int K3_S = 6, K3_X = K3_S * GA_STAGE, K3_MV = 4 * GA_STAGE, K3_LDS = K3_X + 16384;
extern "C" __global__ __launch_bounds__(512, 2) void k_oproj(const char* __restrict__ obuf, const char* __restrict__ wo, const char* __restrict__ x, const float* __restrict__ modf,
                                                  char* __restrict__ x1b, char* __restrict__ yb, f32x2* __restrict__ stat,
                                                  const float* __restrict__ utp, float* __restrict__ utf) {
  const lptr sm = (lptr)smem_raw;
  ST_INIT();
  const int b = blockIdx.x, l = threadIdx.x & 63, w = __builtin_amdgcn_readfirstlane(threadIdx.x >> 6);
  const int xcd = b & 7, j = b >> 3, tb = (xcd >> 1) * 4 + (j & 3), nt = (xcd & 1) * 8 + (j >> 2);
  if (__builtin_expect(w >= 4, LFIRST)) { u32x4 e; const int lw = w - 4;
    gemmA_load<16, K3_S, D3, 0, 0, 2, 3>(obuf + (size_t)tb * 4 * 64 * 1024, wo + nt * 131072, l, lw, [](auto) {}, [&](auto g) {
      const char* s = x + (size_t)(tb * 128 + lw * 32 + g * 16) * 2048 + nt * 128; const unsigned vx = (l >> 3) * 2048 + ((l & 7) ^ (l >> 3)) * 16;
      SETM0(K3_X + lw * 4096 + g * 2048); glds<0>(vx, s); glds<1024>(vx, s + 16384 - 1024);
      if constexpr (g == 0) { SETM0(K3_MV + lw * 1024); glds<0>((l & 16) * 512 + (l & 15) * 16, (const char*)(modf + 2 * DM + nt * 64)); }
      else { CODE_RANGE(k_ffn1, k_ffn2, cb, cs) const unsigned o = (j & 7) * 4096 + lw * 1024 + l * 16; e = gload<0>(o < cs ? o : 0, cb); } });
    waitbar<1>();
    if (b < 28) { const int i = b * 256 + lw * 64 + l; const float v = (utp[i] + utp[2 * FF + i]) + (utp[4 * FF + i] + utp[6 * FF + i]);
      utf[i] = i < FF ? v : -1.4426950408889634f * v; }
    asm volatile("s_waitcnt vmcnt(0)" : "+v"(e)::"memory"); return; }
  const int hi = l >> 5, tb32 = tb * 4 + 2 * (w & 1), n0 = nt * 64 + (w >> 1) * 32;
  f32x16 acc[2]; gemmA_compute<16, K3_S>(sm, l, w, acc[0], acc[1], S_);
  bar();
  f32x4 ga[4], cb[4];
#pragma unroll
  for (int q = 0; q < 4; ++q) { const lptr p = sm + K3_MV + w * 1024 + (w >> 1) * 128 + q * 32 + hi * 16; ga[q] = *(const LDS f32x4*)p; cb[q] = *(const LDS f32x4*)(p + 256); }
  u32x2 xr[2][4];
#pragma unroll
  for (int i = 0; i < 2; ++i)
#pragma unroll
    for (int q = 0; q < 4; ++q) xr[i][q] = *(const LDS u32x2*)(sm + K3_X + ((w & 1) * 64 + i * 32 + (l & 31)) * 128 + (((w >> 1) * 4 + q) ^ (l & 7)) * 16 + hi * 8);
#pragma unroll
  for (int i = 0; i < 2; ++i) { unsigned xo[8], yo[8]; float x1[16], s = 0.f;
#pragma unroll
    for (int h = 0; h < 2; ++h) { const u32x4 xs = {xr[i][2 * h][0], xr[i][2 * h][1], xr[i][2 * h + 1][0], xr[i][2 * h + 1][1]};
#pragma unroll
      for (int e = 0; e < 4; ++e) { const int r = 8 * h + 2 * e, q = r >> 2, c = r & 3;
        const unsigned u = pk(blo(xs[e]) + ga[q][c] * acc[i][r], bhi(xs[e]) + ga[q][c + 1] * acc[i][r + 1]);
        x1[r] = blo(u); x1[r + 1] = bhi(u); xo[4 * h + e] = u; yo[4 * h + e] = pk(x1[r] * cb[q][c], x1[r + 1] * cb[q][c + 1]); s += x1[r] + x1[r + 1]; } }
    const size_t off = (size_t)((tb32 + i) * 64 + (n0 >> 4)) * 1024;
    gstore<0>(l * 16, yb + off, (u32x4){yo[0], yo[1], yo[2], yo[3]}); gstore<1024>(l * 16, yb + off, (u32x4){yo[4], yo[5], yo[6], yo[7]});
    gstore<0>(l * 16, x1b + off, (u32x4){xo[0], xo[1], xo[2], xo[3]}); gstore<1024>(l * 16, x1b + off, (u32x4){xo[4], xo[5], xo[6], xo[7]});
    const float mean = swap32add(s) * (1.f / 32.f); float m2 = 0.f;
#pragma unroll
    for (int e = 0; e < 16; ++e) m2 += (x1[e] - mean) * (x1[e] - mean);
    m2 = swap32add(m2);
    if (hi == 0) stat[((tb32 + i) * 32 + (n0 >> 5)) * 32 + l] = (f32x2){mean, m2}; }
  ST_END(3);
}

constexpr int K4_STAGE = 45056, K4_UT = 3 * K4_STAGE, K4_RS = K4_UT + 2048, K4_LDS = K4_RS + 1024;
struct FragB { bf16x8 a[4]; s16x4 b[14]; };
extern "C" __global__ __launch_bounds__(512, 2) void k_ffn1(const char* __restrict__ yb, const char* __restrict__ w1, const char* __restrict__ stat,
                                                            const char* __restrict__ utp, char* __restrict__ gb) {
  const lptr sm = (lptr)smem_raw;
  ST_INIT();
  const int b = blockIdx.x, t = threadIdx.x, l = t & 63, w = __builtin_amdgcn_readfirstlane(t >> 6);
  const int xcd = b & 7, j = b >> 3, tb = (xcd >> 2) * 8 + (j & 7), nt = (xcd & 3) * 4 + (j >> 3);
  constexpr float NL2E = -1.4426950408889634f;
  if (__builtin_expect(w >= 4, LFIRST)) { const int lw = w - 4;
    const char* ap = yb + (size_t)(tb * 4 + lw) * 64 * 1024; const char* wp = w1 + nt * 448;
    unsigned vb[7];
#pragma unroll
    for (int i = 0; i < 7; ++i) { const int c = (lw * 7 + i) * 64 + l, r = c / 28; vb[i] = r * (FF * 2) + ((c - r * 28) ^ ((r >> 1) & 2)) * 16 + 3072 - i * 1024; }
    const unsigned va = l * 16;
    u32x4 st[8], e;
    loader<16, 3, 3, 11, K4_STAGE, 8, 1, 1, 2>([&](int tt, int slot) {
      SETM0(slot + lw * 4096); const char* a = ap + (size_t)tt * 4096;
      glds<0>(va, a); glds<1024>(va, a); glds<2048>(va, a); glds<3072>(va, a);
      SETM0(slot + 16384 + lw * 7168 + 3072); const char* s = wp + (size_t)tt * (64 * FF * 2);
      glds<-3072>(vb[0], s); glds<-2048>(vb[1], s); glds<-1024>(vb[2], s); glds<0>(vb[3], s); glds<1024>(vb[4], s); glds<2048>(vb[5], s); glds<3072>(vb[6], s); },
      [&](auto i) { st[i] = gload<0>(va, stat + (size_t)(tb * 4 + lw) * 8192 + i * 1024); },
      [&](auto) { SETM0(K4_UT + (lw & 1) * 1024); glds<0>(l < 56 ? va : 0, utp + ((lw & 1) * FF + nt * 224) * 4);
        CODE_RANGE(k_ffn2, k_mod, cb, cs) const unsigned o = (j & 3) * 4096 + lw * 1024 + l * 16; e = gload<0>(o < cs ? o : 0, cb); });
#pragma unroll
    for (int i = 0; i < 8; ++i) asm volatile("" : "+v"(st[i]));
    f32x4 o;
#pragma unroll
    for (int k = 0; k < 2; ++k) { float mean = 0.f, m2 = 0.f;
#pragma unroll
      for (int i = 0; i < 8; ++i) mean += __uint_as_float(st[i][2 * k]);
      mean = swap32add(swap16add(mean)) * (1.f / 32.f);
#pragma unroll
      for (int i = 0; i < 8; ++i) { const float d = __uint_as_float(st[i][2 * k]) - mean; m2 += __uint_as_float(st[i][2 * k + 1]) + 32.f * d * d; }
      const float rs = NL2E * __builtin_amdgcn_rsqf(swap32add(swap16add(m2)) * (1.f / 1024.f) + 1e-5f); o[2 * k] = rs; o[2 * k + 1] = -rs * mean; }
    if (l < 16) *(LDS f32x4*)(sm + K4_RS + (lw * 32 + 2 * l) * 8) = o;
    waitbar<1>();
    asm volatile("s_waitcnt vmcnt(0)" : "+v"(e)::"memory");
    return; }
  const int g = l >> 4, th = w & 1, nh = w >> 1, sw = (g & 1) * 32;
  const lptr ra = sm + th * 8192 + (g >> 1) * 1024 + (g & 1) * 512 + (l & 15) * 16;
  const lptr rb = sm + 16384 + (16 * (g >> 1) + 4 * (g & 1) + ((l >> 2) & 3)) * 448 + (l & 3) * 8;
  lptr base[4] = {ra, rb + nh * 256 + sw, rb + nh * 256 - sw, rb + (((6 + nh) * 32) ^ sw)};
  PIN(base[0]); PIN(base[1]); PIN(base[2]); PIN(base[3]);
  lptr p[4] = {base[0], base[1], base[2], base[3]};
  f32x4 acc[7][4];
  FragB f, h; int slot = 0; IT_INIT();
  auto rd = [&](FragB& d, int i, int ks) {
    if (i < 4) d.a[i] = ldsA(p[0] + (i >> 1) * 4096 + (i & 1) * 256 + ks * 2048);
    else { const int n = (i - 4) >> 1; d.b[i - 4] = ldsT1((n == 6 ? p[3] : p[1 + (n & 1)] + n * 32) + ks * (32 * 448) + (i & 1) * (8 * 448)); } };
  auto kstep = [&](FragB& cur, FragB& nxt, int ks, auto first) {
#pragma unroll
    for (int n = 0; n < 7; ++n)
#pragma unroll
      for (int i = 0; i < 4; ++i) { const int m = n * 4 + i;
        acc[n][i] = mfma16(cat(cur.b[2 * n], cur.b[2 * n + 1]), cur.a[i], first ? (f32x4){} : acc[n][i]); SB();
        if (m < 18) rd(nxt, m, ks); else if (ks == 1 && m < 22) { p[m - 18] = base[m - 18] + slot; PIN(p[m - 18]); } else if (m == W4POS) { ldswait(); IT_B(); } else if (m == 22) IT_A();
        SB(); } };
  bar(); ST(2);
#pragma unroll
  for (int i = 0; i < 18; ++i) rd(f, i, 0);
  slot = K4_STAGE; kstep(f, h, 1, std::true_type{}); sbar(); kstep(h, f, 0, std::false_type{});
  ST(4);
#pragma unroll U4
  for (int tt = 1; tt < 15; ++tt) { slot = slot == 2 * K4_STAGE ? 0 : slot + K4_STAGE; kstep(f, h, 1, std::false_type{}); sbar(); kstep(h, f, 0, std::false_type{});
  }
  ST(7);
  kstep(f, h, 1, std::false_type{});
#pragma unroll
  for (int n = 0; n < 7; ++n)
#pragma unroll
    for (int i = 0; i < 4; ++i) acc[n][i] = mfma16(cat(h.b[2 * n], h.b[2 * n + 1]), h.a[i], acc[n][i]);
  mfma_done(); bar();
  ST(3);
  f32x2 rn[4];
#pragma unroll
  for (int i = 0; i < 4; ++i) rn[i] = *(const LDS f32x2*)(sm + K4_RS + (th * 64 + i * 16 + (l & 15)) * 8);
  struct UT { f32x4 u, t; };
  auto ut = [&](int n) { const int col = ((n < 6 ? 8 * nh + n : 6 + nh) * 16 + 4 * g) * 4; return UT{*(const LDS f32x4*)(sm + K4_UT + col), *(const LDS f32x4*)(sm + K4_UT + 1024 + col)}; };
  auto act = [&](int n, int i, const UT& c) { u32x2 o;
#pragma unroll
    for (int e = 0; e < 2; ++e) { const f32x2 z = rn[i][0] * (f32x2){acc[n][i][2 * e], acc[n][i][2 * e + 1]} + (rn[i][1] * (f32x2){c.u[2 * e], c.u[2 * e + 1]} + (f32x2){c.t[2 * e], c.t[2 * e + 1]});
      const f32x2 dn = NL2E * (f32x2){EX2(z[0]), EX2(z[1])} + NL2E;
      const f32x2 r = z * (f32x2){__builtin_amdgcn_rcpf(dn[0]), __builtin_amdgcn_rcpf(dn[1])}; o[e] = pk(r[0], r[1]); }
    return o; };
  auto dst = [&](int gi, int i) { const int kk = nt * 7 + gi; return gb + (size_t)(((tb * 56 + (kk >> 1)) * 8 + th * 4 + i) * 2 + (kk & 1)) * 1024; };
  const unsigned vo = l * 16;
  UT c[7];
#pragma unroll
  for (int n = 0; n < 2; ++n) c[n] = ut(n);
#pragma unroll
  for (int p = 0; p < 3; ++p) { c[2 * p + 2] = ut(2 * p + 2); if (p < 2) c[2 * p + 3] = ut(2 * p + 3);
#pragma unroll
    for (int i = 0; i < 4; ++i) { const u32x2 lo = act(2 * p, i, c[2 * p]), hi = act(2 * p + 1, i, c[2 * p + 1]); gstore<0>(vo, dst(4 * nh + p, i), (u32x4){lo[0], lo[1], hi[0], hi[1]}); } }
#pragma unroll
  for (int i = 0; i < 4; ++i) gstore2<0>(vo, dst(3, i) + nh * 8, act(6, i, c[6]));
  IT_END(0); ST_END(4);
}

constexpr int K5_STAGE = 32768, K5_S = 5, K5_X = 3 * K5_STAGE, K5_LDS = K5_S * K5_STAGE;
struct FragC { bf16x8 a[4]; s16x4 b[8]; };
extern "C" __global__ __launch_bounds__(512, 2) void k_ffn2(const char* __restrict__ gb, const char* __restrict__ w2, const char* __restrict__ x1b,
                                                            const float* __restrict__ modf, char* __restrict__ out, char* xch) {
  const lptr sm = (lptr)smem_raw;
  ST_INIT();
  const int b = blockIdx.x, l = threadIdx.x & 63, w = __builtin_amdgcn_readfirstlane(threadIdx.x >> 6);
  const int kh = b & 1, j = b >> 3, tb = ((b & 7) >> 1) * 4 + (j & 3), nt = j >> 2;
  if (__builtin_expect(w >= 4, LFIRST)) { const int lw = w - 4;
    const char* ap = gb + (size_t)(tb * 56 + kh * 28) * 16384 + lw * 4096; const char* wp = w2 + (size_t)(nt * 56 + kh * 28) * 16384 + lw * 4096;
    const unsigned va = l * 16;
    loader<28, K5_S, D5, 8, K5_STAGE, 0, 0, 1, 5>([&](int t, int slot) {
      SETM0(slot + lw * 4096); const char* a = ap + (size_t)t * 16384; glds<0>(va, a); glds<1024>(va, a); glds<2048>(va, a); glds<3072>(va, a);
      SETM0(slot + 16384 + lw * 4096); const char* s = wp + (size_t)t * 16384;
      glds<0>(va, s); glds<1024>(va, s); glds<2048>(va, s); glds<3072>(va, s); }, [](auto) {},
      [&](auto) {
        const char* s = x1b + (size_t)((tb * 4 + (lw & 1) * 2 + kh) * 64 + nt * 8 + (lw >> 1) * 4) * 1024;
        SETM0(K5_X + lw * 4096); glds<0>(va, s); glds<1024>(va, s); glds<2048>(va, s); glds<3072>(va, s);
        SETM0(K5_X + 16384 + lw * 1024); glds<0>((l & 15) * 16, (const char*)(modf + 5 * DM + nt * 128 + (lw >> 1) * 64)); });
    waitbar<0>();
    return; }
  const int g = l >> 4, th = w & 1, nh = w >> 1, rx = 4 * (g & 1) + ((l >> 2) & 3);
  const int tok0 = tb * 128 + th * 64 + kh * 32, n0 = nt * 128 + nh * 64;
  u32x2 xr[2][4]; f32x4 gv[4];
  lptr base[6], p[6];
  base[4] = sm + th * 8192 + kh * 4096 + l * 16; base[5] = sm + th * 8192 + (kh ^ 1) * 4096 + l * 16;
#pragma unroll
  for (int n = 0; n < 4; ++n) base[n] = sm + 16384 + (4 * g + ((l >> 2) & 3)) * 256 + ((4 * nh + n) ^ rx) * 32 + (l & 3) * 8;
#pragma unroll
  for (int n = 0; n < 6; ++n) { PIN(base[n]); p[n] = base[n]; }
  f32x4 acc[4][4];
  FragC f, h; int slot = 0; IT_INIT();
  auto rd = [&](FragC& d, int i, int ks) {
    if (i < 4) d.a[i] = ldsA(p[4 + (i >> 1)] + (i & 1) * 2048 + ks * 1024); else d.b[i - 4] = ldsT1(p[(i - 4) >> 1] + ks * 8192 + (i & 1) * 4096); };
  auto kstep = [&](FragC& cur, FragC& nxt, int ks, auto first) {
#pragma unroll
    for (int n = 0; n < 4; ++n)
#pragma unroll
      for (int i = 0; i < 4; ++i) { const int m = n * 4 + i;
        acc[n][i] = mfma16(cat(cur.b[2 * n], cur.b[2 * n + 1]), cur.a[i], first ? (f32x4){} : acc[n][i]); SB();
        if (m < 12) rd(nxt, m, ks);
        else if (ks == 1) { p[m - 12] = base[m - 12] + slot; PIN(p[m - 12]); if (m < 14) { p[m - 8] = base[m - 8] + slot; PIN(p[m - 8]); } }
        if (W5V == 0 || ks == 1) { if (m == 15) { ldswait(); IT_B(); } else if (m == 12) IT_A(); } else if (m == 15) ldsw<4>();
        if (W5V == 1 && ks == 1 && m == 7) ldsw<8>();
        SB(); } };
  bar(); ST(2);
#pragma unroll
  for (int i = 0; i < 12; ++i) rd(f, i, 0);
  slot = K5_STAGE; kstep(f, h, 1, std::true_type{}); sbar(); kstep(h, f, 0, std::false_type{});
  ST(1);
#pragma unroll U5
  for (int tt = 1; tt < 25; ++tt) { slot = slot == (K5_S - 1) * K5_STAGE ? 0 : slot + K5_STAGE; kstep(f, h, 1, std::false_type{}); sbar(); kstep(h, f, 0, std::false_type{});
  }
  ST(7);
  auto hstep = [&](FragC& cur, FragC& nxt, int half, int nhalf, int ns) {
    const int off = (ns >> 1) * K5_STAGE + (ns & 1) * 1024, offw = (ns >> 1) * K5_STAGE + (ns & 1) * 8192;
#pragma unroll
    for (int m = 0; m < 8; ++m) { const int n = m >> 1, i = 2 * half + (m & 1);
      acc[n][i] = mfma16(cat(cur.b[2 * n], cur.b[2 * n + 1]), cur.a[i], acc[n][i]); SB();
      if (ns >= 0) { if (m < 2) nxt.a[2 * nhalf + m] = ldsA(base[4 + nhalf] + off + m * 2048); nxt.b[m] = ldsT1(base[m >> 1] + offw + (m & 1) * 4096); }
      SB(); } };
  hstep(f, h, 1, 1, 1); bar(); hstep(h, f, 1, 1, 2); hstep(f, h, 1, 1, 3); bar(); hstep(h, f, 1, 1, 4); hstep(f, h, 1, 1, 5); hstep(h, f, 1, 0, 0);
  ST(3);
  hstep(f, h, 0, 0, 1);
  { char* d = xch + (size_t)(b * 4 + w) * 4096;
#pragma unroll
    for (int i = 0; i < 2; ++i)
#pragma unroll
      for (int q = 0; q < 2; ++q) { const f32x4 u = acc[2 * q][2 + i], v = acc[2 * q + 1][2 + i];
        gstore<0>(l * 16, d + (i * 2 + q) * 1024, (u32x4){min(pk(u[0], u[1]), ~1u), min(pk(u[2], u[3]), ~1u), min(pk(v[0], v[1]), ~1u), min(pk(v[2], v[3]), ~1u)}); } }
  SB();
  hstep(h, f, 0, 0, 2); hstep(f, h, 0, 0, 3); hstep(h, f, 0, 0, 4); hstep(f, h, 0, 0, 5); hstep(h, f, 0, 0, -1);
  ST(4);
  sbar();
#pragma unroll
  for (int n = 0; n < 4; ++n) { gv[n] = *(const LDS f32x4*)(sm + K5_X + 16384 + w * 1024 + n * 64 + g * 16);
#pragma unroll
    for (int i = 0; i < 2; ++i) xr[i][n] = *(const LDS u32x2*)(sm + K5_X + w * 4096 + n * 1024 + (g & 1) * 512 + (i * 16 + (l & 15)) * 16 + (g >> 1) * 8); }
  u32x4 pr[4];
  { const char* s = xch + (size_t)((b ^ 1) * 4 + w) * 4096; const unsigned vo = l * 16;
    for (int spin = 0; spin < (1 << 16); ++spin) {
      pr[0] = gloadd<0>(vo, s); pr[1] = gloadd<1024>(vo, s); pr[2] = gloadd<2048>(vo, s); pr[3] = gloadd<3072>(vo, s);
      asm volatile("s_waitcnt vmcnt(0)" : "+v"(pr[0]), "+v"(pr[1]), "+v"(pr[2]), "+v"(pr[3])::"memory");
      unsigned m = 0;
#pragma unroll
      for (int i = 0; i < 4; ++i) m = max(max(max(m, pr[i][0]), max(pr[i][1], pr[i][2])), pr[i][3]);
      if (!__any(m == ~0u)) break;
    } }
  ST(6);
#pragma unroll
  for (int i = 0; i < 2; ++i)
#pragma unroll
    for (int p = 0; p < 2; ++p) { unsigned o[2][2];
#pragma unroll
      for (int q = 0; q < 2; ++q) { const int n = 2 * p + q; const unsigned u0 = pr[2 * i + p][2 * q], u1 = pr[2 * i + p][2 * q + 1];
        const f32x4 a = acc[n][i] + (f32x4){blo(u0), bhi(u0), blo(u1), bhi(u1)};
        o[q][0] = pk(blo(xr[i][n][0]) + gv[n][0] * a[0], bhi(xr[i][n][0]) + gv[n][1] * a[1]); o[q][1] = pk(blo(xr[i][n][1]) + gv[n][2] * a[2], bhi(xr[i][n][1]) + gv[n][3] * a[3]); }
      auto r0 = __builtin_amdgcn_permlane16_swap(o[0][0], o[1][0], false, false); auto r1 = __builtin_amdgcn_permlane16_swap(o[0][1], o[1][1], false, false);
      gstore<0>((l & 15) * 2048 + (g & 1) * 32 + (g >> 1) * 16, out + (size_t)(tok0 + i * 16) * 2048 + (n0 + 32 * p) * 2, (u32x4){r0[0], r1[0], r0[1], r1[1]}); }
  IT_END(1); ST_END(5);
}

extern "C" __global__ __launch_bounds__(512) void k_mod(const uint16_t* __restrict__ c, const char* __restrict__ wmod, float* __restrict__ modp,
                                             const char* __restrict__ x, const char* __restrict__ wq, const char* __restrict__ wk, const char* __restrict__ wv, char* __restrict__ wqp) {
  __shared__ __attribute__((aligned(16))) float red[8][64];
  ST_INIT();
  const int t = threadIdx.x;
  unsigned own;
  { unsigned long long pc; int n; asm volatile("s_getpc_b64 %0\n.Lown0:\n\ts_mov_b32 %1, .Lown1-.Lown0" : "=s"(pc), "=s"(n)); const int o = (t & 63) * 64;
    own = gload1<0>(o < n ? o : 0, (const char*)(pc & ~3ull)); }
  if (blockIdx.x >= PF0 && blockIdx.x < PF0 + 128) { const int p = blockIdx.x - PF0; u32x4 d[7];
    const char* xb = x + (size_t)((p & 7) * 16 + (p >> 3)) * 32768;
    { const char* s = wq + (size_t)(p & 7) * 262144 + (p >> 3) * 128; const unsigned v = imgA(t & 63) + (t >> 6) * 32768; d[4] = gload<0>(v, s); d[5] = gload<0>(v, s + 16384); }
#pragma unroll
    for (int i = 0; i < 4; ++i) d[i] = gload<0>(t * 16, xb + i * 8192);
    d[6] = gload<0>((t & 63) * 16, (p & 64 ? wv : wk) + (p & 63) * 1024);
    { CODE_RANGE(k_ln1kv, k_mod, cb, cs) const unsigned o = (p >> 3) * 8192 + t * 16; u32x4 e = gload<0>(o < cs ? o : 0, cb);
      asm volatile("s_waitcnt vmcnt(6)" : "+v"(d[4]), "+v"(d[5])::"memory");
      const unsigned v = (t & 63) * 16 + (t >> 6) * 2048; char* o2 = wqp + ((p >> 3) * 8 + (p & 7)) * 16384; gstore<0>(v, o2, d[4]); gstore<1024>(v, o2, d[5]);
      asm volatile("s_waitcnt vmcnt(0)" : "+v"(e)::"memory"); }
    asm volatile("s_waitcnt vmcnt(0)" : "+v"(d[0]), "+v"(d[1]), "+v"(d[2]), "+v"(d[3]), "+v"(d[4]), "+v"(d[5]), "+v"(d[6])::"memory");
    asm volatile(".Lown1:" : "+v"(own));
    return; }
  const int b = blockIdx.x < PF0 ? blockIdx.x : blockIdx.x - 128, cg = b % 96, ks = b / 96;
  const int row0 = ks * 256 + (t >> 3) * 4;
  const char* p = wmod + (size_t)row0 * (NMOD * 2) + cg * 128 + (t & 7) * 16;
  u32x4 w[4]; const u32x2 cv = *(const u32x2*)(c + row0);
#pragma unroll
  for (int i = 0; i < 4; ++i) w[i] = *(const u32x4*)(p + (size_t)i * NMOD * 2);
  float a[8] = {0, 0, 0, 0, 0, 0, 0, 0};
#pragma unroll
  for (int i = 0; i < 4; ++i) { const float f = rbf(silu(i & 1 ? bhi(cv[i >> 1]) : blo(cv[i >> 1])));
#pragma unroll
    for (int e = 0; e < 4; ++e) { a[2 * e] += f * blo(w[i][e]); a[2 * e + 1] += f * bhi(w[i][e]); } }
#pragma unroll
  for (int e = 0; e < 8; ++e) a[e] = swap32add(swap16add(dppadd<0x128>(a[e])));
  if ((t & 63) < 8) { *(f32x4*)&red[t >> 6][(t & 7) * 8] = (f32x4){a[0], a[1], a[2], a[3]}; *(f32x4*)&red[t >> 6][(t & 7) * 8 + 4] = (f32x4){a[4], a[5], a[6], a[7]}; }
  __syncthreads();
  PIN(own);
  if (t < 64) modp[ks * NMOD + cg * 64 + t] = ((red[0][t] + red[1][t]) + (red[2][t] + red[3][t])) + ((red[4][t] + red[5][t]) + (red[6][t] + red[7][t]));
  ST_END(0);
}

extern "C" void kernel_launch(void* const* d_in, const int* in_sizes, int n_in, void* d_out, int out_size, void* d_ws, size_t ws_size, hipStream_t stream) {
  const char* x = (const char*)d_in[0]; const char* wq = (const char*)d_in[1]; const char* wk = (const char*)d_in[2]; const char* wv = (const char*)d_in[3];
  const char* wo = (const char*)d_in[4]; const char* w1 = (const char*)d_in[6]; const char* w2 = (const char*)d_in[7];
  const uint16_t* c = (const uint16_t*)d_in[8]; const char* wmod = (const char*)d_in[9];
  char* ws = (char*)d_ws;
  (void)hipFuncSetAttribute((const void*)k_ln1kv, hipFuncAttributeMaxDynamicSharedMemorySize, K1_LDS);
  (void)hipFuncSetAttribute((const void*)k_qattn, hipFuncAttributeMaxDynamicSharedMemorySize, K2_LDS);
  (void)hipFuncSetAttribute((const void*)k_oproj, hipFuncAttributeMaxDynamicSharedMemorySize, K3_LDS);
  (void)hipFuncSetAttribute((const void*)k_ffn1, hipFuncAttributeMaxDynamicSharedMemorySize, K4_LDS);
  (void)hipFuncSetAttribute((const void*)k_ffn2, hipFuncAttributeMaxDynamicSharedMemorySize, K5_LDS);
  float* modp = (float*)(ws + WS_MODP); float* modf = (float*)(ws + WS_MODF);
  k_mod<<<512, 512, 0, stream>>>(c, wmod, modp, x, wq, wk, wv, ws + WS_WQP);
  k_ln1kv<<<256, 512, K1_LDS, stream>>>(x, wk, wv, (const char*)modp, modf, (uint16_t*)(ws + WS_MODH), ws + WS_H, ws + WS_KV);
  k_qattn<<<256, 512, K2_LDS, stream>>>(ws + WS_H, ws + WS_WQP, ws + WS_KV, ws + WS_O, w1, ws + WS_MODH, (float*)(ws + WS_UTP), wo, w2, ws + WS_XCH, ws + WS_WOP, ws + WS_W2P, (const char*)d_out);
  k_oproj<<<256, 512, K3_LDS, stream>>>(ws + WS_O, ws + WS_WOP, x, modf, ws + WS_X1, ws + WS_Y, (f32x2*)(ws + WS_STAT), (const float*)(ws + WS_UTP), (float*)(ws + WS_UTF));
  k_ffn1<<<256, 512, K4_LDS, stream>>>(ws + WS_Y, w1, ws + WS_STAT, ws + WS_UTF, ws + WS_G);
  k_ffn2<<<256, 512, K5_LDS, stream>>>(ws + WS_G, ws + WS_W2P, ws + WS_X1, modf, (char*)d_out, ws + WS_XCH);
}
```

```cpp
#include <hip/hip_runtime.h>
#include <hip/hip_bf16.h>
#include <stdint.h>
#include <type_traits>

typedef __attribute__((ext_vector_type(2))) float f32x2;
typedef __attribute__((ext_vector_type(4))) float f32x4;
typedef __attribute__((ext_vector_type(16))) float f32x16;
typedef __attribute__((ext_vector_type(2))) __bf16 bf16x2;
typedef __attribute__((ext_vector_type(8))) __bf16 bf16x8;
typedef __attribute__((ext_vector_type(4))) short s16x4;
typedef __attribute__((ext_vector_type(2))) unsigned u32x2;
typedef __attribute__((ext_vector_type(4))) unsigned u32x4;
typedef __attribute__((ext_vector_type(8))) unsigned u32x8;
typedef __attribute__((ext_vector_type(16))) __bf16 bf16x16;
#define LDS __attribute__((address_space(3)))
typedef LDS char* lptr;
constexpr int D2 = 4, D3 = 4, D5 = 4;
constexpr int PF0 = 384;
#define DEV __device__ __forceinline__

constexpr int SEQ = 2048, DM = 1024, FF = 3584, NMOD = 6 * DM;
constexpr size_t WS_MODP = 0;
constexpr size_t WS_UTP = 98304;
constexpr size_t WS_MODF = 212992;
constexpr size_t WS_MODH = 237568;
constexpr size_t WS_STAT = 262144;
constexpr size_t WS_KV = 786432;
constexpr size_t WS_UTF = (size_t)48 << 20;
constexpr size_t WS_H = (size_t)1 << 20;
constexpr size_t WS_O = (size_t)5 << 20;
constexpr size_t WS_X1 = (size_t)9 << 20;
constexpr size_t WS_Y = (size_t)13 << 20;
constexpr size_t WS_G = (size_t)17 << 20;
constexpr size_t WS_WQP = (size_t)36 << 20;
constexpr size_t WS_WOP = (size_t)38 << 20;
constexpr size_t WS_W2P = (size_t)40 << 20;
constexpr size_t WS_XCH = (size_t)32 << 20;

DEV unsigned pk(float a, float b) { return __builtin_bit_cast(unsigned, __builtin_convertvector((f32x2){a, b}, bf16x2)); }
DEV float rbf(float a) { return __uint_as_float(pk(a, 0.f) << 16); }
DEV float blo(unsigned u) { return __uint_as_float(u << 16); }
DEV float bhi(unsigned u) { return __uint_as_float(u & 0xffff0000u); }
DEV float silu(float z) { return z * __builtin_amdgcn_rcpf(1.f + __builtin_amdgcn_exp2f(-1.4426950408889634f * z)); }
DEV f32x16 mfma32(bf16x8 a, bf16x8 b, f32x16 c) { return __builtin_amdgcn_mfma_f32_32x32x16_bf16(a, b, c, 0, 0, 0); }
DEV f32x4 mfma16(bf16x8 a, bf16x8 b, f32x4 c) { return __builtin_amdgcn_mfma_f32_16x16x32_bf16(a, b, c, 0, 0, 0); }
DEV bf16x8 asb(u32x4 v) { return __builtin_bit_cast(bf16x8, v); }
DEV bf16x8 ldsA(lptr p) { return *(const LDS bf16x8*)p; }
DEV bf16x8 ldsT(lptr p, int second) {
  s16x4 a = __builtin_amdgcn_ds_read_tr16_b64_v4i16((LDS s16x4*)p);
  s16x4 b = __builtin_amdgcn_ds_read_tr16_b64_v4i16((LDS s16x4*)(p + second));
  return __builtin_bit_cast(bf16x8, __builtin_shufflevector(a, b, 0, 1, 2, 3, 4, 5, 6, 7));
}
DEV s16x4 ldsT1(lptr p) { return __builtin_amdgcn_ds_read_tr16_b64_v4i16((LDS s16x4*)p); }
DEV bf16x8 cat(s16x4 a, s16x4 b) { return __builtin_bit_cast(bf16x8, __builtin_shufflevector(a, b, 0, 1, 2, 3, 4, 5, 6, 7)); }
template <int C> DEV float dppadd(float v) { return v + __int_as_float(__builtin_amdgcn_update_dpp(0, __float_as_int(v), C, 0xf, 0xf, true)); }
DEV float swap32add(float v) { auto r = __builtin_amdgcn_permlane32_swap(__float_as_uint(v), __float_as_uint(v), false, false); return __uint_as_float(r[0]) + __uint_as_float(r[1]); }
DEV float swap16add(float v) { auto r = __builtin_amdgcn_permlane16_swap(__float_as_uint(v), __float_as_uint(v), false, false); return __uint_as_float(r[0]) + __uint_as_float(r[1]); }
DEV float swap32max(float v) { auto r = __builtin_amdgcn_permlane32_swap(__float_as_uint(v), __float_as_uint(v), false, false); return __builtin_fmaxf(__uint_as_float(r[0]), __uint_as_float(r[1])); }
DEV float wavesum(float v) { v = dppadd<0xB1>(v); v = dppadd<0x4E>(v); v = dppadd<0x141>(v); v = dppadd<0x140>(v); v = swap16add(v); return swap32add(v); }

#define SETM0(x) asm volatile("s_mov_b32 m0, %0\n\ts_nop 0" ::"s"(x) : "memory")
template <int OFF> DEV void glds(unsigned voff, const void* base) { asm volatile("global_load_lds_dwordx4 %0, %1 offset:%2" ::"v"(voff), "s"(base), "i"(OFF) : "memory"); }
template <int N> DEV void waitbar() { asm volatile("s_waitcnt vmcnt(%0) lgkmcnt(0)\n\ts_barrier" ::"i"(N) : "memory"); }
template <int OFF> DEV u32x4 gload(unsigned voff, const void* base) { u32x4 r; asm volatile("global_load_dwordx4 %0, %1, %2 offset:%3" : "=v"(r) : "v"(voff), "s"(base), "i"(OFF) : "memory"); return r; }
template <int OFF> DEV u32x2 gload2(unsigned voff, const void* base) { u32x2 r; asm volatile("global_load_dwordx2 %0, %1, %2 offset:%3" : "=v"(r) : "v"(voff), "s"(base), "i"(OFF) : "memory"); return r; }
template <int OFF> DEV unsigned gload1(unsigned voff, const void* base) { unsigned r; asm volatile("global_load_dword %0, %1, %2 offset:%3" : "=v"(r) : "v"(voff), "s"(base), "i"(OFF) : "memory"); return r; }
template <int OFF> DEV u32x4 gloadd(unsigned voff, const void* base) { u32x4 r; asm volatile("global_load_dwordx4 %0, %1, %2 offset:%3 sc1" : "=v"(r) : "v"(voff), "s"(base), "i"(OFF) : "memory"); return r; }
template <int OFF> DEV void gstore2(unsigned voff, void* base, u32x2 v) { asm volatile("global_store_dwordx2 %0, %1, %2 offset:%3 sc1" ::"v"(voff), "v"(v), "s"(base), "i"(OFF) : "memory"); }
template <int OFF> DEV void gstore(unsigned voff, void* base, u32x4 v) { asm volatile("global_store_dwordx4 %0, %1, %2 offset:%3 sc1\n\ts_nop 1" ::"v"(voff), "v"(v), "s"(base), "i"(OFF) : "memory"); }
#define CODE_RANGE(A, B, base, size) const char* base; unsigned size; { unsigned long long pc_; int o_; \
  asm volatile("s_getpc_b64 %0\n.Lcr%=:\n\ts_mov_b32 %1, " #A "-.Lcr%=\n\ts_mov_b32 %2, " #B "-" #A : "=s"(pc_), "=s"(o_), "=s"(size)); base = (const char*)(pc_ + o_); }
#define SB() __builtin_amdgcn_sched_barrier(0)
#define PIN(x) asm volatile("" : "+v"(x))
extern __shared__ __attribute__((aligned(16))) char smem_raw[];

constexpr int K1_W = 0, K1_H = 65536, K1_MV = K1_H + 64 * 528, K1_RED = K1_MV + 8192, K1_LDS = K1_RED + 32768;
extern "C" __global__ __launch_bounds__(512, 2) void k_ln1kv(const char* __restrict__ x, const char* __restrict__ wk, const char* __restrict__ wv,
                                                  const char* __restrict__ modp, float* __restrict__ modf, uint16_t* __restrict__ modh,
                                                  char* __restrict__ hbuf, char* __restrict__ kv) {
  const lptr sm = (lptr)smem_raw;
  const int b = blockIdx.x, t = threadIdx.x, l = t & 63, w = __builtin_amdgcn_readfirstlane(t >> 6);
  const int which = (b >> 3) & 1, rb = (b & 7) * 16 + (b >> 4);
  u32x4 xv[4], mp[4];
  { const char* xb = x + (size_t)(rb * 16 + 2 * w) * 2048; const unsigned vo = l * 32;
    xv[0] = gload<0>(vo, xb); xv[1] = gload<16>(vo, xb); xv[2] = gload<2048>(vo, xb); xv[3] = gload<2064>(vo, xb);
    asm volatile("s_waitcnt vmcnt(0)" ::: "memory");
#pragma unroll
    for (int s = 0; s < 4; ++s) mp[s] = gload<0>(t * 16, modp + s * NMOD * 4); }
  asm volatile("s_waitcnt vmcnt(4)" : "+v"(xv[0]), "+v"(xv[1]), "+v"(xv[2]), "+v"(xv[3])::"memory");
  const char* wsrc = (which ? wv : wk) + w * 8192;
  auto wload = [&](int g) { SETM0(K1_W + w * 8192 + g * 2048); glds<0>(l * 16, wsrc + g * 2048); glds<1024>(l * 16, wsrc + g * 2048); };
  float rs[2], nm[2];
  { float s[4] = {0.f, 0.f, 0.f, 0.f}; const bf16x2 one = __builtin_bit_cast(bf16x2, 0x3f803f80u);
#pragma unroll
    for (int e = 0; e < 8; ++e)
#pragma unroll
      for (int r = 0; r < 2; ++r) { const unsigned xx = xv[2 * r + (e >> 2)][e & 3]; const bf16x2 u = __builtin_bit_cast(bf16x2, xx);
        s[r] = __builtin_amdgcn_fdot2_f32_bf16(u, one, s[r], false); s[2 + r] = __builtin_amdgcn_fdot2_f32_bf16(u, u, s[2 + r], false); }
#pragma unroll
    for (int i = 0; i < 4; ++i) s[i] = wavesum(s[i]) * (1.f / 1024.f);
#pragma unroll
    for (int r = 0; r < 2; ++r) { rs[r] = __builtin_amdgcn_rsqf(__builtin_fmaxf(s[2 + r] - s[r] * s[r], 0.f) + 1e-5f); nm[r] = -s[r] * rs[r]; } }
  PIN(rs[0]); PIN(rs[1]);
  asm volatile("s_waitcnt vmcnt(0)" : "+v"(mp[0]), "+v"(mp[1]), "+v"(mp[2]), "+v"(mp[3])::"memory");
  { f32x4 o;
#pragma unroll
    for (int e = 0; e < 4; ++e) { const float v = rbf((__uint_as_float(mp[0][e]) + __uint_as_float(mp[1][e])) + (__uint_as_float(mp[2][e]) + __uint_as_float(mp[3][e])));
      o[e] = rbf(v + (w >= 4 ? 1.f : 0.f)); }
    *(LDS f32x4*)(sm + K1_MV + (t & 256) * 16 + (t & 3) * 1024 + ((t & 255) >> 2) * 16) = o; }
  wload(0); wload(1);
  asm volatile("s_waitcnt lgkmcnt(0)\n\ts_barrier" ::: "memory");
  { unsigned u[2][8];
#pragma unroll
    for (int e = 0; e < 4; ++e) { const f32x4 a = *(const LDS f32x4*)(sm + K1_MV + e * 1024 + l * 16), cc = *(const LDS f32x4*)(sm + K1_MV + 4096 + e * 1024 + l * 16);
      if (e < 2) { SB(); wload(e + 2); SB(); }
#pragma unroll
      for (int r = 0; r < 2; ++r)
#pragma unroll
        for (int i = 0; i < 2; ++i) { const unsigned xx = xv[2 * r + (e >> 1)][(2 * e + i) & 3];
          const f32x2 h = ((f32x2){blo(xx), bhi(xx)} * rs[r] + nm[r]) * (f32x2){cc[2 * i], cc[2 * i + 1]} + (f32x2){a[2 * i], a[2 * i + 1]}; u[r][2 * e + i] = pk(h[0], h[1]); } }
#pragma unroll
    for (int r = 0; r < 2; ++r) { const lptr d = sm + K1_H + l * 528 + (2 * w + r) * 16;
      *(LDS u32x4*)d = (u32x4){u[r][0], u[r][1], u[r][4], u[r][5]}; *(LDS u32x4*)(d + 256) = (u32x4){u[r][2], u[r][3], u[r][6], u[r][7]}; } }
  waitbar<0>();
  auto hst = [&]() {
#pragma unroll
    for (int i = 0; i < 2; ++i) { const int id = t + 512 * i, tok = id & 15, h2 = (id >> 4) & 1, kb = which * 32 + (id >> 5);
      gstore<0>(kb * 1024 + (h2 * 32 + tok) * 16, hbuf + (size_t)(rb >> 1) * 65536 + (rb & 1) * 256, *(const LDS u32x4*)(sm + K1_H + kb * 528 + h2 * 256 + tok * 16)); }
  };
  f32x16 acc = {};
  { const int hi = l >> 5; const lptr hp = sm + K1_H + w * 8 * 528 + hi * 256 + (l & 15) * 16;
    const lptr wp = sm + K1_W + (w * 128 + 4 * hi + ((l >> 2) & 3)) * 64 + ((l >> 4) & 1) * 32 + (l & 3) * 8;
    bf16x8 fa[8], fb[8];
#pragma unroll
    for (int i = 0; i < 8; ++i) { fa[i] = ldsA(hp + i * 528); fb[i] = ldsT(wp + i * 1024, 512); }
    hst();
    if (which == 0) {
#pragma unroll
      for (int i = 0; i < 8; ++i) acc = mfma32(fb[i], fa[i], acc);
    } else {
#pragma unroll
      for (int i = 0; i < 8; ++i) acc = mfma32(fa[i], fb[i], acc);
    } }
#pragma unroll
  for (int q = 0; q < 4; ++q) *(LDS f32x4*)(sm + K1_RED + w * 4096 + q * 1024 + l * 16) = (f32x4){acc[4 * q], acc[4 * q + 1], acc[4 * q + 2], acc[4 * q + 3]};
  asm volatile("s_waitcnt lgkmcnt(0)\n\ts_barrier" ::: "memory");

  if (w == 0) {
    const lptr rp = sm + K1_RED + (which ? l * 16 : (l >> 5) * 2048 + (((l >> 4) & 1) * 32 + (l & 15)) * 16);
    f32x4 a = {}, c = {};
#pragma unroll
    for (int u = 0; u < 8; ++u) { a += *(const LDS f32x4*)(rp + u * 4096); c += *(const LDS f32x4*)(rp + u * 4096 + 1024); }
    char* base = kv + (rb >> 1) * 4096;
    char* d = which ? base + 2048 + (rb & 1) * 1024 + l * 16 : base + (l >> 5) * 1024 + (((l >> 4) & 1) * 32 + (rb & 1) * 16 + (l & 15)) * 16;
    *(u32x4*)d = (u32x4){pk(a[0], a[1]), pk(a[2], a[3]), pk(c[0], c[1]), pk(c[2], c[3])};
  } else if (b < 64 && w == 1) {
    const int col = 2048 + b * 64 + l; const float* mpf = (const float*)modp;
    float v2 = rbf((mpf[col] + mpf[NMOD + col]) + (mpf[2 * NMOD + col] + mpf[3 * NMOD + col]));
    if ((col >> 10) == 4) v2 = rbf(1.f + v2);
    modf[col] = v2; modh[col] = (uint16_t)(__float_as_uint(v2) >> 16);
  }
}

DEV void mfma_done() { SB(); asm volatile("s_nop 7\n\ts_nop 7\n\ts_nop 7"); SB(); }
DEV void ldswait() { __builtin_amdgcn_s_waitcnt(0xC07F); }
template <int N> DEV void ldsw() { __builtin_amdgcn_s_waitcnt(0xC07F | (N << 8)); }
DEV void sbar() { asm volatile("s_barrier" ::: "memory"); }
DEV void bar() { asm volatile("" ::: "memory"); ldswait(); asm volatile("s_barrier" ::: "memory"); }
template <int N, int G, int K, int LN, int LP, class L> DEV void loader_tail(L&& late) {
  waitbar<G * N + LP * (K < LN ? K : LN)>(); if constexpr (K < LN) late(std::integral_constant<int, K>{});
  if constexpr (N > 0) loader_tail<N - 1, G, K + 1, LN, LP>(late); }
constexpr int younger(int t, int D, int EN) { const int a = t - D + 2 < 1 ? 1 : t - D + 2, b = t < EN ? t : EN; return b >= a ? b - a + 1 : 0; }
template <int T, int T0, int S, int D, int G, int STAGE, int EN, int EP, class I, class E> DEV void loader_ramp(I&& issue, E&& early) {
#pragma unroll
  for (int s = T == 0 ? 0 : D + T - 2; s < D + T - 1; ++s) issue(s, (s % S) * STAGE);
  if constexpr (T >= 1 && T <= EN) early(std::integral_constant<int, T - 1>{});
  waitbar<G*(D - 2) + EP * younger(T, D, EN)>();
  if constexpr (T < T0) loader_ramp<T + 1, T0, S, D, G, STAGE, EN, EP>(issue, early);
}
template <int KT, int S, int D, int G, int STAGE, int EN, int EP, int LN, int LP, class I, class E, class L>
DEV void loader(I&& issue, E&& early, L&& late) {
  static_assert(D <= S && EN + D - 2 <= KT - D, "");
  loader_ramp<0, KT - D, S, D, G, STAGE, EN, EP>(issue, early);
  issue(KT - 1, ((KT - 1) % S) * STAGE);
  loader_tail<D - 2, G, 0, LN, LP>(late);
}
constexpr int GA_STAGE = 24576;
DEV unsigned imgA(int l) { return (l >> 3) * (DM * 2) + (((l & 7) ^ (((l >> 4) & 1) << 2)) * 16); }
template <int KT, int S, int D, int EN, int EP, int LN, int LP, class E, class L>
DEV void gemmA_load(const char* act, const char* wgt, int l, int lw, E&& early, L&& late) {
  const char* ap = act + (size_t)lw * (KT * 4096);
  const char* wp = wgt + lw * 2048;
  const unsigned va = l * 16;
  loader<KT, S, D, 6, GA_STAGE, EN, EP, LN, LP>([&](int t, int slot) {
    SETM0(slot + lw * 4096); const char* a = ap + (size_t)t * 4096; glds<0>(va, a); glds<1024>(va, a); glds<2048>(va, a); glds<3072>(va, a);
    SETM0(slot + 16384 + lw * 2048); const char* s = wp + t * 8192; glds<0>(va, s); glds<1024>(va, s); }, early, late);
}
struct FragA { bf16x8 a0[4], a1[4], b[4]; };
template <int KT, int S>
DEV void gemmA_compute(const lptr sm, int l, int w, f32x16& c0, f32x16& c1) {
  const int q = (l >> 2) & 3, p = l & 3;
  const lptr ra = sm + (w & 1) * 8192 + l * 16;
  const lptr rb = sm + 16384 + (4 * (l >> 5) + q) * 128 + ((((w >> 1) * 4 + ((l >> 4) & 1) * 2 + (p >> 1)) ^ ((q >> 1) << 2)) * 16) + (p & 1) * 8;
  c0 = (f32x16){}; c1 = (f32x16){};
  FragA f, g; int slot = 0;
  auto rd = [&](FragA& d, int k) { d.a0[k] = ldsA(ra + slot + k * 1024); d.b[k] = ldsT(rb + slot + k * 2048, 1024); d.a1[k] = ldsA(ra + slot + 4096 + k * 1024); };
  auto step = [&](FragA& cur, FragA& nxt) { bar(); slot = slot == (S - 1) * GA_STAGE ? 0 : slot + GA_STAGE;
#pragma unroll
    for (int k = 0; k < 4; ++k) { if (k < 2) rd(nxt, 2 * k); c0 = mfma32(cur.b[k], cur.a0[k], c0); SB(); if (k < 2) rd(nxt, 2 * k + 1); c1 = mfma32(cur.b[k], cur.a1[k], c1); SB(); } };
  bar();
#pragma unroll
  for (int k = 0; k < 4; ++k) rd(f, k);
  for (int t = 0; t < KT - 2; t += 2) { step(f, g); step(g, f); }
  step(f, g);
#pragma unroll
  for (int k = 0; k < 4; ++k) { c0 = mfma32(g.b[k], g.a0[k], c0); c1 = mfma32(g.b[k], g.a1[k], c1); }
  mfma_done();
}

constexpr int K2_KV0 = 98304, K2_KV1 = 131072, K2_KV2 = 0, K2_UW = 32768, K2_Q = 65536, K2_RED = 73728, K2_DUMP = 81920, K2_LDS = 163840;
DEV void pack16(const f32x16& s, u32x4& a, u32x4& b) {
  a = (u32x4){pk(s[0], s[1]), pk(s[2], s[3]), pk(s[4], s[5]), pk(s[6], s[7])};
  b = (u32x4){pk(s[8], s[9]), pk(s[10], s[11]), pk(s[12], s[13]), pk(s[14], s[15])}; }
DEV float max16(const f32x16& s) { float m = s[0];
#pragma unroll
  for (int i = 1; i < 16; ++i) m = __builtin_fmaxf(m, s[i]);
  return swap32max(m); }
#define EX2(v) __builtin_amdgcn_exp2f(v)

extern "C" __global__ __launch_bounds__(512, 2) void k_qattn(const char* __restrict__ hbuf, const char* __restrict__ wq, const char* __restrict__ kv, char* __restrict__ obuf,
                                                  const char* __restrict__ w1, const char* __restrict__ modh, float* __restrict__ utp,
                                                  const char* __restrict__ wo, const char* __restrict__ w2, char* __restrict__ xch, char* __restrict__ wop, char* __restrict__ w2p) {
  const lptr sm = (lptr)smem_raw;
  const int b = blockIdx.x, l = threadIdx.x & 63, w = __builtin_amdgcn_readfirstlane(threadIdx.x >> 6);
  const int xcd = b & 7, j = b >> 3, tb = (xcd >> 1) * 4 + (j & 3), hp = (xcd & 1) * 8 + (j >> 2);
  const unsigned l16 = l * 16;
  if (w >= 4) { const int lw = w - 4;
    auto kvload = [&](int st, int slot) {
#pragma unroll
      for (int g = 0; g < 2; ++g) { SETM0(slot + lw * 8192 + g * 4096); const char* s = kv + st * 32768 + lw * 8192 + g * 4096;
        glds<0>(l16, s); glds<1024>(l16, s); glds<2048>(l16, s); glds<3072>(l16, s); } };
    gemmA_load<16, 4, D2, 8, 1, 2, 4>(hbuf + (size_t)tb * 4 * 64 * 1024, wq + hp * 131072, l, lw,
      [&](auto i) { SETM0(K2_KV0 + lw * 8192 + i * 1024); glds<0>(l16, kv + lw * 8192 + i * 1024); },
      [&](auto g) { SETM0(K2_KV1 + lw * 8192 + g * 4096); const char* s = kv + 32768 + lw * 8192 + g * 4096; glds<0>(l16, s); glds<1024>(l16, s); glds<2048>(l16, s); glds<3072>(l16, s); });
    waitbar<8>();
    const int ub = b < 224 ? b : b - 224, cg = ub % 56, ks = ub / 56;
    u32x2 vf[8];
    waitbar<0>(); kvload(2, K2_KV2);
    { const unsigned vo = (l >> 3) * (FF * 2) + (((l & 7) ^ (((l >> 4) & 1) << 2)) * 16);
#pragma unroll
      for (int g = 0; g < 2; ++g) { SETM0(K2_UW + lw * 8192 + g * 4096); const char* s = w1 + (size_t)(ks * 256 + lw * 64 + g * 32) * (FF * 2) + cg * 128;
        glds<0>(vo, s); glds<1024>(vo, s + 8 * FF * 2 - 1024); glds<2048>(vo, s + 16 * FF * 2 - 2048); glds<3072>(vo, s + 24 * FF * 2 - 3072); }
      const char* mv = modh + (ks * 256 + lw * 64) * 2; const unsigned vm = ((4 - (l & 1)) * DM + 4 * (l >> 5)) * 2;
      vf[0] = gload2<0>(vm, mv); vf[1] = gload2<16>(vm, mv); vf[2] = gload2<32>(vm, mv); vf[3] = gload2<48>(vm, mv);
      vf[4] = gload2<64>(vm, mv); vf[5] = gload2<80>(vm, mv); vf[6] = gload2<96>(vm, mv); vf[7] = gload2<112>(vm, mv); }
    waitbar<16>(); kvload(3, K2_KV0);
    u32x4 rp[9];
    { const char* s = wo + (size_t)((b & 15) * 64 + lw * 16) * 2048 + (b >> 4) * 128; rp[7] = gload<0>(imgA(l), s); rp[8] = gload<0>(imgA(l), s + 16384);
      const int r = ((b & 31) * 4 + lw) * 7; const unsigned v0 = (l >> 4) * 2048 + ((l & 15) ^ ((l >> 4) * 2)) * 16;
#pragma unroll
      for (int i = 0; i < 7; ++i) rp[i] = gload<0>(v0 ^ (((r + i) & 1) * 128), w2 + (size_t)(r + i) * 8192 + (b >> 5) * 256); }
    waitbar<9>(); kvload(4, K2_KV1);
    { const int q = (l >> 2) & 3, p = l & 3; f32x16 d0 = {}, d1 = {};
      const lptr rb = sm + K2_UW + (lw * 64 + 4 * (l >> 5) + q) * 128 + (((((l >> 4) & 1) * 2 + (p >> 1)) ^ ((q >> 1) << 2)) * 16) + (p & 1) * 8;
      const lptr rc = sm + K2_UW + (lw * 64 + 4 * (l >> 5) + q) * 128 + (((4 + ((l >> 4) & 1) * 2 + (p >> 1)) ^ ((q >> 1) << 2)) * 16) + (p & 1) * 8;
#pragma unroll
      for (int kk = 0; kk < 4; ++kk) { PIN(vf[2 * kk]); PIN(vf[2 * kk + 1]); const bf16x8 vv = asb((u32x4){vf[2 * kk][0], vf[2 * kk][1], vf[2 * kk + 1][0], vf[2 * kk + 1][1]});
        d0 = mfma32(ldsT(rb + kk * 2048, 1024), vv, d0); d1 = mfma32(ldsT(rc + kk * 2048, 1024), vv, d1); }
      if ((l & 31) < 2) { const lptr d = sm + K2_RED + lw * 512 + (l & 31) * 256 + (l >> 5) * 16;
#pragma unroll
        for (int i = 0; i < 4; ++i) { *(LDS f32x4*)(d + i * 32) = (f32x4){d0[4 * i], d0[4 * i + 1], d0[4 * i + 2], d0[4 * i + 3]};
          *(LDS f32x4*)(d + 128 + i * 32) = (f32x4){d1[4 * i], d1[4 * i + 1], d1[4 * i + 2], d1[4 * i + 3]}; } } }
    waitbar<0>(); kvload(5, K2_KV2);
    if (b < 224 && lw < 2) { float v = 0.f;
#pragma unroll
      for (int u = 0; u < 4; ++u) v += *(const LDS float*)(sm + K2_RED + u * 512 + lw * 256 + l * 4);
      utp[(ks * 2 + lw) * FF + cg * 64 + l] = v; }
    waitbar<0>(); kvload(6, K2_KV0);
    { char* d = xch + b * 16384 + lw * 4096; const u32x4 e = {~0u, ~0u, ~0u, ~0u}; gstore<0>(l16, d, e); gstore<1024>(l16, d, e); gstore<2048>(l16, d, e); gstore<3072>(l16, d, e);
#pragma unroll
      for (int i = 0; i < 9; ++i) PIN(rp[i]);
      char* o = wop + b * 8192 + lw * 2048; gstore<0>(l16, o, rp[7]); gstore<1024>(l16, o, rp[8]);
#pragma unroll
      for (int i = 0; i < 7; ++i) gstore<0>(l16, w2p + (size_t)(b * 4 + lw) * 7168 + i * 1024, rp[i]); }
    waitbar<13>(); kvload(7, K2_KV1);
    { CODE_RANGE(k_oproj, k_mod, cb, cs) const unsigned o = (j & 15) * 4096 + lw * 1024 + l16; SETM0(K2_DUMP + lw * 1024); glds<0>(o < cs ? o : 0, cb); }
    waitbar<1>();
    asm volatile("s_waitcnt vmcnt(0)" ::: "memory");
    return; }
  bf16x8 qA0, qA1, qB0, qB1;
  { f32x16 c0, c1; gemmA_compute<16, 4>(sm, l, w, c0, c1);
    constexpr float QS = 0.17677669529663687f * 1.4426950408889634f;
    c0 *= QS; c1 *= QS;
    u32x4 a, c; pack16(c0, a, c); qA0 = asb(a); qA1 = asb(c); pack16(c1, a, c); qB0 = asb(a); qB1 = asb(c); }
  bar();
  const unsigned onea = (l & 6) == 0 ? 0x3f803f80u : 0u, oneb = (l & 6) == 4 ? 0x3f803f80u : 0u;
  const bf16x8 ones = asb((u32x4){onea, onea, oneb, oneb}); const int sel = (l & 1) ? 0xEEEEEEEE : 0x44444444;
  auto rowsum = [&](u32x8 p, f32x4 a) { return __builtin_amdgcn_smfmac_f32_16x16x64_bf16(ones, __builtin_bit_cast(bf16x16, p), a, sel, 0, 0); };
  auto lo = [](u32x8 p) { return asb(__builtin_shufflevector(p, p, 0, 1, 2, 3)); };
  auto hi = [](u32x8 p) { return asb(__builtin_shufflevector(p, p, 4, 5, 6, 7)); };
  const lptr kp0 = sm + K2_KV0 + l16, kp1 = sm + K2_KV1 + l16, kp2 = sm + K2_KV2 + l16;
  bf16x8 k0a = ldsA(kp0), k1a = ldsA(kp0 + 1024), v0a = ldsA(kp0 + 2048), v1a = ldsA(kp0 + 3072), k0b = ldsA(kp0 + 4096), k1b = ldsA(kp0 + 5120), v0b, v1b = k0a;
  f32x16 nA, nB, sA, sB, oA = {}, oB = {}; f32x4 lA = {}, lB = {};
  { f32x16 z = {}, y = {}; z = mfma32(k0a, qA0, z); y = mfma32(k0a, qB0, y); z = mfma32(k1a, qA1, z); y = mfma32(k1a, qB1, y); const float m = -max16(z), n = -max16(y);
#pragma unroll
    for (int i = 0; i < 16; ++i) { nA[i] = m; nB[i] = n; } }
  PIN(nA); PIN(nB);
  sA = mfma32(k0a, qA0, nA); sA = mfma32(k1a, qA1, sA);
  u32x8 pA = {}, pB = {};
#define HALF(S, N, Q0, Q1, NEG, OS, ON, LN, PS, PN, K0, V1, K1, V0, W, R0, R1)                                                   \
  { S[0] = EX2(S[0]); S[1] = EX2(S[1]); PIN(S); SB();                                                                 \
    SB(); N = mfma32(K0, Q0, NEG); SB(); R0; SB();                                                                          \
    S[2] = EX2(S[2]); S[3] = EX2(S[3]); PS[0] = pk(S[0], S[1]); PS[1] = pk(S[2], S[3]); PIN(PS); SB();                \
    ON = mfma32(V1, hi(PN), ON); SB(); R1; SB();                                                                      \
    S[4] = EX2(S[4]); S[5] = EX2(S[5]); S[6] = EX2(S[6]); S[7] = EX2(S[7]); PS[2] = pk(S[4], S[5]); PS[3] = pk(S[6], S[7]); PIN(PS); SB(); \
    SB(); LN = rowsum(PN, LN); SB();                                                                                        \
    S[8] = EX2(S[8]); PIN(S); SB();                                                                                   \
    SB(); N = mfma32(K1, Q1, N); SB(); SB();                                                                             \
    S[9] = EX2(S[9]); S[10] = EX2(S[10]); S[11] = EX2(S[11]); PS[4] = pk(S[8], S[9]); PS[5] = pk(S[10], S[11]); PIN(PS); SB(); \
    S[12] = EX2(S[12]); S[13] = EX2(S[13]); PIN(S); SB();                                                             \
    W; SB(); OS = mfma32(V0, lo(PS), OS); SB();                                                                                \
    S[14] = EX2(S[14]); S[15] = EX2(S[15]); PS[6] = pk(S[12], S[13]); PS[7] = pk(S[14], S[15]); PIN(PS); SB(); }
#define TILE(X, Y, C, N)                                                                                               \
  HALF(sA, sB, qB0, qB1, nB, oA, oB, lB, pA, pB, k0##C, v1##N, k1##C, v0##C, __builtin_amdgcn_s_waitcnt(0xC07F), , )   \
  HALF(sB, sA, qA0, qA1, nA, oB, oA, lA, pB, pA, k0##N, v1##C, k1##N, v0##C, , (v0##N = ldsA((X) + 2048), v1##N = ldsA((X) + 3072)), (k0##C = ldsA(Y), k1##C = ldsA((Y) + 1024)))
#define TILE2(T, X) TILE(T + 4096, X, a, b) TILE(X, X + 4096, b, a)
#define STAGE(P, Q, BAR) TILE2(P, P + 2 * 4096) TILE2(P + 2 * 4096, P + 4 * 4096) TILE2(P + 4 * 4096, P + 6 * 4096) BAR TILE2(P + 6 * 4096, Q)
  { lptr c = kp0, n = kp1, f = kp2;
    for (int st = 0; st < 7; ++st) { STAGE(c, n, bar();) const lptr t = c; c = n; n = f; f = t; }
    STAGE(c, n, ) }
  oB = mfma32(v1b, hi(pB), oB); lB = rowsum(pB, lB);
  mfma_done();
  auto finish = [&](f32x16& o, float lsum, bf16x8 q0, bf16x8 q1, int i) {
    float z0 = 0.f, z1 = 0.f;
#pragma unroll
    for (int e = 0; e < 8; ++e) { z0 = __builtin_fmaf(o[e], 0.f, z0); z1 = __builtin_fmaf(o[8 + e], 0.f, z1); }
    bool bad = !(lsum + (z0 + z1) < 0x1p80f);
    if (__any(bad)) {
      float m = -INFINITY; lsum = 0.f; o = (f32x16){};
      for (int t = 0; t < 64; ++t) { const bf16x8* g = (const bf16x8*)(kv + t * 4096) + l;
        f32x16 s = {}; s = mfma32(g[0], q0, s); s = mfma32(g[64], q1, s); mfma_done();
        const float mn = __builtin_fmaxf(m, max16(s)), al = EX2(m - mn); m = mn; float rsum = 0.f;
#pragma unroll
        for (int e = 0; e < 16; ++e) { s[e] = rbf(EX2(s[e] - mn)); rsum += s[e]; o[e] *= al; }
        lsum = lsum * al + swap32add(rsum);
        u32x4 a, c; pack16(s, a, c); o = mfma32(g[128], asb(a), o); o = mfma32(g[192], asb(c), o); mfma_done(); } }
    o *= 1.f / lsum;
    u32x4 o0, o1; pack16(o, o0, o1);
    char* d = obuf + (size_t)((tb * 4 + 2 * (w & 1) + i) * 64 + (hp * 2 + (w >> 1)) * 2) * 1024;
    gstore<0>(l16, d, o0); gstore<1024>(l16, d, o1); };
  finish(oA, lA[0] + lA[1], qA0, qA1, 0); finish(oB, lB[0] + lB[1], qB0, qB1, 1);
}

constexpr int K3_S = 6, K3_X = K3_S * GA_STAGE, K3_MV = 4 * GA_STAGE, K3_LDS = K3_X + 16384;
extern "C" __global__ __launch_bounds__(512, 2) void k_oproj(const char* __restrict__ obuf, const char* __restrict__ wo, const char* __restrict__ x, const float* __restrict__ modf,
                                                  char* __restrict__ x1b, char* __restrict__ yb, f32x2* __restrict__ stat,
                                                  const float* __restrict__ utp, float* __restrict__ utf) {
  const lptr sm = (lptr)smem_raw;
  const int b = blockIdx.x, l = threadIdx.x & 63, w = __builtin_amdgcn_readfirstlane(threadIdx.x >> 6);
  const int xcd = b & 7, j = b >> 3, tb = (xcd >> 1) * 4 + (j & 3), nt = (xcd & 1) * 8 + (j >> 2);
  if (w < 4) {
  const int hi = l >> 5, tb32 = tb * 4 + 2 * (w & 1), n0 = nt * 64 + (w >> 1) * 32;
  f32x16 acc[2]; gemmA_compute<16, K3_S>(sm, l, w, acc[0], acc[1]);
  bar();
  f32x4 ga[4], cb[4];
#pragma unroll
  for (int q = 0; q < 4; ++q) { const lptr p = sm + K3_MV + w * 1024 + (w >> 1) * 128 + q * 32 + hi * 16; ga[q] = *(const LDS f32x4*)p; cb[q] = *(const LDS f32x4*)(p + 256); }
  u32x2 xr[2][4];
#pragma unroll
  for (int i = 0; i < 2; ++i)
#pragma unroll
    for (int q = 0; q < 4; ++q) xr[i][q] = *(const LDS u32x2*)(sm + K3_X + ((w & 1) * 64 + i * 32 + (l & 31)) * 128 + (((w >> 1) * 4 + q) ^ (l & 7)) * 16 + hi * 8);
#pragma unroll
  for (int i = 0; i < 2; ++i) { unsigned xo[8], yo[8]; float s = 0.f, s2 = 0.f;
#pragma unroll
    for (int h = 0; h < 2; ++h) { const u32x4 xs = {xr[i][2 * h][0], xr[i][2 * h][1], xr[i][2 * h + 1][0], xr[i][2 * h + 1][1]};
#pragma unroll
      for (int e = 0; e < 4; ++e) { const int r = 8 * h + 2 * e, q = r >> 2, c = r & 3;
        const unsigned u = pk(blo(xs[e]) + ga[q][c] * acc[i][r], bhi(xs[e]) + ga[q][c + 1] * acc[i][r + 1]);
        xo[4 * h + e] = u; yo[4 * h + e] = pk(blo(u) * cb[q][c], bhi(u) * cb[q][c + 1]);
        const bf16x2 v = __builtin_bit_cast(bf16x2, u); s = __builtin_amdgcn_fdot2_f32_bf16(v, __builtin_bit_cast(bf16x2, 0x3f803f80u), s, false); s2 = __builtin_amdgcn_fdot2_f32_bf16(v, v, s2, false); } }
    const size_t off = (size_t)((tb32 + i) * 64 + (n0 >> 4)) * 1024;
    gstore<0>(l * 16, yb + off, (u32x4){yo[0], yo[1], yo[2], yo[3]}); gstore<1024>(l * 16, yb + off, (u32x4){yo[4], yo[5], yo[6], yo[7]});
    gstore<0>(l * 16, x1b + off, (u32x4){xo[0], xo[1], xo[2], xo[3]}); gstore<1024>(l * 16, x1b + off, (u32x4){xo[4], xo[5], xo[6], xo[7]});
    s = swap32add(s); const float mean = s * (1.f / 32.f), m2 = __builtin_fmaxf(swap32add(s2) - s * mean, 0.f);
    if (hi == 0) stat[((tb32 + i) * 32 + (n0 >> 5)) * 32 + l] = (f32x2){mean, m2}; }
  return; }
  { u32x4 e; const int lw = w - 4;
    gemmA_load<16, K3_S, D3, 0, 0, 2, 3>(obuf + (size_t)tb * 4 * 64 * 1024, wo + nt * 131072, l, lw, [](auto) {}, [&](auto g) {
      const char* s = x + (size_t)(tb * 128 + lw * 32 + g * 16) * 2048 + nt * 128; const unsigned vx = (l >> 3) * 2048 + ((l & 7) ^ (l >> 3)) * 16;
      SETM0(K3_X + lw * 4096 + g * 2048); glds<0>(vx, s); glds<1024>(vx, s + 16384 - 1024);
      if constexpr (g == 0) { SETM0(K3_MV + lw * 1024); glds<0>((l & 16) * 512 + (l & 15) * 16, (const char*)(modf + 2 * DM + nt * 64)); }
      else { CODE_RANGE(k_ffn1, k_ffn2, cb, cs) const unsigned o = (j & 7) * 4096 + lw * 1024 + l * 16; e = gload<0>(o < cs ? o : 0, cb); } });
    waitbar<1>();
    if (b < 28) { const int i = b * 256 + lw * 64 + l; const float v = (utp[i] + utp[2 * FF + i]) + (utp[4 * FF + i] + utp[6 * FF + i]);
      utf[i] = i < FF ? v : -1.4426950408889634f * v; }
    asm volatile("s_waitcnt vmcnt(0)" : "+v"(e)::"memory"); }
}

constexpr int K4_STAGE = 45056, K4_UT = 3 * K4_STAGE, K4_RS = K4_UT + 2048, K4_LDS = K4_RS + 1024;
struct FragB { bf16x8 a[4]; s16x4 b[14]; };
extern "C" __global__ __launch_bounds__(512, 2) void k_ffn1(const char* __restrict__ yb, const char* __restrict__ w1, const char* __restrict__ stat,
                                                            const char* __restrict__ utp, char* __restrict__ gb) {
  const lptr sm = (lptr)smem_raw;
  const int b = blockIdx.x, t = threadIdx.x, l = t & 63, w = __builtin_amdgcn_readfirstlane(t >> 6);
  const int xcd = b & 7, j = b >> 3, tb = (xcd >> 2) * 8 + (j & 7), nt = (xcd & 3) * 4 + (j >> 3);
  constexpr float NL2E = -1.4426950408889634f;
  if (w < 4) {
  const int g = l >> 4, th = w & 1, nh = w >> 1, sw = (g & 1) * 32;
  const lptr ra = sm + th * 8192 + (g >> 1) * 1024 + (g & 1) * 512 + (l & 15) * 16;
  const lptr rb = sm + 16384 + (16 * (g >> 1) + 4 * (g & 1) + ((l >> 2) & 3)) * 448 + (l & 3) * 8;
  lptr base[4] = {ra, rb + nh * 256 + sw, rb + nh * 256 - sw, rb + (((6 + nh) * 32) ^ sw)};
  PIN(base[0]); PIN(base[1]); PIN(base[2]); PIN(base[3]);
  lptr p[4] = {base[0], base[1], base[2], base[3]};
  f32x4 acc[7][4];
  FragB f, h; int slot = 0;
  auto rd = [&](FragB& d, int i, int ks) {
    if (i < 4) d.a[i] = ldsA(p[0] + (i >> 1) * 4096 + (i & 1) * 256 + ks * 2048);
    else { const int n = (i - 4) >> 1; d.b[i - 4] = ldsT1((n == 6 ? p[3] : p[1 + (n & 1)] + n * 32) + ks * (32 * 448) + (i & 1) * (8 * 448)); } };
  auto kstep = [&](FragB& cur, FragB& nxt, int ks, auto first) {
#pragma unroll
    for (int n = 0; n < 7; ++n)
#pragma unroll
      for (int i = 0; i < 4; ++i) { const int m = n * 4 + i;
        acc[n][i] = mfma16(cat(cur.b[2 * n], cur.b[2 * n + 1]), cur.a[i], first ? (f32x4){} : acc[n][i]); SB();
        if (m < 18) rd(nxt, m, ks); else if (ks == 1 && m < 22) { p[m - 18] = base[m - 18] + slot; PIN(p[m - 18]); } else if (m == 27) ldswait();
        SB(); } };
  bar();
#pragma unroll
  for (int i = 0; i < 18; ++i) rd(f, i, 0);
  slot = K4_STAGE; kstep(f, h, 1, std::true_type{}); sbar(); kstep(h, f, 0, std::false_type{});
#pragma unroll 7
  for (int tt = 1; tt < 15; ++tt) { slot = slot == 2 * K4_STAGE ? 0 : slot + K4_STAGE; kstep(f, h, 1, std::false_type{}); sbar(); kstep(h, f, 0, std::false_type{});
  }
  kstep(f, h, 1, std::false_type{});
#pragma unroll
  for (int n = 0; n < 7; ++n)
#pragma unroll
    for (int i = 0; i < 4; ++i) acc[n][i] = mfma16(cat(h.b[2 * n], h.b[2 * n + 1]), h.a[i], acc[n][i]);
  mfma_done(); bar();
  f32x2 rn[4];
#pragma unroll
  for (int i = 0; i < 4; ++i) rn[i] = *(const LDS f32x2*)(sm + K4_RS + (th * 64 + i * 16 + (l & 15)) * 8);
  struct UT { f32x4 u, t; };
  auto ut = [&](int n) { const int col = ((n < 6 ? 8 * nh + n : 6 + nh) * 16 + 4 * g) * 4; return UT{*(const LDS f32x4*)(sm + K4_UT + col), *(const LDS f32x4*)(sm + K4_UT + 1024 + col)}; };
  auto act = [&](int n, int i, const UT& c) { u32x2 o;
#pragma unroll
    for (int e = 0; e < 2; ++e) { const f32x2 z = rn[i][0] * (f32x2){acc[n][i][2 * e], acc[n][i][2 * e + 1]} + (rn[i][1] * (f32x2){c.u[2 * e], c.u[2 * e + 1]} + (f32x2){c.t[2 * e], c.t[2 * e + 1]});
      const f32x2 dn = NL2E * (f32x2){EX2(z[0]), EX2(z[1])} + NL2E;
      const f32x2 r = z * (f32x2){__builtin_amdgcn_rcpf(dn[0]), __builtin_amdgcn_rcpf(dn[1])}; o[e] = pk(r[0], r[1]); }
    return o; };
  auto dst = [&](int gi, int i) { const int kk = nt * 7 + gi; return gb + (size_t)(((tb * 56 + (kk >> 1)) * 8 + th * 4 + i) * 2 + (kk & 1)) * 1024; };
  const unsigned vo = l * 16;
  UT c[7];
#pragma unroll
  for (int n = 0; n < 2; ++n) c[n] = ut(n);
#pragma unroll
  for (int p = 0; p < 3; ++p) { c[2 * p + 2] = ut(2 * p + 2); if (p < 2) c[2 * p + 3] = ut(2 * p + 3);
#pragma unroll
    for (int i = 0; i < 4; ++i) { const u32x2 lo = act(2 * p, i, c[2 * p]), hi = act(2 * p + 1, i, c[2 * p + 1]); gstore<0>(vo, dst(4 * nh + p, i), (u32x4){lo[0], lo[1], hi[0], hi[1]}); } }
#pragma unroll
  for (int i = 0; i < 4; ++i) gstore2<0>(vo, dst(3, i) + nh * 8, act(6, i, c[6]));
  return; }
  { const int lw = w - 4;
    const char* ap = yb + (size_t)(tb * 4 + lw) * 64 * 1024; const char* wp = w1 + nt * 448;
    unsigned vb[7];
    const unsigned va = l * 16;
    u32x4 st[8], e;
    loader<16, 3, 3, 11, K4_STAGE, 8, 1, 1, 2>([&](int tt, int slot) {
      SETM0(slot + lw * 4096); const char* a = ap + (size_t)tt * 4096;
      glds<0>(va, a); glds<1024>(va, a); glds<2048>(va, a); glds<3072>(va, a);
      if (tt == 0) { SB();
#pragma unroll
        for (int i = 0; i < 7; ++i) { const unsigned c = (lw * 7 + i) * 64 + l, r = __umul24(c, 2341) >> 16;
          vb[i] = __umul24(r, FF * 2) + ((c - __umul24(r, 28)) ^ ((r >> 1) & 2)) * 16 + 3072 - i * 1024; } }
      SETM0(slot + 16384 + lw * 7168 + 3072); const char* s = wp + (size_t)tt * (64 * FF * 2);
      glds<-3072>(vb[0], s); glds<-2048>(vb[1], s); glds<-1024>(vb[2], s); glds<0>(vb[3], s); glds<1024>(vb[4], s); glds<2048>(vb[5], s); glds<3072>(vb[6], s); },
      [&](auto i) { st[i] = gload<0>(va, stat + (size_t)(tb * 4 + lw) * 8192 + i * 1024); },
      [&](auto) { SETM0(K4_UT + (lw & 1) * 1024); glds<0>(l < 56 ? va : 0, utp + ((lw & 1) * FF + nt * 224) * 4);
        CODE_RANGE(k_ffn2, k_mod, cb, cs) const unsigned o = (j & 3) * 4096 + lw * 1024 + l * 16; e = gload<0>(o < cs ? o : 0, cb); });
#pragma unroll
    for (int i = 0; i < 8; ++i) asm volatile("" : "+v"(st[i]));
    f32x4 o;
#pragma unroll
    for (int k = 0; k < 2; ++k) { float mean = 0.f, m2 = 0.f;
#pragma unroll
      for (int i = 0; i < 8; ++i) mean += __uint_as_float(st[i][2 * k]);
      mean = swap32add(swap16add(mean)) * (1.f / 32.f);
#pragma unroll
      for (int i = 0; i < 8; ++i) { const float d = __uint_as_float(st[i][2 * k]) - mean; m2 += __uint_as_float(st[i][2 * k + 1]) + 32.f * d * d; }
      const float rs = NL2E * __builtin_amdgcn_rsqf(swap32add(swap16add(m2)) * (1.f / 1024.f) + 1e-5f); o[2 * k] = rs; o[2 * k + 1] = -rs * mean; }
    if (l < 16) *(LDS f32x4*)(sm + K4_RS + (lw * 32 + 2 * l) * 8) = o;
    waitbar<1>();
    asm volatile("s_waitcnt vmcnt(0)" : "+v"(e)::"memory");
    }
}

constexpr int K5_STAGE = 32768, K5_S = 5, K5_X = 3 * K5_STAGE, K5_LDS = K5_S * K5_STAGE;
struct FragC { bf16x8 a[4]; s16x4 b[8]; };
extern "C" __global__ __launch_bounds__(512, 2) void k_ffn2(const char* __restrict__ gb, const char* __restrict__ w2, const char* __restrict__ x1b,
                                                            const float* __restrict__ modf, char* __restrict__ out, char* xch) {
  const lptr sm = (lptr)smem_raw;
  const int b = blockIdx.x, l = threadIdx.x & 63, w = __builtin_amdgcn_readfirstlane(threadIdx.x >> 6);
  const int kh = b & 1, j = b >> 3, tb = ((b & 7) >> 1) * 4 + (j & 3), nt = j >> 2;
  if (w < 4) {
  const int g = l >> 4, th = w & 1, nh = w >> 1, rx = 4 * (g & 1) + ((l >> 2) & 3);
  const int tok0 = tb * 128 + th * 64 + kh * 32, n0 = nt * 128 + nh * 64;
  u32x2 xr[2][4]; f32x4 gv[4];
  lptr base[6], p[6];
  base[4] = sm + th * 8192 + kh * 4096 + l * 16; base[5] = sm + th * 8192 + (kh ^ 1) * 4096 + l * 16;
#pragma unroll
  for (int n = 0; n < 4; ++n) base[n] = sm + 16384 + (4 * g + ((l >> 2) & 3)) * 256 + ((4 * nh + n) ^ rx) * 32 + (l & 3) * 8;
#pragma unroll
  for (int n = 0; n < 6; ++n) { PIN(base[n]); p[n] = base[n]; }
  f32x4 acc[4][4];
  FragC f, h; int slot = 0;
  auto rd = [&](FragC& d, int i, int ks) {
    if (i < 4) d.a[i] = ldsA(p[4 + (i >> 1)] + (i & 1) * 2048 + ks * 1024); else d.b[i - 4] = ldsT1(p[(i - 4) >> 1] + ks * 8192 + (i & 1) * 4096); };
  auto kstep = [&](FragC& cur, FragC& nxt, int ks, auto first) {
#pragma unroll
    for (int n = 0; n < 4; ++n)
#pragma unroll
      for (int i = 0; i < 4; ++i) { const int m = n * 4 + i;
        acc[n][i] = mfma16(cat(cur.b[2 * n], cur.b[2 * n + 1]), cur.a[i], first ? (f32x4){} : acc[n][i]); SB();
        if (m < 12) rd(nxt, m, ks);
        else if (ks == 1) { p[m - 12] = base[m - 12] + slot; PIN(p[m - 12]); if (m < 14) { p[m - 8] = base[m - 8] + slot; PIN(p[m - 8]); } }
        if (m == 15) { if (ks == 1) ldswait(); else ldsw<4>(); }
        if (ks == 1 && m == 7) ldsw<8>();
        SB(); } };
  bar();
#pragma unroll
  for (int i = 0; i < 12; ++i) rd(f, i, 0);
  slot = K5_STAGE; kstep(f, h, 1, std::true_type{}); sbar(); kstep(h, f, 0, std::false_type{});
#pragma unroll 6
  for (int tt = 1; tt < 25; ++tt) { slot = slot == (K5_S - 1) * K5_STAGE ? 0 : slot + K5_STAGE; kstep(f, h, 1, std::false_type{}); sbar(); kstep(h, f, 0, std::false_type{});
  }
  auto hstep = [&](FragC& cur, FragC& nxt, int half, int nhalf, int ns) {
    const int off = (ns >> 1) * K5_STAGE + (ns & 1) * 1024, offw = (ns >> 1) * K5_STAGE + (ns & 1) * 8192;
#pragma unroll
    for (int m = 0; m < 8; ++m) { const int n = m >> 1, i = 2 * half + (m & 1);
      acc[n][i] = mfma16(cat(cur.b[2 * n], cur.b[2 * n + 1]), cur.a[i], acc[n][i]); SB();
      if (ns >= 0) { if (m < 2) nxt.a[2 * nhalf + m] = ldsA(base[4 + nhalf] + off + m * 2048); nxt.b[m] = ldsT1(base[m >> 1] + offw + (m & 1) * 4096); }
      SB(); } };
  hstep(f, h, 1, 1, 1); bar(); hstep(h, f, 1, 1, 2); hstep(f, h, 1, 1, 3); bar(); hstep(h, f, 1, 1, 4); hstep(f, h, 1, 1, 5); hstep(h, f, 1, 0, 0);
  hstep(f, h, 0, 0, 1);
  { char* d = xch + (size_t)(b * 4 + w) * 4096;
#pragma unroll
    for (int i = 0; i < 2; ++i)
#pragma unroll
      for (int q = 0; q < 2; ++q) { const f32x4 u = acc[2 * q][2 + i], v = acc[2 * q + 1][2 + i];
        gstore<0>(l * 16, d + (i * 2 + q) * 1024, (u32x4){min(pk(u[0], u[1]), ~1u), min(pk(u[2], u[3]), ~1u), min(pk(v[0], v[1]), ~1u), min(pk(v[2], v[3]), ~1u)}); } }
  SB();
  hstep(h, f, 0, 0, 2); hstep(f, h, 0, 0, 3); hstep(h, f, 0, 0, 4); hstep(f, h, 0, 0, 5); hstep(h, f, 0, 0, -1);
  sbar();
#pragma unroll
  for (int n = 0; n < 4; ++n) { gv[n] = *(const LDS f32x4*)(sm + K5_X + 16384 + w * 1024 + n * 64 + g * 16);
#pragma unroll
    for (int i = 0; i < 2; ++i) xr[i][n] = *(const LDS u32x2*)(sm + K5_X + w * 4096 + n * 1024 + (g & 1) * 512 + (i * 16 + (l & 15)) * 16 + (g >> 1) * 8); }
  u32x4 pr[4];
  { const char* s = xch + (size_t)((b ^ 1) * 4 + w) * 4096; const unsigned vo = l * 16;
    for (int spin = 0; spin < (1 << 16); ++spin) {
      pr[0] = gloadd<0>(vo, s); pr[1] = gloadd<1024>(vo, s); pr[2] = gloadd<2048>(vo, s); pr[3] = gloadd<3072>(vo, s);
      asm volatile("s_waitcnt vmcnt(0)" : "+v"(pr[0]), "+v"(pr[1]), "+v"(pr[2]), "+v"(pr[3])::"memory");
      unsigned m = 0;
#pragma unroll
      for (int i = 0; i < 4; ++i) m = max(max(max(m, pr[i][0]), max(pr[i][1], pr[i][2])), pr[i][3]);
      if (!__any(m == ~0u)) break;
    } }
#pragma unroll
  for (int i = 0; i < 2; ++i)
#pragma unroll
    for (int p = 0; p < 2; ++p) { unsigned o[2][2];
#pragma unroll
      for (int q = 0; q < 2; ++q) { const int n = 2 * p + q; const unsigned u0 = pr[2 * i + p][2 * q], u1 = pr[2 * i + p][2 * q + 1];
        const f32x4 a = acc[n][i] + (f32x4){blo(u0), bhi(u0), blo(u1), bhi(u1)};
        o[q][0] = pk(blo(xr[i][n][0]) + gv[n][0] * a[0], bhi(xr[i][n][0]) + gv[n][1] * a[1]); o[q][1] = pk(blo(xr[i][n][1]) + gv[n][2] * a[2], bhi(xr[i][n][1]) + gv[n][3] * a[3]); }
      auto r0 = __builtin_amdgcn_permlane16_swap(o[0][0], o[1][0], false, false); auto r1 = __builtin_amdgcn_permlane16_swap(o[0][1], o[1][1], false, false);
      gstore<0>((l & 15) * 2048 + (g & 1) * 32 + (g >> 1) * 16, out + (size_t)(tok0 + i * 16) * 2048 + (n0 + 32 * p) * 2, (u32x4){r0[0], r1[0], r0[1], r1[1]}); }
  return; }
  { const int lw = w - 4;
    const char* ap = gb + (size_t)(tb * 56 + kh * 28) * 16384 + lw * 4096; const char* wp = w2 + (size_t)(nt * 56 + kh * 28) * 16384 + lw * 4096;
    const unsigned va = l * 16;
    loader<28, K5_S, D5, 8, K5_STAGE, 0, 0, 1, 5>([&](int t, int slot) {
      SETM0(slot + lw * 4096); const char* a = ap + (size_t)t * 16384; glds<0>(va, a); glds<1024>(va, a); glds<2048>(va, a); glds<3072>(va, a);
      SETM0(slot + 16384 + lw * 4096); const char* s = wp + (size_t)t * 16384;
      glds<0>(va, s); glds<1024>(va, s); glds<2048>(va, s); glds<3072>(va, s); }, [](auto) {},
      [&](auto) {
        const char* s = x1b + (size_t)((tb * 4 + (lw & 1) * 2 + kh) * 64 + nt * 8 + (lw >> 1) * 4) * 1024;
        SETM0(K5_X + lw * 4096); glds<0>(va, s); glds<1024>(va, s); glds<2048>(va, s); glds<3072>(va, s);
        SETM0(K5_X + 16384 + lw * 1024); glds<0>((l & 15) * 16, (const char*)(modf + 5 * DM + nt * 128 + (lw >> 1) * 64)); });
    waitbar<0>();
    }
}

extern "C" __global__ __launch_bounds__(512) void k_mod(const uint16_t* __restrict__ c, const char* __restrict__ wmod, float* __restrict__ modp,
                                             const char* __restrict__ x, const char* __restrict__ wq, const char* __restrict__ wk, const char* __restrict__ wv, char* __restrict__ wqp) {
  __shared__ __attribute__((aligned(16))) float red[8][64];
  const int t = threadIdx.x;
  unsigned own;
  { unsigned long long pc; int n; asm volatile("s_getpc_b64 %0\n.Lown0:\n\ts_mov_b32 %1, .Lown1-.Lown0" : "=s"(pc), "=s"(n)); const int o = (t & 63) * 64;
    own = gload1<0>(o < n ? o : 0, (const char*)(pc & ~3ull)); }
  if (blockIdx.x >= PF0 && blockIdx.x < PF0 + 128) { const int p = blockIdx.x - PF0; u32x4 d[7];
    const char* xb = x + (size_t)((p & 7) * 16 + (p >> 3)) * 32768;
    { const char* s = wq + (size_t)(p & 7) * 262144 + (p >> 3) * 128; const unsigned v = imgA(t & 63) + (t >> 6) * 32768; d[4] = gload<0>(v, s); d[5] = gload<0>(v, s + 16384); }
#pragma unroll
    for (int i = 0; i < 4; ++i) d[i] = gload<0>(t * 16, xb + i * 8192);
    d[6] = gload<0>((t & 63) * 16, (p & 64 ? wv : wk) + (p & 63) * 1024);
    { CODE_RANGE(k_ln1kv, k_mod, cb, cs) const unsigned o = (p >> 3) * 8192 + t * 16; u32x4 e = gload<0>(o < cs ? o : 0, cb);
      asm volatile("s_waitcnt vmcnt(6)" : "+v"(d[4]), "+v"(d[5])::"memory");
      const unsigned v = (t & 63) * 16 + (t >> 6) * 2048; char* o2 = wqp + ((p >> 3) * 8 + (p & 7)) * 16384; gstore<0>(v, o2, d[4]); gstore<1024>(v, o2, d[5]);
      asm volatile("s_waitcnt vmcnt(0)" : "+v"(e)::"memory"); }
    asm volatile("s_waitcnt vmcnt(0)" : "+v"(d[0]), "+v"(d[1]), "+v"(d[2]), "+v"(d[3]), "+v"(d[4]), "+v"(d[5]), "+v"(d[6])::"memory");
    asm volatile(".Lown1:" : "+v"(own));
    return; }
  const int b = blockIdx.x < PF0 ? blockIdx.x : blockIdx.x - 128, cg = b % 96, ks = b / 96;
  const int row0 = ks * 256 + (t >> 3) * 4;
  const char* p = wmod + (size_t)row0 * (NMOD * 2) + cg * 128 + (t & 7) * 16;
  u32x4 w[4]; const u32x2 cv = *(const u32x2*)(c + row0);
#pragma unroll
  for (int i = 0; i < 4; ++i) w[i] = *(const u32x4*)(p + (size_t)i * NMOD * 2);
  float a[8] = {0, 0, 0, 0, 0, 0, 0, 0};
#pragma unroll
  for (int i = 0; i < 4; ++i) { const float f = rbf(silu(i & 1 ? bhi(cv[i >> 1]) : blo(cv[i >> 1])));
#pragma unroll
    for (int e = 0; e < 4; ++e) { a[2 * e] += f * blo(w[i][e]); a[2 * e + 1] += f * bhi(w[i][e]); } }
#pragma unroll
  for (int e = 0; e < 8; ++e) a[e] = swap32add(swap16add(dppadd<0x128>(a[e])));
  if ((t & 63) < 8) { *(f32x4*)&red[t >> 6][(t & 7) * 8] = (f32x4){a[0], a[1], a[2], a[3]}; *(f32x4*)&red[t >> 6][(t & 7) * 8 + 4] = (f32x4){a[4], a[5], a[6], a[7]}; }
  __syncthreads();
  PIN(own);
  if (t < 64) modp[ks * NMOD + cg * 64 + t] = ((red[0][t] + red[1][t]) + (red[2][t] + red[3][t])) + ((red[4][t] + red[5][t]) + (red[6][t] + red[7][t]));
}

extern "C" void kernel_launch(void* const* d_in, const int* in_sizes, int n_in, void* d_out, int out_size, void* d_ws, size_t ws_size, hipStream_t stream) {
  const char* x = (const char*)d_in[0]; const char* wq = (const char*)d_in[1]; const char* wk = (const char*)d_in[2]; const char* wv = (const char*)d_in[3];
  const char* wo = (const char*)d_in[4]; const char* w1 = (const char*)d_in[6]; const char* w2 = (const char*)d_in[7];
  const uint16_t* c = (const uint16_t*)d_in[8]; const char* wmod = (const char*)d_in[9];
  char* ws = (char*)d_ws;
  (void)hipFuncSetAttribute((const void*)k_ln1kv, hipFuncAttributeMaxDynamicSharedMemorySize, K1_LDS);
  (void)hipFuncSetAttribute((const void*)k_qattn, hipFuncAttributeMaxDynamicSharedMemorySize, K2_LDS);
  (void)hipFuncSetAttribute((const void*)k_oproj, hipFuncAttributeMaxDynamicSharedMemorySize, K3_LDS);
  (void)hipFuncSetAttribute((const void*)k_ffn1, hipFuncAttributeMaxDynamicSharedMemorySize, K4_LDS);
  (void)hipFuncSetAttribute((const void*)k_ffn2, hipFuncAttributeMaxDynamicSharedMemorySize, K5_LDS);
  float* modp = (float*)(ws + WS_MODP); float* modf = (float*)(ws + WS_MODF);
  k_mod<<<512, 512, 0, stream>>>(c, wmod, modp, x, wq, wk, wv, ws + WS_WQP);
  k_ln1kv<<<256, 512, K1_LDS, stream>>>(x, wk, wv, (const char*)modp, modf, (uint16_t*)(ws + WS_MODH), ws + WS_H, ws + WS_KV);
  k_qattn<<<256, 512, K2_LDS, stream>>>(ws + WS_H, ws + WS_WQP, ws + WS_KV, ws + WS_O, w1, ws + WS_MODH, (float*)(ws + WS_UTP), wo, w2, ws + WS_XCH, ws + WS_WOP, ws + WS_W2P);
  k_oproj<<<256, 512, K3_LDS, stream>>>(ws + WS_O, ws + WS_WOP, x, modf, ws + WS_X1, ws + WS_Y, (f32x2*)(ws + WS_STAT), (const float*)(ws + WS_UTP), (float*)(ws + WS_UTF));
  k_ffn1<<<256, 512, K4_LDS, stream>>>(ws + WS_Y, w1, ws + WS_STAT, ws + WS_UTF, ws + WS_G);
  k_ffn2<<<256, 512, K5_LDS, stream>>>(ws + WS_G, ws + WS_W2P, ws + WS_X1, modf, (char*)d_out, ws + WS_XCH);
}
```
